```python
import torch

def _softmax(x, axis=-1):
    return torch.softmax(x, dim=axis)
def _gelu(x):
    return torch.nn.functional.gelu(x, approximate="tanh")
def _silu(x):
    return torch.nn.functional.silu(x)
def _sigmoid(x):
    return torch.sigmoid(x)
def _layernorm(x, axis=-1, eps=1e-5):
    m = x.mean(dim=axis, keepdim=True)
    v = x.var(dim=axis, keepdim=True, unbiased=False)
    return (x - m) / torch.sqrt(v + eps)
def _scatter_add(tgt, idx, src, axis=0):
    return tgt.clone().index_add_(axis, idx, src)
def _segment_sum(src, seg, n):
    out = torch.zeros((n,) + src.shape[1:], dtype=src.dtype)
    return out.index_add_(0, seg, src)
def _pad(x, pw):
    flat = tuple(v for pair in reversed(pw) for v in pair)
    return torch.nn.functional.pad(x, flat)

kernel_name = 'interleave_layers__sliding_block__dense_activation_silu__prompt'

def setup_inputs():

    _w = (lambda a: (lambda t: t.to(torch.bfloat16) if t.is_floating_point() else t)(torch.as_tensor(a)))
    def _A_setup():
        B, S, D = 1, 2048, 512
        return {
            "x": _w((lambda *s: torch.randn(*s, dtype=torch.bfloat16) * 0.05)(B, S, D)),
            "wqkv": _w((lambda *s: torch.randn(*s, dtype=torch.bfloat16) * 0.05)(D, 3 * D)),
            "wo": _w((lambda *s: torch.randn(*s, dtype=torch.bfloat16) * 0.05)(D, D)),
        }
    _A_in = _A_setup()

    def _B_setup():
        B, S, D, F = 1, 2048, 512, 1792
        return {
            "x": _w((lambda *s: torch.randn(*s, dtype=torch.bfloat16) * 0.05)(B, S, D)),
            "w1": _w((lambda *s: torch.randn(*s, dtype=torch.bfloat16) * 0.8)(D, F) * (D ** -0.5)),
            "w2": _w((lambda *s: torch.randn(*s, dtype=torch.bfloat16) * 0.8)(F, D) * (F ** -0.5)),
        }
    _B_in = _B_setup()

    return {
        "A_x": _A_in["x"],
        "A_wqkv": _A_in["wqkv"],
        "A_wo": _A_in["wo"],
        "B_x": _B_in["x"],
        "B_w1": _B_in["w1"],
        "B_w2": _B_in["w2"],
    }

def reference(A_x, A_wqkv, A_wo, B_x, B_w1, B_w2):
    def _A_ref(x, wqkv, wo):
        B, S, D = x.shape
        H = 32; Dh = D // H; W = 256
        qkv = (lambda x, *d: x.permute(*d))((x @ wqkv).reshape(B, S, 3, H, Dh), 2, 0, 3, 1, 4)
        q, k, v = qkv[0], qkv[1], qkv[2]
        scores = torch.einsum("bhqd,bhkd->bhqk", q, k) * (Dh ** -0.5)
        idx = torch.arange(S)
        mask = (idx[:, None] - idx[None, :] >= 0) & (idx[:, None] - idx[None, :] < W)
        scores = torch.where(mask[None, None], scores, -1e9)
        p = _softmax(scores, axis=-1)
        o = torch.einsum("bhqk,bhkd->bhqd", p, v)
        return (lambda x, *d: x.permute(*d))(o, 0, 2, 1, 3).reshape(B, S, D) @ wo

    def _B_ref(x, w1, w2):
        h = torch.einsum("bsd,df->bsf", x, w1)
        h = _silu(h)
        out = torch.einsum("bsf,fd->bsd", h, w2)
        return out

    x = A_x
    x = x + _A_ref(_layernorm(x, axis=-1), A_wqkv, A_wo)
    x = x + _B_ref(_layernorm(x, axis=-1), B_w1, B_w2)
    x = x + _A_ref(_layernorm(x, axis=-1), A_wqkv, A_wo)
    x = x + _B_ref(_layernorm(x, axis=-1), B_w1, B_w2)
    return x
```

```cpp
#include <hip/hip_runtime.h>
#include <hip/hip_bf16.h>
#include <stdint.h>

#define GAS __attribute__((address_space(1)))
#define LAS __attribute__((address_space(3)))
#define DI __device__ __forceinline__

typedef __bf16 bf16x8 __attribute__((ext_vector_type(8)));
typedef __bf16 bf16x2 __attribute__((ext_vector_type(2)));
typedef short s16x4 __attribute__((ext_vector_type(4)));
typedef float f32x16 __attribute__((ext_vector_type(16)));
typedef float f32x4 __attribute__((ext_vector_type(4)));
typedef float f32x2 __attribute__((ext_vector_type(2)));
typedef unsigned u32x4 __attribute__((ext_vector_type(4)));
typedef unsigned u32x2 __attribute__((ext_vector_type(2)));
typedef GAS unsigned gu32;

#define TS(i)

namespace {
constexpr int DM = 512, FF = 1792;
constexpr float EPS = 1e-5f;
constexpr float LOG2E = 1.4426950408889634f;
constexpr float QSCALE = 0.25f * LOG2E;

constexpr int MB = 1 << 20;
constexpr int XF_OFF = 0;
constexpr int O_OFF = 4 * MB;
constexpr int X2_OFF = 8 * MB;
constexpr int H_OFF = 12 * MB;
constexpr int KV_OFF = 28 * MB;
constexpr int ST_OFF = 40 * MB;
constexpr int ST_BUF = 262144;
constexpr int FLAG_OFF = 42 * MB;
constexpr int FLAG_BYTES = 11 * 256 * 128;
constexpr int TMO_OFF = FLAG_OFF + FLAG_BYTES;
constexpr int WS_USED = 44 * MB;

constexpr int L_WQ = 0, L_Q = 96 * 1024, L_KV = 104 * 1024, L_ONES = 152 * 1024;
constexpr int L_WO = 0, L_W1A = 64 * 1024, L_W1B = 0, L_RED2 = 142 * 1024;
constexpr int L_W2 = 0, L_RED4 = 96 * 1024;
constexpr int L_TAB = 158 * 1024, L_CSQ = L_TAB, L_CS1 = L_TAB + 512, L_STT = L_TAB + 1024;
constexpr int LDS_BYTES = 160 * 1024;
constexpr int WQ_ROW = 192, W1_ROW = 224;
constexpr int AUX_SC1 = 16;

struct Params {
    const uint16_t* x; const uint16_t* wqkv; const uint16_t* wo; const uint16_t* w1; const uint16_t* w2;
    uint16_t* out; char* ws;
};

DI f32x16 mfma32(bf16x8 a, bf16x8 b, f32x16 c) { return __builtin_amdgcn_mfma_f32_32x32x16_bf16(a, b, c, 0, 0, 0); }
DI f32x4 mfma16(bf16x8 a, bf16x8 b, f32x4 c) { return __builtin_amdgcn_mfma_f32_16x16x32_bf16(a, b, c, 0, 0, 0); }
DI unsigned pk(float a, float b) { bf16x2 v = {(__bf16)a, (__bf16)b}; return __builtin_bit_cast(unsigned, v); }
DI float bflo(unsigned u) { return __uint_as_float(u << 16); }
DI float bfhi(unsigned u) { return __uint_as_float(u & 0xffff0000u); }
DI bf16x8 asfrag(u32x4 v) { return __builtin_bit_cast(bf16x8, v); }
DI s16x4 trd(const LAS char* p) { return __builtin_amdgcn_ds_read_tr16_b64_v4i16((LAS s16x4*)p); }
DI bf16x8 wfrag(const LAS char* p, int off2) {
    s16x4 lo = trd(p), hi = trd(p + off2);
    return __builtin_bit_cast(bf16x8, __builtin_shufflevector(lo, hi, 0, 1, 2, 3, 4, 5, 6, 7));
}
DI float dot8_ones(const LAS char* p, int off2, float acc) {
    const u32x2 lo = __builtin_bit_cast(u32x2, trd(p)), hi = __builtin_bit_cast(u32x2, trd(p + off2));
    unsigned w0 = lo.x, w1 = lo.y, w2 = hi.x, w3 = hi.y;
    asm volatile("v_dot2c_f32_bf16 %0, %1, %5\n\tv_dot2c_f32_bf16 %0, %2, %5\n\tv_dot2c_f32_bf16 %0, %3, %5\n\tv_dot2c_f32_bf16 %0, %4, %5\n\ts_nop 2"
                 : "+v"(acc) : "v"(w0), "v"(w1), "v"(w2), "v"(w3), "v"(0x3f803f80u));
    return acc;
}
DI float xor32(float v) { auto r = __builtin_amdgcn_permlane32_swap(__float_as_uint(v), __float_as_uint(v), false, false); return __uint_as_float((threadIdx.x & 32) ? r[0] : r[1]); }
DI float sum_xor32(float v) { auto r = __builtin_amdgcn_permlane32_swap(__float_as_uint(v), __float_as_uint(v), false, false); return __uint_as_float(r[0]) + __uint_as_float(r[1]); }
DI float max_xor32(float v) { auto r = __builtin_amdgcn_permlane32_swap(__float_as_uint(v), __float_as_uint(v), false, false); return fmaxf(__uint_as_float(r[0]), __uint_as_float(r[1])); }
DI float sum_xor16(float v) { auto r = __builtin_amdgcn_permlane16_swap(__float_as_uint(v), __float_as_uint(v), false, false); return __uint_as_float(r[0]) + __uint_as_float(r[1]); }
DI void glds16(const void* g, unsigned lds_addr) {
    unsigned keep;
    asm volatile("s_mov_b32 %0, m0\n\ts_mov_b32 m0, %2\n\ts_nop 0\n\tglobal_load_lds_dwordx4 %1, off\n\ts_mov_b32 m0, %0" : "=&s"(keep) : "v"(g), "s"(lds_addr) : "memory");
}
#define BAR() asm volatile("s_waitcnt lgkmcnt(0)\n\ts_barrier" ::: "memory")
#define DRAIN() asm volatile("s_waitcnt vmcnt(0)" ::: "memory")
#define SB() __builtin_amdgcn_sched_barrier(0)

typedef __amdgpu_buffer_rsrc_t rsrc_t;
DI u32x4 ldw(rsrc_t rs, int voff, int soff) { return __builtin_amdgcn_raw_buffer_load_b128(rs, voff, soff, AUX_SC1); }
DI void stw(rsrc_t rs, int voff, int soff, u32x4 v) { __builtin_amdgcn_raw_buffer_store_b128(v, rs, voff + soff, 0, AUX_SC1); }
DI f32x2 ldst(rsrc_t rs, int voff, int soff) { return __builtin_bit_cast(f32x2, __builtin_amdgcn_raw_buffer_load_b64(rs, voff, soff, AUX_SC1)); }
DI void stst(rsrc_t rs, int voff, int soff, f32x2 v) { __builtin_amdgcn_raw_buffer_store_b64(__builtin_bit_cast(u32x2, v), rs, voff, soff, AUX_SC1); }

DI gu32* flagp(const Params& p, int ph, int g, int r) { return (gu32*)(p.ws + FLAG_OFF) + ((ph * 16 + g) * 16 + r) * 32; }
DI void set_flag(const Params& p, int ph, int g, int r, unsigned tag) { __hip_atomic_store(flagp(p, ph, g, r), tag, __ATOMIC_RELAXED, __HIP_MEMORY_SCOPE_AGENT); }
DI void wait_flags(const Params& p, gu32* f, int stride, int n, unsigned tag, int lane) {
    gu32* a = f + (lane < n ? lane : 0) * stride;
    if (n <= 0) return;
    for (unsigned spins = 0;; ++spins) {
        const unsigned v = __hip_atomic_load(a, __ATOMIC_RELAXED, __HIP_MEMORY_SCOPE_AGENT);
        if (__all(v == tag)) break;
        if (spins > (1u << 20)) { __hip_atomic_store((gu32*)(p.ws + TMO_OFF), 1u, __ATOMIC_RELAXED, __HIP_MEMORY_SCOPE_AGENT); break; }
        __builtin_amdgcn_s_sleep(1);
    }
    __builtin_amdgcn_fence(__ATOMIC_ACQUIRE, "wavefront");
}

DI void stats_store(rsrc_t rs, int buf, int T, int u, int lane, const unsigned (&c)[8]) {
    float s = 0.f, ss = 0.f;
#pragma unroll
    for (int i = 0; i < 8; ++i) { float lo = bflo(c[i]), hi = bfhi(c[i]); s += lo + hi; ss += lo * lo + hi * hi; }
    s = sum_xor32(s); ss = sum_xor32(ss);
    if (lane < 32) stst(rs, lane * 8, ST_OFF + buf * ST_BUF + (T * 16 + u) * 256, f32x2{s, ss});
}

DI void stage_wq(const Params& p, unsigned lds0, int r, int hw, int lane) {
#pragma unroll 4
    for (int ii = 0; ii < 24; ++ii) {
        const int i = ii * 4 + hw, L = i * 64 + lane, k = L / 12, c = L % 12;
        glds16(p.wqkv + (size_t)k * 1536 + (c >> 2) * 512 + 32 * r + (c & 3) * 8, lds0 + L_WQ + i * 1024);
    }
}
DI void stage64(const uint16_t* wsrc, int row0, unsigned dst, int lane) {
    const int rr = lane >> 3, slot = lane & 7, chunk = slot ^ (((rr >> 1) & 1) << 2);
    glds16(wsrc + (size_t)(row0 + rr) * DM + chunk * 8, dst);
}
DI void stage_wo(const Params& p, unsigned lds0, int n, int hw, int lane) {
#pragma unroll 4
    for (int ii = 0; ii < 16; ++ii) { const int i = ii * 4 + hw; stage64(p.wo + 64 * n, 8 * i, lds0 + L_WO + i * 1024, lane); }
}
DI void stage_w1(const Params& p, unsigned lds0, int r, int i0, int i1, int hw, int lane) {
    for (int i = i0 + hw; i < i1; i += 4) {
        const int L = i * 64 + lane, k = L / 14, c = L % 14;
        const unsigned dst = i < 77 ? lds0 + L_W1A + i * 1024 : lds0 + L_W1B + (i - 77) * 1024;
        glds16(p.w1 + (size_t)k * FF + 112 * r + c * 8, dst);
    }
}
DI void stage_w2(const Params& p, unsigned lds0, int n, int c, int slot, int hw, int lane) {
#pragma unroll
    for (int ii = 0; ii < 4; ++ii) {
        const int i = hw * 4 + ii;
        stage64(p.w2 + 64 * n, (i >> 3) * 896 + 64 * c + 8 * (i & 7), lds0 + L_W2 + slot * 16384 + i * 1024, lane);
    }
}

#define PHASE_VARS() \
    int lane = threadIdx.x & 63; asm volatile("" : "+v"(lane)); \
    int lyr = layer; asm volatile("" : "+s"(lyr)); \
    int g = g_, r = r_; asm volatile("" : "+s"(g), "+s"(r)); \
    const int h = lane >> 5, tok = lane & 31; const int m = r & 1, n = r >> 1; (void)h; (void)tok; (void)m; (void)n; (void)lyr;

DI void attn_unit(LAS char* lds, rsrc_t rs, int w, int g, int r, int lyr, int lane) {
    const int h = lane >> 5, tok = lane & 31;
    const int tw = w & 3, hd = w >> 2, T = 4 * g + tw;
    const bf16x8 qf = *(const LAS bf16x8*)(lds + L_Q + tw * 2048 + hd * 1024 + lane * 16);
    const int j0 = T >= 8 ? 0 : 8 - T;
    f32x16 mD, mF;
#pragma unroll
    for (int i = 0; i < 16; ++i) { const int kk = (i & 3) + 8 * (i >> 2) + 4 * h; mD[i] = kk <= tok ? 0.f : -INFINITY; mF[i] = kk > tok ? 0.f : -INFINITY; }
    const LAS char* kb = lds + L_KV + tw * 4096 + hd * 1024 + lane * 16;
    const bool isdata = ((lane >> 4) & 1) == hd;
    bf16x8 kf[9];
    float mx = -INFINITY;
    const f32x16 zero = {};
#pragma unroll
    for (int jj = 0; jj < 9; ++jj) {
        if (jj >= j0) {
            kf[jj] = *(const LAS bf16x8*)(kb + jj * 4096);
            f32x16 s = mfma32(kf[jj], qf, jj == 0 ? mF : (jj == 8 ? mD : zero));
#pragma unroll
            for (int i = 0; i < 16; i += 2) mx = fmaxf(fmaxf(mx, s[i]), s[i + 1]);
        }
    }
    mx = max_xor32(mx);
    f32x16 cm, cD, cF;
#pragma unroll
    for (int i = 0; i < 16; ++i) { cm[i] = -mx; cD[i] = mD[i] - mx; cF[i] = mF[i] - mx; }
    f32x16 o = {};
#pragma unroll
    for (int jj = 0; jj < 9; ++jj) {
        if (jj >= j0) {
            const LAS char* va = isdata ? (lds + L_KV + (tw + jj) * 4096 + 2048 + lane * 16) : (lds + L_ONES);
            bf16x8 v0 = *(const LAS bf16x8*)(va), v1 = *(const LAS bf16x8*)(va + 1024);
            f32x16 s = mfma32(kf[jj], qf, jj == 0 ? cF : (jj == 8 ? cD : cm));
            unsigned pw[8];
#pragma unroll
            for (int i = 0; i < 8; ++i) pw[i] = pk(__builtin_amdgcn_exp2f(s[2 * i]), __builtin_amdgcn_exp2f(s[2 * i + 1]));
            o = mfma32(v0, asfrag(u32x4{pw[0], pw[1], pw[2], pw[3]}), o);
            o = mfma32(v1, asfrag(u32x4{pw[4], pw[5], pw[6], pw[7]}), o);
        }
    }
    float ov[8]; float l;
    if (hd == 0) { l = o[8];
#pragma unroll
        for (int i = 0; i < 8; ++i) ov[i] = o[i];
    } else { l = o[0];
#pragma unroll
        for (int i = 0; i < 8; ++i) ov[i] = o[8 + i];
    }
    const float inv = __builtin_amdgcn_rcpf(l);
    stw(rs, lane * 16, O_OFF + lyr * 2 * MB + (T * 32 + 2 * r + hd) * 1024,
        u32x4{pk(ov[0] * inv, ov[1] * inv), pk(ov[2] * inv, ov[3] * inv), pk(ov[4] * inv, ov[5] * inv), pk(ov[6] * inv, ov[7] * inv)});
}
DI void halo_load(LAS char* lds, rsrc_t rs, int w, int g, int r, int lyr, int lane) {
    const int tile = 4 * g - 8 + w;
    if (tile >= 0) {
        const int kvo = KV_OFF + lyr * 4 * MB + (tile * 16 + r) * 4096;
        u32x4 t0 = ldw(rs, lane * 16, kvo), t1 = ldw(rs, lane * 16, kvo + 1024), t2 = ldw(rs, lane * 16, kvo + 2048), t3 = ldw(rs, lane * 16, kvo + 3072);
        LAS char* kvl = lds + L_KV + w * 4096 + lane * 16;
        *(LAS u32x4*)(kvl) = t0; *(LAS u32x4*)(kvl + 1024) = t1; *(LAS u32x4*)(kvl + 2048) = t2; *(LAS u32x4*)(kvl + 3072) = t3;
    }
}

DI void main_path(const Params& p, LAS char* lds, rsrc_t rs, const int w, const int g_, const int r_, const unsigned tag) {
    const int tid = threadIdx.x;
    {
        const int layer = 0; PHASE_VARS();
        const int T = 4 * g + (r & 3), u = (r >> 2) * 4 + w;
        const uint16_t* row = p.x + (size_t)(T * 32 + tok) * DM + 32 * u + 4 * h;
        u32x2 a0 = *(const GAS u32x2*)(row), a1 = *(const GAS u32x2*)(row + 8), a2 = *(const GAS u32x2*)(row + 16), a3 = *(const GAS u32x2*)(row + 24);
        stw(rs, lane * 16, XF_OFF + (T * 32 + 2 * u) * 1024, u32x4{a0.x, a0.y, a1.x, a1.y});
        stw(rs, lane * 16, XF_OFF + (T * 32 + 2 * u + 1) * 1024, u32x4{a2.x, a2.y, a3.x, a3.y});
        const unsigned c[8] = {a0.x, a0.y, a1.x, a1.y, a2.x, a2.y, a3.x, a3.y};
        stats_store(rs, 0, T, u, lane, c);
        DRAIN();
        BAR();
        if (tid == 0) set_flag(p, 0, g, r, tag);
    }
    TS(1);
    for (int layer = 0; layer < 2; ++layer) {
        const int tb = 2 + layer * 24; (void)tb;
        {
            PHASE_VARS();
            if (w == 0) wait_flags(p, flagp(p, lyr == 0 ? 0 : 5, g, 0), 32, 16, tag, lane);
            BAR();
            TS(tb + 0);
            const int T = 4 * g + w;
            const int a = (lane & 15) >> 2, pp = lane & 3, ch = (lane >> 4) & 1;
            const LAS char* wb = lds + L_WQ + (4 * h + a) * WQ_ROW + ch * 32 + pp * 8;
            LAS float* csq = (LAS float*)(lds + L_CSQ);
            LAS float* stt = (LAS float*)(lds + L_STT);
            f32x16 aq = {}, ak = {}, av = {};
            u32x4 xa[32];
            f32x2 sv[8];
            const int sto = ST_OFF + 2 * lyr * ST_BUF + T * 4096, xo = XF_OFF + lyr * 2 * MB + T * 32768;
#pragma unroll
            for (int i = 0; i < 8; ++i) sv[i] = ldst(rs, (8 * h * 32 + tok) * 8 + i * 256, sto);
#pragma unroll
            for (int s = 0; s < 32; ++s) xa[s] = ldw(rs, lane * 16 + (s & 3) * 1024, xo + (s >> 2) * 4096);
            SB();
            bf16x8 wf[3][3];
#pragma unroll
            for (int s = 0; s < 2; ++s)
#pragma unroll
                for (int t = 0; t < 3; ++t) wf[s][t] = wfrag(wb + s * 16 * WQ_ROW + t * 64, 8 * WQ_ROW);
#pragma unroll
            for (int s = 0; s < 32; ++s) {
                if (s + 2 < 32) {
#pragma unroll
                    for (int t = 0; t < 3; ++t) wf[(s + 2) % 3][t] = wfrag(wb + (s + 2) * 16 * WQ_ROW + t * 64, 8 * WQ_ROW);
                }
                const bf16x8 x = asfrag(xa[s]);
                aq = mfma32(wf[s % 3][0], x, aq); ak = mfma32(wf[s % 3][1], x, ak); av = mfma32(x, wf[s % 3][2], av);
                __builtin_amdgcn_sched_group_barrier(0x100, 2, 0); __builtin_amdgcn_sched_group_barrier(0x8, 1, 0);
                __builtin_amdgcn_sched_group_barrier(0x100, 2, 0); __builtin_amdgcn_sched_group_barrier(0x8, 1, 0);
                __builtin_amdgcn_sched_group_barrier(0x100, 2, 0); __builtin_amdgcn_sched_group_barrier(0x8, 1, 0);
                SB();
            }
            float S = 0.f, SS = 0.f;
#pragma unroll
            for (int i = 0; i < 8; ++i) { S += sv[i].x; SS += sv[i].y; }
            S = sum_xor32(S); SS = sum_xor32(SS);
            const float mean = S * (1.f / DM); const float var = fmaxf(SS * (1.f / DM) - mean * mean, 0.f); const float rstd = 1.f / sqrtf(var + EPS);
            if (h == 0) { stt[w * 64 + tok] = rstd; stt[w * 64 + 32 + tok] = mean * rstd; }
            TS(tb + 1);
            BAR();
            TS(tb + 2);
            const float mr = mean * rstd;
            float q[16], k[16], v[16];
            const float cv = csq[64 + tok];
#pragma unroll
            for (int g4 = 0; g4 < 4; ++g4) {
                f32x4 cq = *(LAS f32x4*)(csq + 8 * g4 + 4 * h), ck = *(LAS f32x4*)(csq + 32 + 8 * g4 + 4 * h);
                f32x4 rsv = *(LAS f32x4*)(stt + w * 64 + 8 * g4 + 4 * h), ms = *(LAS f32x4*)(stt + w * 64 + 32 + 8 * g4 + 4 * h);
#pragma unroll
                for (int i = 0; i < 4; ++i) {
                    q[4 * g4 + i] = (aq[4 * g4 + i] * rstd - mr * cq[i]) * QSCALE;
                    k[4 * g4 + i] = ak[4 * g4 + i] * rstd - mr * ck[i];
                    v[4 * g4 + i] = av[4 * g4 + i] * rsv[i] - ms[i] * cv;
                }
            }
            const u32x4 k0 = {pk(k[0], k[1]), pk(k[2], k[3]), pk(k[4], k[5]), pk(k[6], k[7])};
            const u32x4 k1 = {pk(k[8], k[9]), pk(k[10], k[11]), pk(k[12], k[13]), pk(k[14], k[15])};
            const u32x4 v0 = {pk(v[0], v[1]), pk(v[2], v[3]), pk(v[4], v[5]), pk(v[6], v[7])};
            const u32x4 v1 = {pk(v[8], v[9]), pk(v[10], v[11]), pk(v[12], v[13]), pk(v[14], v[15])};
            const int kvo = KV_OFF + lyr * 4 * MB + (T * 16 + r) * 4096;
            stw(rs, lane * 16, kvo, k0); stw(rs, lane * 16 + 1024, kvo, k1); stw(rs, lane * 16 + 2048, kvo, v0); stw(rs, lane * 16 + 3072, kvo, v1);
            LAS char* kvl = lds + L_KV + (8 + w) * 4096 + lane * 16;
            *(LAS u32x4*)(kvl) = k0; *(LAS u32x4*)(kvl + 1024) = k1; *(LAS u32x4*)(kvl + 2048) = v0; *(LAS u32x4*)(kvl + 3072) = v1;
            LAS char* ql = lds + L_Q + w * 2048 + lane * 16;
            *(LAS u32x4*)(ql) = u32x4{pk(q[0], q[1]), pk(q[2], q[3]), pk(q[4], q[5]), pk(q[6], q[7])};
            *(LAS u32x4*)(ql + 1024) = u32x4{pk(q[8], q[9]), pk(q[10], q[11]), pk(q[12], q[13]), pk(q[14], q[15])};
            DRAIN();
            BAR();
            if (tid == 0) set_flag(p, 1 + 5 * lyr, g, r, tag);
            TS(tb + 3);
            BAR();
            TS(tb + 4);
            halo_load(lds, rs, w, g, r, lyr, lane);
            BAR();
            TS(tb + 5);
        }
        {
            PHASE_VARS();
            attn_unit(lds, rs, w, g, r, lyr, lane);
            TS(tb + 6);
            DRAIN();
            BAR();
            if (tid == 0) set_flag(p, 2 + 5 * lyr, g, r, tag);
            TS(tb + 7);
        }
        {
            PHASE_VARS();
            const int mt = w & 1, kh = (w >> 1) & 1, T = 4 * g + 2 * m + mt, u = 2 * n + kh;
            const int xo = XF_OFF + lyr * 2 * MB + (T * 32 + 2 * u) * 1024;
            const u32x4 r0 = ldw(rs, lane * 16, xo), r1 = ldw(rs, lane * 16 + 1024, xo);
            if (w == 0) wait_flags(p, flagp(p, 2 + 5 * lyr, g, 0), 32, 16, tag, lane);
            BAR();
            TS(tb + 8);
            f32x16 acc[2] = {};
            u32x4 xa[16];
            const int oo = O_OFF + lyr * 2 * MB + (T * 32 + 16 * kh) * 1024;
#pragma unroll
            for (int s = 0; s < 16; ++s) xa[s] = ldw(rs, lane * 16 + (s & 3) * 1024, oo + (s >> 2) * 4096);
            SB();
            const int a = (lane & 15) >> 2, pp = lane & 3, ch = (lane >> 4) & 1;
            const int sw = ((a >> 1) & 1) << 6;
            const LAS char* wb0 = lds + L_WO + (16 * 16 * kh + 4 * h + a) * 128 + (0 ^ sw) + ch * 32 + pp * 8;
            const LAS char* wb1 = lds + L_WO + (16 * 16 * kh + 4 * h + a) * 128 + (64 ^ sw) + ch * 32 + pp * 8;
            bf16x8 wf[4][2];
#pragma unroll
            for (int s = 0; s < 3; ++s) { wf[s][0] = wfrag(wb0 + s * 2048, 1024); wf[s][1] = wfrag(wb1 + s * 2048, 1024); }
#pragma unroll
            for (int s = 0; s < 16; ++s) {
                if (s + 3 < 16) { wf[(s + 3) & 3][0] = wfrag(wb0 + (s + 3) * 2048, 1024); wf[(s + 3) & 3][1] = wfrag(wb1 + (s + 3) * 2048, 1024); }
                const bf16x8 x = asfrag(xa[s]);
                acc[0] = mfma32(wf[s & 3][0], x, acc[0]); acc[1] = mfma32(wf[s & 3][1], x, acc[1]);
                __builtin_amdgcn_sched_group_barrier(0x100, 2, 0); __builtin_amdgcn_sched_group_barrier(0x8, 1, 0);
                __builtin_amdgcn_sched_group_barrier(0x100, 2, 0); __builtin_amdgcn_sched_group_barrier(0x8, 1, 0);
                SB();
            }
            const f32x16 snd = kh ? acc[0] : acc[1];
#pragma unroll
            for (int g4 = 0; g4 < 4; ++g4) *(LAS f32x4*)(lds + L_RED2 + ((w * 4 + g4) * 64 + lane) * 16) = f32x4{snd[4 * g4], snd[4 * g4 + 1], snd[4 * g4 + 2], snd[4 * g4 + 3]};
            TS(tb + 9);
            BAR();
            const f32x16 own = kh ? acc[1] : acc[0];
            const int pw = w ^ 2;
            float v[16];
#pragma unroll
            for (int g4 = 0; g4 < 4; ++g4) { f32x4 t = *(LAS f32x4*)(lds + L_RED2 + ((pw * 4 + g4) * 64 + lane) * 16);
#pragma unroll
                for (int i = 0; i < 4; ++i) v[4 * g4 + i] = own[4 * g4 + i] + t[i]; }
            const unsigned rr[8] = {r0.x, r0.y, r0.z, r0.w, r1.x, r1.y, r1.z, r1.w};
            unsigned c[8];
#pragma unroll
            for (int i = 0; i < 8; ++i) c[i] = pk(v[2 * i] + bflo(rr[i]), v[2 * i + 1] + bfhi(rr[i]));
            const int T16 = 2 * T + ((lane & 31) >> 4);
            const int vo = (T16 * 16 * 64 + (lane & 15) + 16 * h) * 16;
            stw(rs, vo, X2_OFF + lyr * 2 * MB + u * 1024, u32x4{c[0], c[1], c[2], c[3]});
            stw(rs, vo + 512, X2_OFF + lyr * 2 * MB + u * 1024, u32x4{c[4], c[5], c[6], c[7]});
            stats_store(rs, 2 * lyr + 1, T, u, lane, c);
            DRAIN();
            BAR();
            if (tid == 0) set_flag(p, 3 + 5 * lyr, g, r, tag);
            TS(tb + 10);
        }
        {
            PHASE_VARS();
            if (w == 0) wait_flags(p, flagp(p, 3 + 5 * lyr, g, 0), 32, 16, tag, lane);
            BAR();
            TS(tb + 11);
            const int T = 4 * g + w, tk = lane & 15, q = lane >> 4;
            const int a = (lane & 15) >> 2, pp = lane & 3;
            const LAS char* wb = lds + (16 * (q >> 1) + 4 * (q & 1) + a) * W1_ROW + pp * 8;
            LAS float* cs1 = (LAS float*)(lds + L_CS1);
            f32x4 acc[2][7] = {};
            float rstd[2], mr[2];
            u32x4 xa[2][16];
            f32x2 sv[2][4];
            const int sto = ST_OFF + (2 * lyr + 1) * ST_BUF + T * 4096, xo = X2_OFF + lyr * 2 * MB + 2 * T * 16384;
#pragma unroll
            for (int mt = 0; mt < 2; ++mt)
#pragma unroll
                for (int i = 0; i < 4; ++i) sv[mt][i] = ldst(rs, ((4 * q + i) * 32 + 16 * mt + tk) * 8, sto);
#pragma unroll
            for (int u = 0; u < 16; ++u)
#pragma unroll
                for (int mt = 0; mt < 2; ++mt) xa[mt][u] = ldw(rs, lane * 16 + (u & 3) * 1024, xo + mt * 16384 + (u >> 2) * 4096);
            SB();
            auto wofs = [](int u) { return u < 11 ? L_W1A + u * 7168 : L_W1B + (u - 11) * 7168; };
            bf16x8 wf[2][7];
#pragma unroll
            for (int t = 0; t < 7; ++t) wf[0][t] = wfrag(wb + wofs(0) + t * 32, 8 * W1_ROW);
#pragma unroll
            for (int u = 0; u < 16; ++u) {
#pragma unroll
                for (int t = 0; t < 7; ++t) {
                    if (u + 1 < 16) wf[(u + 1) & 1][t] = wfrag(wb + wofs(u + 1) + t * 32, 8 * W1_ROW);
                    acc[0][t] = mfma16(wf[u & 1][t], asfrag(xa[0][u]), acc[0][t]);
                    acc[1][t] = mfma16(wf[u & 1][t], asfrag(xa[1][u]), acc[1][t]);
                }
#pragma unroll
                for (int t = 0; t < 7; ++t) { __builtin_amdgcn_sched_group_barrier(0x100, 2, 0); __builtin_amdgcn_sched_group_barrier(0x8, 2, 0); }
                SB();
            }
#pragma unroll
            for (int mt = 0; mt < 2; ++mt) {
                float S = 0.f, SS = 0.f;
#pragma unroll
                for (int i = 0; i < 4; ++i) { S += sv[mt][i].x; SS += sv[mt][i].y; }
                S = sum_xor16(S); SS = sum_xor16(SS); S = sum_xor32(S); SS = sum_xor32(SS);
                const float mean = S * (1.f / DM); const float var = fmaxf(SS * (1.f / DM) - mean * mean, 0.f);
                rstd[mt] = 1.f / sqrtf(var + EPS); mr[mt] = mean * rstd[mt];
            }
            TS(tb + 12);
            BAR();
            TS(tb + 13);
#pragma unroll
            for (int t = 0; t < 7; ++t) {
                const f32x4 c4 = *(LAS f32x4*)(cs1 + t * 16 + 4 * q);
                unsigned d[2][2];
#pragma unroll
                for (int mt = 0; mt < 2; ++mt) {
                    float hv[4];
#pragma unroll
                    for (int i = 0; i < 4; ++i) {
                        const float z = acc[mt][t][i] * rstd[mt] - mr[mt] * c4[i];
                        hv[i] = z * __builtin_amdgcn_rcpf(1.f + __builtin_amdgcn_exp2f(-LOG2E * z));
                    }
                    d[mt][0] = pk(hv[0], hv[1]); d[mt][1] = pk(hv[2], hv[3]);
                }
                auto s0 = __builtin_amdgcn_permlane16_swap(d[0][0], d[1][0], false, false);
                auto s1 = __builtin_amdgcn_permlane16_swap(d[0][1], d[1][1], false, false);
                stw(rs, lane * 16, H_OFF + lyr * 8 * MB + (T * 112 + 7 * r + t) * 1024, u32x4{s0[0], s1[0], s0[1], s1[1]});
            }
            DRAIN();
            BAR();
            if (tid == 0) set_flag(p, 4 + 5 * lyr, g, r, tag);
            TS(tb + 14);
        }
        {
            PHASE_VARS();
            const int mt = w & 1, kh = (w >> 1) & 1, T = 4 * g + 2 * m + mt, u = 2 * n + kh;
            const int T16 = 2 * T + ((lane & 31) >> 4);
            const int vo = (T16 * 16 * 64 + (lane & 15) + 16 * h) * 16;
            const u32x4 r0 = ldw(rs, vo, X2_OFF + lyr * 2 * MB + u * 1024), r1 = ldw(rs, vo + 512, X2_OFF + lyr * 2 * MB + u * 1024);
            if (w == 0) wait_flags(p, flagp(p, 4 + 5 * lyr, g, 0), 32, 16, tag, lane);
            BAR();
            TS(tb + 15);
            const int a = (lane & 15) >> 2, pp = lane & 3, ch = (lane >> 4) & 1;
            const int sw = ((a >> 1) & 1) << 6;
            const LAS char* wb0 = lds + L_W2 + kh * 8192 + (8 * h + a) * 128 + (0 ^ sw) + ch * 32 + pp * 8;
            const LAS char* wb1 = lds + L_W2 + kh * 8192 + (8 * h + a) * 128 + (64 ^ sw) + ch * 32 + pp * 8;
            f32x16 acc[2] = {};
            const int ho = H_OFF + lyr * 8 * MB + (T * 112 + 56 * kh) * 1024;
            u32x4 xa[36];
            auto compute = [&](int s0, int s1, int slot0, int xoff) {
                bf16x8 wf[4][2];
                auto woff = [&](int s) { return (slot0 + (s >> 2) - (s0 >> 2)) * 16384 + (s & 3) * 2048; };
#pragma unroll
                for (int s = s0; s < s0 + 3; ++s) { wf[s & 3][0] = wfrag(wb0 + woff(s), 512); wf[s & 3][1] = wfrag(wb1 + woff(s), 512); }
#pragma unroll
                for (int s = s0; s < s1; ++s) {
                    if (s + 3 < s1) { wf[(s + 3) & 3][0] = wfrag(wb0 + woff(s + 3), 512); wf[(s + 3) & 3][1] = wfrag(wb1 + woff(s + 3), 512); }
                    const bf16x8 x = asfrag(xa[s - xoff]);
                    acc[0] = mfma32(wf[s & 3][0], x, acc[0]); acc[1] = mfma32(wf[s & 3][1], x, acc[1]);
                    __builtin_amdgcn_sched_group_barrier(0x100, 2, 0); __builtin_amdgcn_sched_group_barrier(0x8, 1, 0);
                    __builtin_amdgcn_sched_group_barrier(0x100, 2, 0); __builtin_amdgcn_sched_group_barrier(0x8, 1, 0);
                    SB();
                }
            };
#pragma unroll
            for (int s = 0; s < 36; ++s) xa[s] = ldw(rs, lane * 16 + (s & 3) * 1024, ho + (s >> 2) * 4096);
            SB();
            compute(0, 20, 0, 0);
#pragma unroll
            for (int s = 0; s < 20; ++s) xa[s] = ldw(rs, lane * 16 + (s & 3) * 1024, ho + 36 * 1024 + (s >> 2) * 4096);
            SB();
            BAR();
            TS(tb + 16);
            compute(20, 36, 5, 0);
            BAR();
            TS(tb + 17);
            compute(36, 56, 0, 36);
            const f32x16 snd = kh ? acc[0] : acc[1];
#pragma unroll
            for (int g4 = 0; g4 < 4; ++g4) *(LAS f32x4*)(lds + L_RED4 + ((w * 4 + g4) * 64 + lane) * 16) = f32x4{snd[4 * g4], snd[4 * g4 + 1], snd[4 * g4 + 2], snd[4 * g4 + 3]};
            TS(tb + 18);
            BAR();
            const f32x16 own = kh ? acc[1] : acc[0];
            const int pw = w ^ 2;
            float v[16];
#pragma unroll
            for (int g4 = 0; g4 < 4; ++g4) { f32x4 t = *(LAS f32x4*)(lds + L_RED4 + ((pw * 4 + g4) * 64 + lane) * 16);
#pragma unroll
                for (int i = 0; i < 4; ++i) v[4 * g4 + i] = own[4 * g4 + i] + t[i]; }
            const unsigned rr[8] = {r0.x, r0.y, r0.z, r0.w, r1.x, r1.y, r1.z, r1.w};
            unsigned c[8];
#pragma unroll
            for (int i = 0; i < 8; ++i) c[i] = pk(v[2 * i] + bflo(rr[i]), v[2 * i + 1] + bfhi(rr[i]));
            if (lyr == 0) {
                stw(rs, lane * 16, XF_OFF + 2 * MB + (T * 32 + 2 * u) * 1024, u32x4{c[0], c[1], c[2], c[3]});
                stw(rs, lane * 16 + 1024, XF_OFF + 2 * MB + (T * 32 + 2 * u) * 1024, u32x4{c[4], c[5], c[6], c[7]});
                stats_store(rs, 2, T, u, lane, c);
                DRAIN();
                BAR();
                if (tid == 0) set_flag(p, 5, g, r, tag);
            } else {
                auto s0 = __builtin_amdgcn_permlane32_swap(c[0], c[2], false, false);
                auto s1 = __builtin_amdgcn_permlane32_swap(c[1], c[3], false, false);
                auto s2 = __builtin_amdgcn_permlane32_swap(c[4], c[6], false, false);
                auto s3 = __builtin_amdgcn_permlane32_swap(c[5], c[7], false, false);
                uint16_t* orow = p.out + (size_t)(T * 32 + (lane & 31)) * DM + 32 * u + 8 * h;
                *(GAS u32x4*)(orow) = u32x4{s0[0], s1[0], s0[1], s1[1]};
                *(GAS u32x4*)(orow + 16) = u32x4{s2[0], s3[0], s2[1], s3[1]};
            }
            TS(tb + 19);
        }
    }
}

DI void helper_path(const Params& p, LAS char* lds, unsigned lds0, rsrc_t rs, const int w, const int g_, const int r_, const unsigned tag) {
    const int hw = w - 4;
    { const int layer = 0; PHASE_VARS(); stage_wq(p, lds0, r, hw, lane); BAR(); }
    for (int layer = 0; layer < 2; ++layer) {
        {
            PHASE_VARS();
            DRAIN();
            BAR();
            if (lyr == 0 && w < 7) {
                const int a = (lane & 15) >> 2, pp = lane & 3, ch = (lane >> 4) & 1;
                const LAS char* wb = lds + L_WQ + (4 * h + a) * WQ_ROW + ch * 32 + pp * 8;
                LAS float* csq = (LAS float*)(lds + L_CSQ);
                const int tile = w - 4; float acc = 0.f;
#pragma unroll 4
                for (int s = 0; s < 32; ++s) acc = dot8_ones(wb + s * 16 * WQ_ROW + tile * 64, 8 * WQ_ROW, acc);
                acc = sum_xor32(acc);
                if (h == 0) csq[tile * 32 + tok] = acc;
            }
            if (w == 7 && lane < 2) *(LAS u32x4*)(lds + L_ONES + lane * 1024) = u32x4{0x3f803f80u, 0x3f803f80u, 0x3f803f80u, 0x3f803f80u};
            BAR();
            stage_wo(p, lds0, n, hw, lane);
            BAR();
            if (w == 7) { const int nh = g >= 2 ? 2 : g; wait_flags(p, flagp(p, 1 + 5 * lyr, g - 1, r), -16 * 32, nh, tag, lane); }
            BAR();
            halo_load(lds, rs, w, g, r, lyr, lane);
            BAR();
        }
        {   PHASE_VARS();
            attn_unit(lds, rs, w, g, r, lyr, lane);
            DRAIN();
            BAR();
            stage_w1(p, lds0, r, 0, 77, hw, lane);
        }
        {
            PHASE_VARS();
            BAR();
            BAR();
            stage_w1(p, lds0, r, 77, 112, hw, lane);
            BAR();
        }
        {
            PHASE_VARS();
            DRAIN();
            BAR();
            if (lyr == 0) {
                const int q = lane >> 4, a = (lane & 15) >> 2, pp = lane & 3;
                const LAS char* wb = lds + (16 * (q >> 1) + 4 * (q & 1) + a) * W1_ROW + pp * 8;
                LAS float* cs1 = (LAS float*)(lds + L_CS1);
#pragma unroll
                for (int tt = 0; tt < 2; ++tt) {
                    const int t = hw + 4 * tt;
                    if (t < 7) {
                        float s = 0.f;
#pragma unroll
                        for (int u = 0; u < 16; ++u) s = dot8_ones(wb + (u < 11 ? L_W1A + u * 7168 : L_W1B + (u - 11) * 7168) + t * 32, 8 * W1_ROW, s);
                        s = sum_xor16(s); s = sum_xor32(s);
                        if (lane < 16) cs1[t * 16 + lane] = s;
                    }
                }
            }
            BAR();
#pragma unroll
            for (int c = 0; c < 9; ++c) stage_w2(p, lds0, n, c, c, hw, lane);
            BAR();
        }
        {
            PHASE_VARS();
            DRAIN();
            BAR();
            BAR();
#pragma unroll
            for (int c = 9; c < 14; ++c) stage_w2(p, lds0, n, c, c - 9, hw, lane);
            DRAIN();
            BAR();
            BAR();
            if (lyr == 0) { stage_wq(p, lds0, r, hw, lane); BAR(); }
        }
    }
}

__global__ __launch_bounds__(512, 2) void k_mega(Params p) {
    extern __shared__ __attribute__((aligned(16))) char smem_[];
    LAS char* lds = (LAS char*)smem_;
    const unsigned lds0 = (unsigned)(uintptr_t)smem_;
    const int b = blockIdx.x, idx = b >> 3, g_ = (b & 7) * 2 + (idx >> 4), r_ = idx & 15;
    const int w = __builtin_amdgcn_readfirstlane(threadIdx.x >> 6);
    const unsigned tag = 1u;
    const rsrc_t rs = __builtin_amdgcn_make_buffer_rsrc((void*)p.ws, 0, WS_USED, 0x00020000);
    TS(0);
    if (w < 4) main_path(p, lds, rs, w, g_, r_, tag);
    else helper_path(p, lds, lds0, rs, w, g_, r_, tag);
}
}

extern "C" void kernel_launch(void* const* d_in, const int* in_sizes, int n_in, void* d_out, int out_size, void* d_ws, size_t ws_size, hipStream_t stream) {
    Params p;
    p.x = (const uint16_t*)d_in[0]; p.wqkv = (const uint16_t*)d_in[1]; p.wo = (const uint16_t*)d_in[2];
    p.w1 = (const uint16_t*)d_in[4]; p.w2 = (const uint16_t*)d_in[5];
    p.out = (uint16_t*)d_out; p.ws = (char*)d_ws;
    (void)hipFuncSetAttribute((const void*)k_mega, hipFuncAttributeMaxDynamicSharedMemorySize, LDS_BYTES);
    (void)hipMemsetAsync((char*)d_ws + FLAG_OFF, 0, FLAG_BYTES + 256, stream);
    k_mega<<<256, 512, LDS_BYTES, stream>>>(p);
}
```

```cpp
#include <hip/hip_runtime.h>
#include <hip/hip_bf16.h>
#include <stdint.h>

#define GAS __attribute__((address_space(1)))
#define LAS __attribute__((address_space(3)))
#define DI __device__ __forceinline__

typedef __bf16 bf16x8 __attribute__((ext_vector_type(8)));
typedef __bf16 bf16x2 __attribute__((ext_vector_type(2)));
typedef short s16x4 __attribute__((ext_vector_type(4)));
typedef float f32x16 __attribute__((ext_vector_type(16)));
typedef float f32x4 __attribute__((ext_vector_type(4)));
typedef float f32x2 __attribute__((ext_vector_type(2)));
typedef unsigned u32x4 __attribute__((ext_vector_type(4)));
typedef unsigned u32x2 __attribute__((ext_vector_type(2)));
typedef GAS unsigned gu32;

#define TS(i)
#define TS_DUMP()
#define HTS(i)
#define HTS_DUMP()

#define MAPMODE 0
namespace {
constexpr int DM = 512, FF = 1792;
constexpr float EPS = 1e-5f;
constexpr float LOG2E = 1.4426950408889634f;
constexpr float QSCALE = 0.25f * LOG2E;

constexpr int MB = 1 << 20, KB = 1024;
constexpr int XF_OFF = 0;
constexpr int O_OFF = 4 * MB;
constexpr int X2_OFF = 8 * MB;
constexpr int H_OFF = 12 * MB;
constexpr int KV_OFF = 28 * MB;
constexpr int WQT_OFF = 36 * MB;
constexpr int WOT_OFF = WQT_OFF + 1536 * KB;
constexpr int W1T_OFF = 38 * MB;
constexpr int W2T_OFF = 40 * MB;
constexpr int ST_OFF = 42 * MB;
constexpr int ST_BUF = 262144;
constexpr int FLAG_OFF = 43 * MB;
constexpr int FLAG_BYTES = 11 * 256 * 128;
constexpr int TMO_OFF = FLAG_OFF + FLAG_BYTES;
constexpr int EPOCH_OFF = TMO_OFF + 256;
constexpr int ACK_OFF = EPOCH_OFF + 256;
constexpr int WS_USED = 44 * MB;

constexpr int L_WQ = 0, L_Q = 96 * KB, L_KV = 104 * KB, L_ONES = 152 * KB;
constexpr int L_WO = 0, L_RED2 = 141 * KB;
constexpr int L_W2 = 0, L_RED4 = 96 * KB;
constexpr int L_SCR = 100 * KB;
constexpr int L_TAB = 158 * KB, L_CSQ = L_TAB, L_CS1 = L_TAB + 512, L_STT = L_TAB + 1024;
constexpr int LDS_BYTES = 160 * KB;
constexpr int w1_off(int s) { return s < 22 ? 64 * KB + s * 3584 : (s - 22) * 3584; }
constexpr int AUX_SC1 = 16;

struct Params {
    const uint16_t* x; const uint16_t* wqkv; const uint16_t* wo; const uint16_t* w1; const uint16_t* w2;
    uint16_t* out; char* ws;
};

DI f32x16 mfma32(bf16x8 a, bf16x8 b, f32x16 c) { return __builtin_amdgcn_mfma_f32_32x32x16_bf16(a, b, c, 0, 0, 0); }
DI unsigned pk(float a, float b) { bf16x2 v = {(__bf16)a, (__bf16)b}; return __builtin_bit_cast(unsigned, v); }
DI float bflo(unsigned u) { return __uint_as_float(u << 16); }
DI float bfhi(unsigned u) { return __uint_as_float(u & 0xffff0000u); }
DI bf16x8 asfrag(u32x4 v) { return __builtin_bit_cast(bf16x8, v); }
DI s16x4 trd(const LAS char* p) { return __builtin_amdgcn_ds_read_tr16_b64_v4i16((LAS s16x4*)p); }
DI u32x4 wfrag_tr(const LAS char* p, int off2) {
    const u32x2 lo = __builtin_bit_cast(u32x2, trd(p)), hi = __builtin_bit_cast(u32x2, trd(p + off2));
    return u32x4{lo.x, lo.y, hi.x, hi.y};
}
DI bf16x8 ldsfrag(const LAS char* p) { return *(const LAS bf16x8*)p; }
DI float dot8_ones(const LAS char* p, float acc) {
    const u32x4 f = *(const LAS u32x4*)p;
    unsigned w0 = f.x, w1 = f.y, w2 = f.z, w3 = f.w;
    asm volatile("v_dot2c_f32_bf16 %0, %1, %5\n\tv_dot2c_f32_bf16 %0, %2, %5\n\tv_dot2c_f32_bf16 %0, %3, %5\n\tv_dot2c_f32_bf16 %0, %4, %5\n\ts_nop 2"
                 : "+v"(acc) : "v"(w0), "v"(w1), "v"(w2), "v"(w3), "v"(0x3f803f80u));
    return acc;
}
DI float sum_xor32(float v) { auto r = __builtin_amdgcn_permlane32_swap(__float_as_uint(v), __float_as_uint(v), false, false); return __uint_as_float(r[0]) + __uint_as_float(r[1]); }
DI float max_xor32(float v) { auto r = __builtin_amdgcn_permlane32_swap(__float_as_uint(v), __float_as_uint(v), false, false); return fmaxf(__uint_as_float(r[0]), __uint_as_float(r[1])); }
template <bool SC1> DI void glds16s(const void* sbase, unsigned voff, unsigned lds_addr) {
    unsigned keep;
    if (SC1) asm volatile("s_mov_b32 %0, m0\n\ts_mov_b32 m0, %3\n\ts_nop 0\n\tglobal_load_lds_dwordx4 %1, %2 sc1\n\ts_mov_b32 m0, %0" : "=&s"(keep) : "v"(voff), "s"(sbase), "s"(lds_addr) : "memory");
    else asm volatile("s_mov_b32 %0, m0\n\ts_mov_b32 m0, %3\n\ts_nop 0\n\tglobal_load_lds_dwordx4 %1, %2\n\ts_mov_b32 m0, %0" : "=&s"(keep) : "v"(voff), "s"(sbase), "s"(lds_addr) : "memory");
}
#define BAR() asm volatile("s_waitcnt lgkmcnt(0)\n\ts_barrier" ::: "memory")
#define DRAIN() asm volatile("s_waitcnt vmcnt(0)" ::: "memory")
#define WAITV(n) asm volatile("s_waitcnt vmcnt(" #n ")" ::: "memory")
#define SB() __builtin_amdgcn_sched_barrier(0)
#define SGB(mask, n) __builtin_amdgcn_sched_group_barrier(mask, n, 0)

typedef __amdgpu_buffer_rsrc_t rsrc_t;
DI u32x4 ldw(rsrc_t rs, int voff, int soff) { return __builtin_amdgcn_raw_buffer_load_b128(rs, voff, soff, AUX_SC1); }
DI void stw(rsrc_t rs, int voff, int soff, u32x4 v) { __builtin_amdgcn_raw_buffer_store_b128(v, rs, voff + soff, 0, AUX_SC1); }
DI f32x2 ldst(rsrc_t rs, int voff, int soff) { return __builtin_bit_cast(f32x2, __builtin_amdgcn_raw_buffer_load_b64(rs, voff, soff, AUX_SC1)); }
DI void stst(rsrc_t rs, int voff, int soff, f32x2 v) { __builtin_amdgcn_raw_buffer_store_b64(__builtin_bit_cast(u32x2, v), rs, voff, soff, AUX_SC1); }

DI gu32* flagp(const Params& p, int ph, int g, int r) { return (gu32*)(p.ws + FLAG_OFF) + ((ph * 16 + g) * 16 + r) * 32; }
DI void set_flag(const Params& p, int ph, int g, int r, unsigned tag) { __hip_atomic_store(flagp(p, ph, g, r), tag, __ATOMIC_RELAXED, __HIP_MEMORY_SCOPE_AGENT); }
DI void wait_word(const Params& p, gu32* a, unsigned tag) {
    for (unsigned spins = 0;; ++spins) {
        const unsigned v = __hip_atomic_load(a, __ATOMIC_RELAXED, __HIP_MEMORY_SCOPE_AGENT);
        if (__all(v == tag)) break;
        if (spins > (1u << 20)) { __hip_atomic_store((gu32*)(p.ws + TMO_OFF), 1u, __ATOMIC_RELAXED, __HIP_MEMORY_SCOPE_AGENT); break; }
        __builtin_amdgcn_s_sleep(1);
    }
    __builtin_amdgcn_fence(__ATOMIC_ACQUIRE, "wavefront");
}
DI void wait_group(const Params& p, int ph, int g, unsigned tag, int lane) { wait_word(p, flagp(p, ph, g, lane & 15), tag); }

DI void stats_store(rsrc_t rs, int buf, int T, int u, int lane, const unsigned (&c)[8]) {
    float s = 0.f, ss = 0.f;
#pragma unroll
    for (int i = 0; i < 8; ++i) { float lo = bflo(c[i]), hi = bfhi(c[i]); s += lo + hi; ss += lo * lo + hi * hi; }
    s = sum_xor32(s); ss = sum_xor32(ss);
    if (lane < 32) stst(rs, lane * 8, ST_OFF + buf * ST_BUF + (T * 16 + u) * 256, f32x2{s, ss});
}

DI void stage_lin(const Params& p, unsigned lds0, int src, int dst, int n, int hw, int lane) {
    const unsigned vo = lane * 16;
    for (int i = hw; i < n; i += 4) glds16s<true>(p.ws + src + i * 1024, vo, lds0 + dst + i * 1024);
}
constexpr int w2t_off(int n, int c) { return W2T_OFF + (n * 14 + c) * 16384; }

#define PHASE_VARS() \
    int lane = threadIdx.x & 63; asm volatile("" : "+v"(lane)); \
    int lyr = layer; asm volatile("" : "+s"(lyr)); \
    int g = g_, r = r_; asm volatile("" : "+s"(g), "+s"(r)); \
    const int h = lane >> 5, tok = lane & 31; const int m = r & 1, n = r >> 1; (void)h; (void)tok; (void)m; (void)n; (void)lyr;

DI void attn_finish(rsrc_t rs, const f32x16& o, int hd, int T, int r, int lyr, int lane) {
    float ov[8]; float l;
    if (hd == 0) { l = o[8];
#pragma unroll
        for (int i = 0; i < 8; ++i) ov[i] = o[i];
    } else { l = o[0];
#pragma unroll
        for (int i = 0; i < 8; ++i) ov[i] = o[8 + i];
    }
    const float inv = __builtin_amdgcn_rcpf(l);
    stw(rs, lane * 16, O_OFF + lyr * 2 * MB + (T * 32 + 2 * r + hd) * 1024,
        u32x4{pk(ov[0] * inv, ov[1] * inv), pk(ov[2] * inv, ov[3] * inv), pk(ov[4] * inv, ov[5] * inv), pk(ov[6] * inv, ov[7] * inv)});
}
DI float max16(float m, const f32x16& s) {
#pragma unroll
    for (int i = 0; i < 16; i += 2) m = fmaxf(fmaxf(m, s[i]), s[i + 1]);
    return m;
}
DI void max16x2(float& m0, float& m1, const f32x16& s) {
#pragma unroll
    for (int i = 0; i < 16; i += 4) { m0 = fmaxf(fmaxf(m0, s[i]), s[i + 1]); m1 = fmaxf(fmaxf(m1, s[i + 2]), s[i + 3]); }
}
DI void exp_pack(const f32x16& s, u32x4& p0, u32x4& p1) {
    unsigned pw[8];
#pragma unroll
    for (int i = 0; i < 8; ++i) pw[i] = pk(__builtin_amdgcn_exp2f(s[2 * i]), __builtin_amdgcn_exp2f(s[2 * i + 1]));
    p0 = u32x4{pw[0], pw[1], pw[2], pw[3]}; p1 = u32x4{pw[4], pw[5], pw[6], pw[7]};
}
DI void attn_dma(const Params& p, unsigned lds0, int i, int r, int lane) {
    const int n = r >> 1;
    if (i < 64) glds16s<true>(p.ws + WOT_OFF + n * 65536 + i * 1024, lane * 16, lds0 + L_WO + i * 1024);
    else if (i < 92) glds16s<true>(p.ws + W1T_OFF + r * 114688 + (i - 64) * 1024, lane * 16, lds0 + 64 * KB + (i - 64) * 1024);
}
DI void attn_unit(const Params& p, LAS char* lds, unsigned lds0, rsrc_t rs, int w, int g, int r, int lyr, int lane) {
    const int h = lane >> 5, tok = lane & 31;
    const int tw = w & 3, hd = w >> 2, T = 4 * g + tw;
    const bf16x8 qf = *(const LAS bf16x8*)(lds + L_Q + tw * 2048 + hd * 1024 + lane * 16);
    f32x16 mD, mF;
#pragma unroll
    for (int i = 0; i < 16; ++i) { const int kk = (i & 3) + 8 * (i >> 2) + 4 * h; mD[i] = kk <= tok ? 0.f : -INFINITY; mF[i] = kk > tok ? 0.f : -INFINITY; }
    const LAS char* kb = lds + L_KV + tw * 4096 + hd * 1024 + lane * 16;
    const bool isdata = ((lane >> 4) & 1) == hd;
    const LAS char* vb = isdata ? (lds + L_KV + tw * 4096 + 2048 + lane * 16) : (lds + L_ONES);
    const int vstep = isdata ? 4096 : 0;
    const f32x16 zero = {};
    f32x16 o = {};
    if (T >= 8) {
        bf16x8 kf[9];
#pragma unroll
        for (int jj = 0; jj < 9; ++jj) kf[jj] = *(const LAS bf16x8*)(kb + jj * 4096);
        float mx = -INFINITY, mx1 = -INFINITY;
        f32x16 s[2];
        s[0] = mfma32(kf[0], qf, mF); s[1] = mfma32(kf[1], qf, zero);
#pragma unroll
        for (int jj = 0; jj < 9; ++jj) {
            const f32x16 cur = s[jj & 1];
            max16x2(mx, mx1, cur);
            if (jj + 2 < 9) s[jj & 1] = mfma32(kf[jj + 2], qf, jj + 2 == 8 ? mD : zero);
            if (jj < 3) attn_dma(p, lds0, w + 8 * jj, r, lane);
            SB();
        }
        mx = max_xor32(fmaxf(mx, mx1));
        f32x16 cm, cD, cF;
#pragma unroll
        for (int i = 0; i < 16; ++i) { cm[i] = -mx; cD[i] = mD[i] - mx; cF[i] = mF[i] - mx; }
        bf16x8 v0 = *(const LAS bf16x8*)(vb), v1 = *(const LAS bf16x8*)(vb + 1024);
        s[0] = mfma32(kf[0], qf, cF); s[1] = mfma32(kf[1], qf, cm);
#pragma unroll
        for (int jj = 0; jj < 9; ++jj) {
            u32x4 p0, p1;
            exp_pack(s[jj & 1], p0, p1);
            if (jj + 2 < 9) s[jj & 1] = mfma32(kf[jj + 2], qf, jj + 2 == 8 ? cD : cm);
            o = mfma32(v0, asfrag(p0), o);
            o = mfma32(v1, asfrag(p1), o);
            if (jj + 1 < 9) { const LAS char* va = vb + (jj + 1) * vstep; v0 = *(const LAS bf16x8*)(va); v1 = *(const LAS bf16x8*)(va + 1024); }
            attn_dma(p, lds0, w + 8 * (3 + jj), r, lane);
            SB();
        }
    } else {
        for (int i = w; i < 92; i += 8) attn_dma(p, lds0, i, r, lane);
        const int j0 = 8 - T;
        float mx = -INFINITY;
        for (int jj = j0; jj < 9; ++jj) {
            const bf16x8 kf = *(const LAS bf16x8*)(kb + jj * 4096);
            f32x16 s = mfma32(kf, qf, zero);
            if (jj == 8) {
#pragma unroll
                for (int i = 0; i < 16; ++i) s[i] += mD[i];
            }
            mx = max16(mx, s);
        }
        mx = max_xor32(mx);
        for (int jj = j0; jj < 9; ++jj) {
            const bf16x8 kf = *(const LAS bf16x8*)(kb + jj * 4096);
            const LAS char* va = vb + jj * vstep;
            const bf16x8 v0 = *(const LAS bf16x8*)(va), v1 = *(const LAS bf16x8*)(va + 1024);
            f32x16 s = mfma32(kf, qf, zero);
#pragma unroll
            for (int i = 0; i < 16; ++i) s[i] = (jj == 8 ? s[i] + mD[i] : s[i]) - mx;
            u32x4 p0, p1;
            exp_pack(s, p0, p1);
            o = mfma32(v0, asfrag(p0), o);
            o = mfma32(v1, asfrag(p1), o);
        }
    }
    attn_finish(rs, o, hd, T, r, lyr, lane);
}
DI void halo_load(LAS char* lds, rsrc_t rs, int w, int g, int r, int lyr, int lane) {
    const int tile = 4 * g - 8 + w;
    if (tile >= 0) {
        const int kvo = KV_OFF + lyr * 4 * MB + (tile * 16 + r) * 4096;
        u32x4 t0 = ldw(rs, lane * 16, kvo), t1 = ldw(rs, lane * 16 + 1024, kvo), t2 = ldw(rs, lane * 16 + 2048, kvo), t3 = ldw(rs, lane * 16 + 3072, kvo);
        LAS char* kvl = lds + L_KV + w * 4096 + lane * 16;
        *(LAS u32x4*)(kvl) = t0; *(LAS u32x4*)(kvl + 1024) = t1; *(LAS u32x4*)(kvl + 2048) = t2; *(LAS u32x4*)(kvl + 3072) = t3;
    }
}
DI void ln_stats(const f32x2 (&sv)[8], float& mean, float& rstd) {
    float S = 0.f, SS = 0.f;
#pragma unroll
    for (int i = 0; i < 8; ++i) { S += sv[i].x; SS += sv[i].y; }
    S = sum_xor32(S); SS = sum_xor32(SS);
    mean = S * (1.f / DM); const float var = fmaxf(SS * (1.f / DM) - mean * mean, 0.f); rstd = 1.f / sqrtf(var + EPS);
}
DI unsigned read_tag(const Params& p) {
    return (unsigned)__builtin_amdgcn_readfirstlane(__hip_atomic_load((gu32*)(p.ws + EPOCH_OFF), __ATOMIC_RELAXED, __HIP_MEMORY_SCOPE_AGENT)) + 1u;
}
#define LDA(arr, s, base) arr[s] = ldw(rs, lane * 16 + ((s) & 3) * 1024, (base) + ((s) >> 2) * 4096)

#define PD0 4
#define PDV 12
constexpr int a_issued(int s, int total) { int v = PD0 + 2 * (s + 1); const int cap = s + 1 + PDV; v = v < cap ? v : cap; return v < total ? v : total; }

DI void main_path(const Params& p, LAS char* lds, rsrc_t rs, const int w, const int g_, const int r_) {
    const int tid = threadIdx.x;
    unsigned tag = 0;
    {
        const int layer = 0; PHASE_VARS();
        const int T = 4 * g + (r & 3), u = (r >> 2) * 4 + w;
        const uint16_t* row = p.x + (size_t)(T * 32 + tok) * DM + 32 * u + 4 * h;
        u32x2 a0 = *(const GAS u32x2*)(row), a1 = *(const GAS u32x2*)(row + 8), a2 = *(const GAS u32x2*)(row + 16), a3 = *(const GAS u32x2*)(row + 24);
        if (w == 0) { tag = read_tag(p); asm volatile("" :: "s"(tag) : "memory"); }
        stw(rs, lane * 16, XF_OFF + (T * 32 + 2 * u) * 1024, u32x4{a0.x, a0.y, a1.x, a1.y});
        stw(rs, lane * 16, XF_OFF + (T * 32 + 2 * u + 1) * 1024, u32x4{a2.x, a2.y, a3.x, a3.y});
        const unsigned c[8] = {a0.x, a0.y, a1.x, a1.y, a2.x, a2.y, a3.x, a3.y};
        stats_store(rs, 0, T, u, lane, c);
        DRAIN();
        BAR();
        if (tid == 0) { set_flag(p, 0, g, r, tag); __hip_atomic_store((gu32*)(p.ws + ACK_OFF) + blockIdx.x * 32, tag, __ATOMIC_RELAXED, __HIP_MEMORY_SCOPE_AGENT); }
    }
    TS(1);
    for (int layer = 0; layer < 2; ++layer) {
        const int tb = 2 + layer * 24; (void)tb;
        {
            PHASE_VARS();
            if (w == 0) {
                if (lyr == 0) {
                    const int q = lane >> 4, j = lane & 15;
                    wait_word(p, q == 0 ? flagp(p, 0, g, j) : flagp(p, 0, j, q == 2 ? (r ^ 1) : r), tag);
                } else wait_group(p, 5, g, tag, lane);
            }
            BAR();
            TS(tb + 0);
            const int T = 4 * g + w;
            const LAS char* wb = lds + L_WQ + lane * 16;
            LAS float* csq = (LAS float*)(lds + L_CSQ);
            LAS float* stt = (LAS float*)(lds + L_STT);
            f32x16 aq = {}, ak = {}, av = {};
            u32x4 xa[32];
            f32x2 sv[8];
            const int sto = ST_OFF + 2 * lyr * ST_BUF + T * 4096, xo = XF_OFF + lyr * 2 * MB + T * 32768;
#pragma unroll
            for (int s = 0; s < PD0; ++s) { LDA(xa, s, xo); SB(); }
            if (lyr == 0) BAR();
            bf16x8 wf[3][3];
#pragma unroll
            for (int s = 0; s < 2; ++s)
#pragma unroll
                for (int t = 0; t < 3; ++t) wf[s][t] = ldsfrag(wb + (s * 3 + t) * 1024);
#pragma unroll
            for (int s = 0; s < 32; ++s) {
                if ((s == 6 || s == 14 || s == 22) && lyr == 0) BAR();
#pragma unroll
                for (int j = (s == 0 ? PD0 : a_issued(s - 1, 32)); j < a_issued(s, 32); ++j) LDA(xa, j, xo);
                if (s >= 16 && s < 24) sv[s - 16] = ldst(rs, (8 * h * 32 + tok) * 8 + (s - 16) * 256, sto);
                if (s + 2 < 32) {
#pragma unroll
                    for (int t = 0; t < 3; ++t) wf[(s + 2) % 3][t] = ldsfrag(wb + ((s + 2) * 3 + t) * 1024);
                }
                const bf16x8 x = asfrag(xa[s]);
                aq = mfma32(wf[s % 3][0], x, aq); ak = mfma32(wf[s % 3][1], x, ak); av = mfma32(x, wf[s % 3][2], av);
                SGB(0x100, 1); SGB(0x8, 1); SGB(0x20, 1); SGB(0x100, 1); SGB(0x8, 1); SGB(0x20, 1); SGB(0x100, 1); SGB(0x8, 1);
                SB();
            }
            float mean, rstd; ln_stats(sv, mean, rstd);
            if (h == 0) { stt[w * 64 + tok] = rstd; stt[w * 64 + 32 + tok] = mean * rstd; }
            TS(tb + 1);
            BAR();
            TS(tb + 2);
            const float mr = mean * rstd;
            float q[16], k[16], v[16];
            const float cv = csq[64 + tok];
#pragma unroll
            for (int g4 = 0; g4 < 4; ++g4) {
                f32x4 cq = *(LAS f32x4*)(csq + 8 * g4 + 4 * h), ck = *(LAS f32x4*)(csq + 32 + 8 * g4 + 4 * h);
                f32x4 rsv = *(LAS f32x4*)(stt + w * 64 + 8 * g4 + 4 * h), ms = *(LAS f32x4*)(stt + w * 64 + 32 + 8 * g4 + 4 * h);
#pragma unroll
                for (int i = 0; i < 4; ++i) {
                    k[4 * g4 + i] = ak[4 * g4 + i] * rstd - mr * ck[i];
                    v[4 * g4 + i] = av[4 * g4 + i] * rsv[i] - ms[i] * cv;
                    q[4 * g4 + i] = (aq[4 * g4 + i] * rstd - mr * cq[i]) * QSCALE;
                }
            }
            const u32x4 k0 = {pk(k[0], k[1]), pk(k[2], k[3]), pk(k[4], k[5]), pk(k[6], k[7])};
            const u32x4 k1 = {pk(k[8], k[9]), pk(k[10], k[11]), pk(k[12], k[13]), pk(k[14], k[15])};
            const u32x4 v0 = {pk(v[0], v[1]), pk(v[2], v[3]), pk(v[4], v[5]), pk(v[6], v[7])};
            const u32x4 v1 = {pk(v[8], v[9]), pk(v[10], v[11]), pk(v[12], v[13]), pk(v[14], v[15])};
            const int kvo = KV_OFF + lyr * 4 * MB + (T * 16 + r) * 4096;
            stw(rs, lane * 16, kvo, k0); stw(rs, lane * 16 + 1024, kvo, k1); stw(rs, lane * 16 + 2048, kvo, v0); stw(rs, lane * 16 + 3072, kvo, v1);
            LAS char* kvl = lds + L_KV + (8 + w) * 4096 + lane * 16;
            *(LAS u32x4*)(kvl) = k0; *(LAS u32x4*)(kvl + 1024) = k1; *(LAS u32x4*)(kvl + 2048) = v0; *(LAS u32x4*)(kvl + 3072) = v1;
            LAS char* ql = lds + L_Q + w * 2048 + lane * 16;
            *(LAS u32x4*)(ql) = u32x4{pk(q[0], q[1]), pk(q[2], q[3]), pk(q[4], q[5]), pk(q[6], q[7])};
            *(LAS u32x4*)(ql + 1024) = u32x4{pk(q[8], q[9]), pk(q[10], q[11]), pk(q[12], q[13]), pk(q[14], q[15])};
            DRAIN();
            BAR();
            if (tid == 0) set_flag(p, 1 + 5 * lyr, g, r, tag);
            TS(tb + 3);
            BAR();
            TS(tb + 4);
            halo_load(lds, rs, w, g, r, lyr, lane);
            BAR();
            TS(tb + 5);
        }
        {
            PHASE_VARS();
            attn_unit(p, lds, (unsigned)(uintptr_t)lds, rs, w, g, r, lyr, lane);
            TS(tb + 6);
            DRAIN();
            BAR();
            if (tid == 0) set_flag(p, 2 + 5 * lyr, g, r, tag);
            TS(tb + 7);
        }
        {
            PHASE_VARS();
            const int mt = w & 1, kh = (w >> 1) & 1, T = 4 * g + 2 * m + mt, u = 2 * n + kh;
            const int xo = XF_OFF + lyr * 2 * MB + (T * 32 + 2 * u) * 1024;
            const u32x4 r0 = ldw(rs, lane * 16, xo), r1 = ldw(rs, lane * 16 + 1024, xo);
            if (w == 0) wait_group(p, 2 + 5 * lyr, g, tag, lane);
            BAR();
            TS(tb + 8);
            f32x16 acc[2] = {};
            u32x4 xa[16];
            const int oo = O_OFF + lyr * 2 * MB + (T * 32 + 16 * kh) * 1024;
#pragma unroll
            for (int s = 0; s < PD0; ++s) { LDA(xa, s, oo); SB(); }
            const LAS char* wb = lds + L_WO + kh * 32768 + lane * 16;
            bf16x8 wf[4][2];
#pragma unroll
            for (int s = 0; s < 3; ++s) { wf[s][0] = ldsfrag(wb + s * 2048); wf[s][1] = ldsfrag(wb + s * 2048 + 1024); }
#pragma unroll
            for (int s = 0; s < 16; ++s) {
#pragma unroll
                for (int j = (s == 0 ? PD0 : a_issued(s - 1, 16)); j < a_issued(s, 16); ++j) LDA(xa, j, oo);
                if (s + 3 < 16) { wf[(s + 3) & 3][0] = ldsfrag(wb + (s + 3) * 2048); wf[(s + 3) & 3][1] = ldsfrag(wb + (s + 3) * 2048 + 1024); }
                const bf16x8 x = asfrag(xa[s]);
                acc[0] = mfma32(wf[s & 3][0], x, acc[0]); acc[1] = mfma32(wf[s & 3][1], x, acc[1]);
                SGB(0x100, 1); SGB(0x8, 1); SGB(0x20, 2); SGB(0x100, 1); SGB(0x8, 1);
                SB();
            }
            const f32x16 snd = kh ? acc[0] : acc[1];
#pragma unroll
            for (int g4 = 0; g4 < 4; ++g4) *(LAS f32x4*)(lds + L_RED2 + ((w * 4 + g4) * 64 + lane) * 16) = f32x4{snd[4 * g4], snd[4 * g4 + 1], snd[4 * g4 + 2], snd[4 * g4 + 3]};
            TS(tb + 9);
            BAR();
            const f32x16 own = kh ? acc[1] : acc[0];
            const int pw = w ^ 2;
            float v[16];
#pragma unroll
            for (int g4 = 0; g4 < 4; ++g4) { f32x4 t = *(LAS f32x4*)(lds + L_RED2 + ((pw * 4 + g4) * 64 + lane) * 16);
#pragma unroll
                for (int i = 0; i < 4; ++i) v[4 * g4 + i] = own[4 * g4 + i] + t[i]; }
            const unsigned rr[8] = {r0.x, r0.y, r0.z, r0.w, r1.x, r1.y, r1.z, r1.w};
            unsigned c[8];
#pragma unroll
            for (int i = 0; i < 8; ++i) c[i] = pk(v[2 * i] + bflo(rr[i]), v[2 * i + 1] + bfhi(rr[i]));
            const int yo = X2_OFF + lyr * 2 * MB + (T * 32 + 2 * u) * 1024;
            stw(rs, lane * 16, yo, u32x4{c[0], c[1], c[2], c[3]});
            stw(rs, lane * 16 + 1024, yo, u32x4{c[4], c[5], c[6], c[7]});
            stats_store(rs, 2 * lyr + 1, T, u, lane, c);
            DRAIN();
            BAR();
            if (tid == 0) set_flag(p, 3 + 5 * lyr, g, r, tag);
            TS(tb + 10);
        }
        {
            PHASE_VARS();
            if (w == 0) wait_group(p, 3 + 5 * lyr, g, tag, lane);
            BAR();
            TS(tb + 11);
            const int T = 4 * g + w;
            const LAS char* wbA = lds + lane * 16;
            const LAS char* wbB = lds + 3072 + (h * 16 + (lane & 15)) * 16;
            LAS float* cs1 = (LAS float*)(lds + L_CS1);
            f32x16 acc[4] = {};
            u32x4 xa[32];
            f32x2 sv[8];
            const int sto = ST_OFF + (2 * lyr + 1) * ST_BUF + T * 4096, xo = X2_OFF + lyr * 2 * MB + T * 32768;
#pragma unroll
            for (int s = 0; s < PD0; ++s) { LDA(xa, s, xo); SB(); }
            auto ldW = [&](int s, int t) { return t < 3 ? ldsfrag(wbA + w1_off(s) + t * 1024) : ldsfrag(wbB + w1_off(s)); };
            bf16x8 wf[2][4];
#pragma unroll
            for (int t = 0; t < 4; ++t) wf[0][t] = ldW(0, t);
#pragma unroll
            for (int s = 0; s < 32; ++s) {
                if (s == 20) BAR();
#pragma unroll
                for (int j = (s == 0 ? PD0 : a_issued(s - 1, 32)); j < a_issued(s, 32); ++j) LDA(xa, j, xo);
                if (s >= 16 && s < 24) sv[s - 16] = ldst(rs, (8 * h * 32 + tok) * 8 + (s - 16) * 256, sto);
                const bf16x8 x = asfrag(xa[s]);
#pragma unroll
                for (int t = 0; t < 4; ++t) {
                    if (s + 1 < 32) wf[(s + 1) & 1][t] = ldW(s + 1, t);
                    acc[t] = mfma32(wf[s & 1][t], x, acc[t]);
                }
                SGB(0x100, 1); SGB(0x8, 1); SGB(0x20, 1); SGB(0x100, 1); SGB(0x8, 1); SGB(0x20, 1); SGB(0x100, 1); SGB(0x8, 1); SGB(0x100, 1); SGB(0x8, 1);
                SB();
            }
            float mean, rstd; ln_stats(sv, mean, rstd);
            const float mr = mean * rstd;
            TS(tb + 12);
            BAR();
            TS(tb + 13);
            const int ho = H_OFF + lyr * 8 * MB + (T * 112 + 7 * r) * 1024;
#pragma unroll
            for (int t = 0; t < 4; ++t) {
#pragma unroll
                for (int sp = 0; sp < (t < 3 ? 2 : 1); ++sp) {
                    float hv[8];
#pragma unroll
                    for (int g2 = 0; g2 < 2; ++g2) {
                        const f32x4 c4 = *(LAS f32x4*)(cs1 + t * 32 + 16 * sp + 8 * g2 + 4 * h);
#pragma unroll
                        for (int i = 0; i < 4; ++i) {
                            const float z = acc[t][8 * sp + 4 * g2 + i] * rstd - mr * c4[i];
                            hv[4 * g2 + i] = z * __builtin_amdgcn_rcpf(1.f + __builtin_amdgcn_exp2f(-LOG2E * z));
                        }
                    }
                    stw(rs, lane * 16 + (2 * t + sp) * 1024, ho, u32x4{pk(hv[0], hv[1]), pk(hv[2], hv[3]), pk(hv[4], hv[5]), pk(hv[6], hv[7])});
                }
            }
            DRAIN();
            BAR();
            if (tid == 0) set_flag(p, 4 + 5 * lyr, g, r, tag);
            TS(tb + 14);
        }
        {
            PHASE_VARS();
            const int mt = w & 1, kh = (w >> 1) & 1, T = 4 * g + 2 * m + mt, u = 2 * n + kh;
            const int yo = X2_OFF + lyr * 2 * MB + (T * 32 + 2 * u) * 1024;
            const u32x4 r0 = ldw(rs, lane * 16, yo), r1 = ldw(rs, lane * 16 + 1024, yo);
            if (w == 0) wait_group(p, 4 + 5 * lyr, g, tag, lane);
            BAR();
            TS(tb + 15);
            const LAS char* wb = lds + L_W2 + kh * 8192 + lane * 16;
            f32x16 acc[2] = {};
            const int ho = H_OFF + lyr * 8 * MB + (T * 112 + 56 * kh) * 1024;
            u32x4 xa[56];
#pragma unroll
            for (int s = 0; s < PD0; ++s) { LDA(xa, s, ho); SB(); }
            auto woff = [](int s) { const int c = s >> 2; return (c < 9 ? c : c - 9) * 16384 + (s & 3) * 2048; };
            bf16x8 wf[4][2];
#pragma unroll
            for (int s = 0; s < 3; ++s) { wf[s][0] = ldsfrag(wb + woff(s)); wf[s][1] = ldsfrag(wb + woff(s) + 1024); }
#pragma unroll
            for (int s = 0; s < 56; ++s) {
                if (s == 17) { BAR(); TS(tb + 16); }
                if (s == 33) { BAR(); TS(tb + 17); }
#pragma unroll
                for (int j = (s == 0 ? PD0 : a_issued(s - 1, 56)); j < a_issued(s, 56); ++j) LDA(xa, j, ho);
                if (s + 3 < 56) { wf[(s + 3) & 3][0] = ldsfrag(wb + woff(s + 3)); wf[(s + 3) & 3][1] = ldsfrag(wb + woff(s + 3) + 1024); }
                const bf16x8 x = asfrag(xa[s]);
                acc[0] = mfma32(wf[s & 3][0], x, acc[0]); acc[1] = mfma32(wf[s & 3][1], x, acc[1]);
                SGB(0x100, 1); SGB(0x8, 1); SGB(0x20, 2); SGB(0x100, 1); SGB(0x8, 1);
                SB();
            }
            const f32x16 snd = kh ? acc[0] : acc[1];
#pragma unroll
            for (int g4 = 0; g4 < 4; ++g4) *(LAS f32x4*)(lds + L_RED4 + ((w * 4 + g4) * 64 + lane) * 16) = f32x4{snd[4 * g4], snd[4 * g4 + 1], snd[4 * g4 + 2], snd[4 * g4 + 3]};
            TS(tb + 18);
            BAR();
            const f32x16 own = kh ? acc[1] : acc[0];
            const int pw = w ^ 2;
            float v[16];
#pragma unroll
            for (int g4 = 0; g4 < 4; ++g4) { f32x4 t = *(LAS f32x4*)(lds + L_RED4 + ((pw * 4 + g4) * 64 + lane) * 16);
#pragma unroll
                for (int i = 0; i < 4; ++i) v[4 * g4 + i] = own[4 * g4 + i] + t[i]; }
            const unsigned rr[8] = {r0.x, r0.y, r0.z, r0.w, r1.x, r1.y, r1.z, r1.w};
            unsigned c[8];
#pragma unroll
            for (int i = 0; i < 8; ++i) c[i] = pk(v[2 * i] + bflo(rr[i]), v[2 * i + 1] + bfhi(rr[i]));
            if (lyr == 0) {
                stw(rs, lane * 16, XF_OFF + 2 * MB + (T * 32 + 2 * u) * 1024, u32x4{c[0], c[1], c[2], c[3]});
                stw(rs, lane * 16 + 1024, XF_OFF + 2 * MB + (T * 32 + 2 * u) * 1024, u32x4{c[4], c[5], c[6], c[7]});
                stats_store(rs, 2, T, u, lane, c);
                DRAIN();
                BAR();
                if (tid == 0) set_flag(p, 5, g, r, tag);
            } else {
                auto s0 = __builtin_amdgcn_permlane32_swap(c[0], c[2], false, false);
                auto s1 = __builtin_amdgcn_permlane32_swap(c[1], c[3], false, false);
                auto s2 = __builtin_amdgcn_permlane32_swap(c[4], c[6], false, false);
                auto s3 = __builtin_amdgcn_permlane32_swap(c[5], c[7], false, false);
                uint16_t* orow = p.out + (size_t)(T * 32 + (lane & 31)) * DM + 32 * u + 8 * h;
                *(GAS u32x4*)(orow) = u32x4{s0[0], s1[0], s0[1], s1[1]};
                *(GAS u32x4*)(orow + 16) = u32x4{s2[0], s3[0], s2[1], s3[1]};
            }
            TS(tb + 19);
        }
    }
    asm volatile("s_waitcnt lgkmcnt(0)" ::: "memory");
    if (w == 0) TS_DUMP();
}

DI void pretile(const Params& p, LAS char* lds, unsigned lds0, rsrc_t rs, int hw, int g, int r, int lane) {
    const int h = lane >> 5, a = (lane & 15) >> 2, pp = lane & 3, ch = (lane >> 4) & 1;
    const int n = r >> 1, j = 2 * g + (r & 1);
    const unsigned scr0 = lds0 + L_SCR + hw * 8192;
    const LAS char* scr = lds + L_SCR + hw * 8192;
    if (hw == 0) {
        const char* base = (const char*)(p.wqkv + (size_t)32 * g * 1536 + 32 * r);
#pragma unroll
        for (int i = 0; i < 6; ++i) { const int L = 64 * i + lane, k = L / 12, c = L % 12; glds16s<false>(base, (unsigned)(k * 1536 + (c >> 2) * 512 + (c & 3) * 8) * 2, scr0 + i * 1024); }
        DRAIN();
        const LAS char* wb = scr + (4 * h + a) * 192 + ch * 32 + pp * 8;
#pragma unroll
        for (int s = 0; s < 2; ++s)
#pragma unroll
            for (int t = 0; t < 3; ++t) stw(rs, lane * 16, WQT_OFF + ((r * 32 + 2 * g + s) * 3 + t) * 1024, wfrag_tr(wb + s * 16 * 192 + t * 64, 8 * 192));
    } else if (hw == 1) {
        const char* base = (const char*)(p.w1 + (size_t)32 * g * FF + 112 * r);
#pragma unroll
        for (int b = 0; b < 7; ++b) {
            const int L = 64 * b + lane, G = L / 56, x = L - G * 56;
            const int aa = x < 48 ? ((x >> 2) & 3) : ((x - 48) >> 1);
            const int c = x < 48 ? ((x >> 4) * 4 + (x & 3)) : (12 + (x & 1));
            glds16s<false>(base, (unsigned)((4 * G + aa) * FF + c * 8) * 2, scr0 + b * 1024);
        }
        DRAIN();
        const LAS char* wbA = scr + h * 896 + a * 64 + ch * 32 + pp * 8;
        const LAS char* wbB = scr + h * 896 + 768 + a * 32 + pp * 8;
#pragma unroll
        for (int s = 0; s < 2; ++s) {
            const int dst = W1T_OFF + r * 114688 + (2 * g + s) * 3584;
#pragma unroll
            for (int t = 0; t < 3; ++t) stw(rs, lane * 16, dst + t * 1024, wfrag_tr(wbA + s * 3584 + t * 256, 1792));
            const u32x4 f3 = wfrag_tr(wbB + s * 3584, 1792);
            if (ch == 0) stw(rs, (h * 16 + (lane & 15)) * 16, dst + 3072, f3);
        }
    } else {
        const int rr = lane >> 3, slot = lane & 7, chunk = slot ^ (((rr >> 1) & 1) << 2);
        const unsigned vo = (unsigned)(rr * DM + chunk * 8) * 2;
        const int s0 = hw == 3 ? j : (j < 16 ? 4 * j : 64 + 3 * (j - 16)), ns = hw == 3 ? 1 : (j < 16 ? 4 : 3);
        const char* base = (const char*)((hw == 3 ? p.wo : p.w2) + (size_t)16 * s0 * DM + 64 * n);
        for (int i = 0; i < 2 * ns; ++i) glds16s<false>(base + (size_t)i * 8 * DM * 2, vo, scr0 + i * 1024);
        DRAIN();
        const int sw = ((a >> 1) & 1) << 6;
        const LAS char* wb0 = scr + (4 * h + a) * 128 + (0 ^ sw) + ch * 32 + pp * 8;
        const LAS char* wb1 = scr + (4 * h + a) * 128 + (64 ^ sw) + ch * 32 + pp * 8;
        for (int i = 0; i < ns; ++i) {
            const int s = s0 + i;
            const int kh = s >= 56 ? 1 : 0, sl = s - 56 * kh;
            const int dst = hw == 3 ? WOT_OFF + n * 65536 + s * 2048 : w2t_off(n, sl >> 2) + kh * 8192 + (sl & 3) * 2048;
            stw(rs, lane * 16, dst, wfrag_tr(wb0 + i * 2048, 1024));
            stw(rs, lane * 16, dst + 1024, wfrag_tr(wb1 + i * 2048, 1024));
        }
    }
    DRAIN();
}

DI void helper_path(const Params& p, LAS char* lds, unsigned lds0, rsrc_t rs, const int w, const int g_, const int r_) {
    const int hw = w - 4;
    unsigned tag = 0;
    { const int layer = 0; PHASE_VARS(); if (w == 7) { tag = read_tag(p); asm volatile("" :: "s"(tag) : "memory"); }
      pretile(p, lds, lds0, rs, hw, g, r, lane);
      BAR(); }
    for (int layer = 0; layer < 2; ++layer) {
        {
            PHASE_VARS();
            HTS(layer * 20 + 0);
            if (lyr == 1) DRAIN();
            HTS(layer * 20 + 1);
            BAR();
            if (lyr == 0) {
                stage_lin(p, lds0, WQT_OFF + r * 98304, L_WQ, 96, hw, lane);
                WAITV(18); BAR();
                WAITV(12); BAR();
                WAITV(6); BAR();
                DRAIN(); BAR();
                if (w < 7) {
                    LAS float* csq = (LAS float*)(lds + L_CSQ);
                    float acc = 0.f;
#pragma unroll 4
                    for (int s = 0; s < 32; ++s) acc = dot8_ones(lds + L_WQ + (s * 3 + hw) * 1024 + lane * 16, acc);
                    acc = sum_xor32(acc);
                    if (h == 0) csq[hw * 32 + tok] = acc;
                }
            }
            if (w == 7 && lane < 2) *(LAS u32x4*)(lds + L_ONES + lane * 1024) = u32x4{0x3f803f80u, 0x3f803f80u, 0x3f803f80u, 0x3f803f80u};
            BAR();
            BAR();
            if (w == 7 && g > 0) wait_word(p, flagp(p, 1 + 5 * lyr, (g >= 2 && (lane & 1)) ? g - 2 : g - 1, r), tag);
            BAR();
            halo_load(lds, rs, w, g, r, lyr, lane);
            BAR();
        }
        {   PHASE_VARS();
            attn_unit(p, lds, lds0, rs, w, g, r, lyr, lane);
            HTS(layer * 20 + 4);
            DRAIN();
            HTS(layer * 20 + 5);
            BAR();
            stage_lin(p, lds0, W1T_OFF + r * 114688 + 28 * KB, 64 * KB + 28 * KB, 49, hw, lane);
            HTS(layer * 20 + 6);
        }
        {
            PHASE_VARS();
            BAR();
            BAR();
            BAR();
            stage_lin(p, lds0, W1T_OFF + r * 114688 + 77 * KB, 0, 35, hw, lane);
        }
        {
            PHASE_VARS();
            HTS(layer * 20 + 8);
            if (hw == 3) WAITV(8); else WAITV(9);
            HTS(layer * 20 + 9);
            BAR();
            DRAIN();
            HTS(layer * 20 + 10);
            BAR();
            if (lyr == 0) {
                LAS float* cs1 = (LAS float*)(lds + L_CS1);
                float s = 0.f;
                const LAS char* wb = hw < 3 ? lds + hw * 1024 + lane * 16 : lds + 3072 + (h * 16 + (lane & 15)) * 16;
#pragma unroll
                for (int k = 0; k < 32; ++k) s = dot8_ones(wb + w1_off(k), s);
                s = sum_xor32(s);
                if (h == 0 && (hw < 3 || tok < 16)) cs1[hw * 32 + tok] = s;
            }
            BAR();
            BAR();
            HTS(layer * 20 + 11);
            stage_lin(p, lds0, w2t_off(n, 0), L_W2, 144, hw, lane);
            HTS(layer * 20 + 12);
        }
        {
            PHASE_VARS();
            WAITV(16);
            HTS(layer * 20 + 13);
            BAR();
            DRAIN();
            HTS(layer * 20 + 14);
            BAR();
            stage_lin(p, lds0, w2t_off(n, 9), L_W2, 80, hw, lane);
            if (lyr == 1 && blockIdx.x == 0 && w == 7) {
                bool ok = false;
                for (unsigned spins = 0; spins < (1u << 16) && !ok; ++spins) {
                    bool mine = true;
#pragma unroll
                    for (int j = 0; j < 4; ++j) mine &= __hip_atomic_load((gu32*)(p.ws + ACK_OFF) + (lane + 64 * j) * 32, __ATOMIC_RELAXED, __HIP_MEMORY_SCOPE_AGENT) == tag;
                    ok = __all(mine);
                }
                if (lane == 0) __hip_atomic_store((gu32*)(p.ws + EPOCH_OFF), tag, __ATOMIC_RELAXED, __HIP_MEMORY_SCOPE_AGENT);
            }
            HTS(layer * 20 + 15);
            DRAIN();
            HTS(layer * 20 + 16);
            BAR();
            BAR();
            if (lyr == 0) { BAR(); HTS(17); stage_lin(p, lds0, WQT_OFF + r * 98304, L_WQ, 96, hw, lane); HTS(18); }
        }
    }
    asm volatile("s_waitcnt lgkmcnt(0)" ::: "memory");
    if (w == 4) HTS_DUMP();
}

__global__ __launch_bounds__(512, 2) void k_mega(Params p) {
    extern __shared__ __attribute__((aligned(16))) char smem_[];
    LAS char* lds = (LAS char*)smem_;
    const unsigned lds0 = (unsigned)(uintptr_t)smem_;
    const int b = blockIdx.x, idx = b >> 3;
    const int g_ = (b & 7) * 2 + (idx >> 4), r_ = idx & 15;
    const int w = __builtin_amdgcn_readfirstlane(threadIdx.x >> 6);
    const rsrc_t rs = __builtin_amdgcn_make_buffer_rsrc((void*)p.ws, 0, WS_USED, 0x00020000);
    TS(0);
    if (w < 4) main_path(p, lds, rs, w, g_, r_);
    else helper_path(p, lds, lds0, rs, w, g_, r_);
}
}

extern "C" void kernel_launch(void* const* d_in, const int* in_sizes, int n_in, void* d_out, int out_size, void* d_ws, size_t ws_size, hipStream_t stream) {
    Params p;
    p.x = (const uint16_t*)d_in[0]; p.wqkv = (const uint16_t*)d_in[1]; p.wo = (const uint16_t*)d_in[2];
    p.w1 = (const uint16_t*)d_in[4]; p.w2 = (const uint16_t*)d_in[5];
    p.out = (uint16_t*)d_out; p.ws = (char*)d_ws;
    (void)hipFuncSetAttribute((const void*)k_mega, hipFuncAttributeMaxDynamicSharedMemorySize, LDS_BYTES);
    k_mega<<<256, 512, LDS_BYTES, stream>>>(p);
}
```

```cpp
#include <hip/hip_runtime.h>
#include <hip/hip_bf16.h>
#include <stdint.h>

#define GAS __attribute__((address_space(1)))
#define LAS __attribute__((address_space(3)))
#define DI __device__ __forceinline__

typedef __bf16 bf16x8 __attribute__((ext_vector_type(8)));
typedef __bf16 bf16x2 __attribute__((ext_vector_type(2)));
typedef short s16x4 __attribute__((ext_vector_type(4)));
typedef float f32x16 __attribute__((ext_vector_type(16)));
typedef float f32x4 __attribute__((ext_vector_type(4)));
typedef float f32x2 __attribute__((ext_vector_type(2)));
typedef unsigned u32x4 __attribute__((ext_vector_type(4)));
typedef unsigned u32x2 __attribute__((ext_vector_type(2)));
typedef GAS unsigned gu32;

#define TS_DECL()
#define TS_PARAM
#define TS_ARG(tb)
#define TS(i)
#define TS_DUMP()
#define HTS(i)
#define HTS_DUMP()
#define MAPMODE 1
namespace {
constexpr int DM = 512, FF = 1792;
constexpr float EPS = 1e-5f;
constexpr float LOG2E = 1.4426950408889634f;
constexpr float QSCALE = 0.25f * LOG2E;

constexpr int MB = 1 << 20, KB = 1024;
constexpr int XF_OFF = 0;
constexpr int O_OFF = 4 * MB;
constexpr int X2_OFF = 8 * MB;
constexpr int H_OFF = 12 * MB;
constexpr int KV_OFF = 28 * MB;
constexpr int WQT_OFF = 36 * MB;
constexpr int WOT_OFF = WQT_OFF + 1536 * KB;
constexpr int W1T_OFF = 38 * MB;
constexpr int W2T_OFF = 40 * MB;
constexpr int ST_OFF = 42 * MB;
constexpr int ST_BUF = 262144;
constexpr int KN_OFF = 43 * MB;
constexpr int FLAG_OFF = 43 * MB + 65536;
constexpr int FLAG_BYTES = 11 * 256 * 128;
constexpr int TMO_OFF = FLAG_OFF + FLAG_BYTES;
constexpr int EPOCH_OFF = TMO_OFF + 256;
constexpr int ACK_OFF = EPOCH_OFF + 256;
constexpr int WS_USED = 44 * MB;

constexpr int L_WQ = 0;
constexpr int L_Q = 0, L_KV = 8 * KB, L_ONES = 91 * KB;
constexpr int L_RED2 = 40 * KB;
constexpr int wo_off(int s) { return s < 31 ? 96 * KB + s * 2048 : 94 * KB; }
constexpr int L_RED4 = 96 * KB;
constexpr int L_SCR = 100 * KB;
constexpr int L_TAB = 158 * KB, L_CSQ = L_TAB, L_CS1 = L_TAB + 512, L_STT = L_TAB + 1024;
constexpr int L_KN = L_TAB + 384, L_QN = L_TAB + 384 + 96;
constexpr int LDS_BYTES = 160 * KB;
constexpr int w1_off(int s) { return s < 10 ? 56 * KB + s * 3584 : s < 21 ? (s - 10) * 3584 : 94 * KB + (s - 21) * 3584; }
constexpr int w2_slot(int sp) { return (sp < 38 ? sp : sp - 38) * 4096; }
constexpr int AUX_SC1 = 16;

struct Params {
    const uint16_t* x; const uint16_t* wqkv; const uint16_t* wo; const uint16_t* w1; const uint16_t* w2;
    uint16_t* out; char* ws;
};

DI int lane_id() { return (int)__builtin_amdgcn_mbcnt_hi(~0u, __builtin_amdgcn_mbcnt_lo(~0u, 0u)); }
DI f32x16 mfma32(bf16x8 a, bf16x8 b, f32x16 c) { return __builtin_amdgcn_mfma_f32_32x32x16_bf16(a, b, c, 0, 0, 0); }
DI unsigned pk(float a, float b) { bf16x2 v = {(__bf16)a, (__bf16)b}; return __builtin_bit_cast(unsigned, v); }
DI float bflo(unsigned u) { return __uint_as_float(u << 16); }
DI float bfhi(unsigned u) { return __uint_as_float(u & 0xffff0000u); }
DI bf16x8 asfrag(u32x4 v) { return __builtin_bit_cast(bf16x8, v); }
DI s16x4 trd(const LAS char* p) { return __builtin_amdgcn_ds_read_tr16_b64_v4i16((LAS s16x4*)p); }
DI u32x4 wfrag_tr(const LAS char* p, int off2) {
    const u32x2 lo = __builtin_bit_cast(u32x2, trd(p)), hi = __builtin_bit_cast(u32x2, trd(p + off2));
    return u32x4{lo.x, lo.y, hi.x, hi.y};
}
DI bf16x8 ldsfrag(const LAS char* p) { return *(const LAS bf16x8*)p; }
template <int N, class F> DI float colsum_frags(const LAS char* p, F off, float acc) {
    u32x4 f[N];
#pragma unroll
    for (int i = 0; i < N; ++i) f[i] = *(const LAS u32x4*)(p + off(i));
    float a1 = 0.f;
#pragma unroll
    for (int i = 0; i < N; ++i) {
        unsigned w0 = f[i].x, w1 = f[i].y, w2 = f[i].z, w3 = f[i].w;
        asm("v_dot2c_f32_bf16 %0, %2, %6\n\tv_dot2c_f32_bf16 %1, %3, %6\n\tv_dot2c_f32_bf16 %0, %4, %6\n\tv_dot2c_f32_bf16 %1, %5, %6"
            : "+v"(acc), "+v"(a1) : "v"(w0), "v"(w1), "v"(w2), "v"(w3), "v"(0x3f803f80u));
    }
    asm volatile("s_nop 2" : "+v"(acc), "+v"(a1));
    return acc + a1;
}
DI float sum_xor32(float v) { auto r = __builtin_amdgcn_permlane32_swap(__float_as_uint(v), __float_as_uint(v), false, false); return __uint_as_float(r[0]) + __uint_as_float(r[1]); }
DI float max_xor32(float v) { auto r = __builtin_amdgcn_permlane32_swap(__float_as_uint(v), __float_as_uint(v), false, false); return fmaxf(__uint_as_float(r[0]), __uint_as_float(r[1])); }
template <bool SC1> DI void glds16s(const void* sbase, unsigned voff, unsigned lds_addr) {
    unsigned keep;
    if (SC1) asm volatile("s_mov_b32 %0, m0\n\ts_mov_b32 m0, %3\n\ts_nop 0\n\tglobal_load_lds_dwordx4 %1, %2 sc1\n\ts_mov_b32 m0, %0" : "=&s"(keep) : "v"(voff), "s"(sbase), "s"(lds_addr) : "memory");
    else asm volatile("s_mov_b32 %0, m0\n\ts_mov_b32 m0, %3\n\ts_nop 0\n\tglobal_load_lds_dwordx4 %1, %2\n\ts_mov_b32 m0, %0" : "=&s"(keep) : "v"(voff), "s"(sbase), "s"(lds_addr) : "memory");
}
#define BAR() asm volatile("s_waitcnt lgkmcnt(0)\n\ts_barrier" ::: "memory")
#define DRAIN() asm volatile("s_waitcnt vmcnt(0)" ::: "memory")
#define WAITV(n) asm volatile("s_waitcnt vmcnt(" #n ")" ::: "memory")
#define SB() __builtin_amdgcn_sched_barrier(0)
#define SGB(mask, n) __builtin_amdgcn_sched_group_barrier(mask, n, 0)

typedef __amdgpu_buffer_rsrc_t rsrc_t;
DI u32x4 ldw(rsrc_t rs, int voff, int soff) { return __builtin_amdgcn_raw_buffer_load_b128(rs, voff, soff, AUX_SC1); }
DI void stw(rsrc_t rs, int voff, int soff, u32x4 v) { __builtin_amdgcn_raw_buffer_store_b128(v, rs, voff + soff, 0, AUX_SC1); }
DI f32x2 ldst(rsrc_t rs, int voff, int soff) { return __builtin_bit_cast(f32x2, __builtin_amdgcn_raw_buffer_load_b64(rs, voff, soff, AUX_SC1)); }
DI void stst(rsrc_t rs, int voff, int soff, f32x2 v) { __builtin_amdgcn_raw_buffer_store_b64(__builtin_bit_cast(u32x2, v), rs, voff, soff, AUX_SC1); }

DI gu32* flagp(const Params& p, int ph, int g, int r) { return (gu32*)(p.ws + FLAG_OFF) + ((ph * 16 + g) * 16 + r) * 32; }
DI void set_flag(const Params& p, int ph, int g, int r, unsigned tag) { __hip_atomic_store(flagp(p, ph, g, r), tag, __ATOMIC_RELAXED, __HIP_MEMORY_SCOPE_AGENT); }
DI void wait_word(const Params& p, gu32* a, unsigned tag) {
    for (unsigned spins = 0;; ++spins) {
        const unsigned v = __hip_atomic_load(a, __ATOMIC_RELAXED, __HIP_MEMORY_SCOPE_AGENT);
        if (__all(v == tag)) break;
        if (spins > (1u << 20)) { __hip_atomic_store((gu32*)(p.ws + TMO_OFF), 1u, __ATOMIC_RELAXED, __HIP_MEMORY_SCOPE_AGENT); break; }
        __builtin_amdgcn_s_sleep(1);
    }
    __builtin_amdgcn_fence(__ATOMIC_ACQUIRE, "wavefront");
}
DI void wait_group(const Params& p, int ph, int g, unsigned tag, int lane) { wait_word(p, flagp(p, ph, g, lane & 15), tag); }

DI void stats_store(rsrc_t rs, int buf, int T, int u, int lane, const unsigned (&c)[8]) {
    float s = 0.f, ss = 0.f;
#pragma unroll
    for (int i = 0; i < 8; ++i) { float lo = bflo(c[i]), hi = bfhi(c[i]); s += lo + hi; ss += lo * lo + hi * hi; }
    s = sum_xor32(s); ss = sum_xor32(ss);
    if (lane < 32) stst(rs, lane * 8, ST_OFF + buf * ST_BUF + (T * 16 + u) * 256, f32x2{s, ss});
}

DI void stage_lin(const char* wsb, unsigned lds0, int src, int dst, int n, int hw, int lane, int nw = 4) {
    const unsigned vo = lane * 16;
    for (int i = hw; i < n; i += nw) glds16s<true>(wsb + src + i * 1024, vo, lds0 + dst + i * 1024);
}

#define HELPER_VARS() \
    int hw = w - 4; asm volatile("" : "+s"(hw)); \
    const char* wsb = p.ws; asm volatile("" : "+s"(wsb)); \
    unsigned lds0 = lds0_; asm volatile("" : "+s"(lds0)); (void)hw; (void)wsb; (void)lds0;
#define PHASE_VARS() \
    int lane = lane_id(); asm volatile("" : "+v"(lane)); \
    int lyr = layer; asm volatile("" : "+s"(lyr)); \
    int g = g_, r = r_; asm volatile("" : "+s"(g), "+s"(r)); \
    const int h = lane >> 5, tok = lane & 31; const int m = r & 1, n = r >> 1; (void)h; (void)tok; (void)m; (void)n; (void)lyr;

DI void attn_finish(rsrc_t rs, const f32x16& o, int hd, int T, int r, int lyr, int lane) {
    float ov[8]; float l;
    if (hd == 0) { l = o[8];
#pragma unroll
        for (int i = 0; i < 8; ++i) ov[i] = o[i];
    } else { l = o[0];
#pragma unroll
        for (int i = 0; i < 8; ++i) ov[i] = o[8 + i];
    }
    const float inv = __builtin_amdgcn_rcpf(l);
    stw(rs, lane * 16, O_OFF + lyr * 2 * MB + (T * 32 + 2 * r + hd) * 1024,
        u32x4{pk(ov[0] * inv, ov[1] * inv), pk(ov[2] * inv, ov[3] * inv), pk(ov[4] * inv, ov[5] * inv), pk(ov[6] * inv, ov[7] * inv)});
}
DI float max16(float m, const f32x16& s) {
#pragma unroll
    for (int i = 0; i < 16; i += 2) m = fmaxf(fmaxf(m, s[i]), s[i + 1]);
    return m;
}
DI void max16x2(float& m0, float& m1, const f32x16& s) {
#pragma unroll
    for (int i = 0; i < 16; i += 4) { m0 = fmaxf(fmaxf(m0, s[i]), s[i + 1]); m1 = fmaxf(fmaxf(m1, s[i + 2]), s[i + 3]); }
}
DI void exp_pack(const f32x16& s, u32x4& p0, u32x4& p1) {
    unsigned pw[8];
#pragma unroll
    for (int i = 0; i < 8; ++i) pw[i] = pk(__builtin_amdgcn_exp2f(s[2 * i]), __builtin_amdgcn_exp2f(s[2 * i + 1]));
    p0 = u32x4{pw[0], pw[1], pw[2], pw[3]}; p1 = u32x4{pw[4], pw[5], pw[6], pw[7]};
}
DI float wave32_max(float v) {
    int x = __float_as_int(v);
    x = max(x, __builtin_amdgcn_update_dpp(x, x, 0x111, 0xf, 0xf, false));
    x = max(x, __builtin_amdgcn_update_dpp(x, x, 0x112, 0xf, 0xf, false));
    x = max(x, __builtin_amdgcn_update_dpp(x, x, 0x114, 0xf, 0xf, false));
    x = max(x, __builtin_amdgcn_update_dpp(x, x, 0x118, 0xf, 0xf, false));
    x = max(x, __builtin_amdgcn_update_dpp(x, x, 0x142, 0xa, 0xf, false));
    return __int_as_float(__builtin_amdgcn_readlane(x, 31));
}
DI void attn_seg(unsigned ka, unsigned va, int ks, int vs, int n, const bf16x8 qf, const f32x16 c0, f32x16& o) {
    int d;
    asm volatile(
        "ds_read_b128 v[248:251], %[ka]\n\tv_add_u32 %[ka], %[ks], %[ka]\n\tds_read_b128 v[236:239], %[ka]\n\tv_add_u32 %[ka], %[ks], %[ka]\n\t"
        "ds_read_b128 v[240:243], %[va]\n\tds_read_b128 v[244:247], %[va] offset:1024\n\tv_add_u32 %[va], %[vs], %[va]\n\t"
        "s_lshr_b32 %[d], %[n], 1\n\ts_waitcnt lgkmcnt(3)\n\t"
        "v_mfma_f32_32x32x16_bf16 v[204:219], v[248:251], %[q], %[c0]\n\t"
        "s_cmp_eq_u32 %[d], 0\n\ts_nop 10\n\ts_cbranch_scc1 Ltail_%=\n"
        "Lloop_%=:\n\t"
        "v_exp_f32 v204, v204\n\t"
        "v_exp_f32 v205, v205\n\t"
        "v_exp_f32 v206, v206\n\t"
        "v_exp_f32 v207, v207\n\t"
        "v_exp_f32 v208, v208\n\t"
        "v_exp_f32 v209, v209\n\t"
        "v_exp_f32 v210, v210\n\t"
        "v_exp_f32 v211, v211\n\t"
        "v_cvt_pk_bf16_f32 v204, v204, v205\n\t"
        "v_cvt_pk_bf16_f32 v205, v206, v207\n\t"
        "v_cvt_pk_bf16_f32 v206, v208, v209\n\t"
        "v_cvt_pk_bf16_f32 v207, v210, v211\n\t"
        "s_waitcnt lgkmcnt(1)\n\ts_nop 0\n\t"
        "v_mfma_f32_32x32x16_bf16 %[o], v[240:243], v[204:207], %[o]\n\t"
        "v_exp_f32 v212, v212\n\t"
        "v_exp_f32 v213, v213\n\t"
        "v_exp_f32 v214, v214\n\t"
        "v_exp_f32 v215, v215\n\t"
        "v_mfma_f32_32x32x16_bf16 v[220:235], v[236:239], %[q], 0\n\t"
        "ds_read_b128 v[236:239], %[ka]\n\tds_read_b128 v[248:251], %[va]\n\tds_read_b128 v[252:255], %[va] offset:1024\n\t"
        "v_add_u32 %[ka], %[ks], %[ka]\n\tv_add_u32 %[va], %[vs], %[va]\n\t"
        "v_exp_f32 v216, v216\n\t"
        "v_exp_f32 v217, v217\n\t"
        "v_exp_f32 v218, v218\n\t"
        "v_exp_f32 v219, v219\n\t"
        "v_cvt_pk_bf16_f32 v212, v212, v213\n\t"
        "v_cvt_pk_bf16_f32 v213, v214, v215\n\t"
        "v_cvt_pk_bf16_f32 v214, v216, v217\n\t"
        "v_cvt_pk_bf16_f32 v215, v218, v219\n\t"
        "s_waitcnt lgkmcnt(3)\n\ts_nop 0\n\t"
        "v_mfma_f32_32x32x16_bf16 %[o], v[244:247], v[212:215], %[o]\n\t"
        "v_exp_f32 v220, v220\n\t"
        "v_exp_f32 v221, v221\n\t"
        "v_exp_f32 v222, v222\n\t"
        "v_exp_f32 v223, v223\n\t"
        "v_exp_f32 v224, v224\n\t"
        "v_exp_f32 v225, v225\n\t"
        "v_exp_f32 v226, v226\n\t"
        "v_exp_f32 v227, v227\n\t"
        "v_cvt_pk_bf16_f32 v220, v220, v221\n\t"
        "v_cvt_pk_bf16_f32 v221, v222, v223\n\t"
        "v_cvt_pk_bf16_f32 v222, v224, v225\n\t"
        "v_cvt_pk_bf16_f32 v223, v226, v227\n\t"
        "s_waitcnt lgkmcnt(1)\n\ts_nop 0\n\t"
        "v_mfma_f32_32x32x16_bf16 %[o], v[248:251], v[220:223], %[o]\n\t"
        "v_exp_f32 v228, v228\n\t"
        "v_exp_f32 v229, v229\n\t"
        "v_exp_f32 v230, v230\n\t"
        "v_exp_f32 v231, v231\n\t"
        "v_mfma_f32_32x32x16_bf16 v[204:219], v[236:239], %[q], 0\n\t"
        "ds_read_b128 v[236:239], %[ka]\n\tds_read_b128 v[240:243], %[va]\n\tds_read_b128 v[244:247], %[va] offset:1024\n\t"
        "v_add_u32 %[ka], %[ks], %[ka]\n\tv_add_u32 %[va], %[vs], %[va]\n\t"
        "v_exp_f32 v232, v232\n\t"
        "v_exp_f32 v233, v233\n\t"
        "v_exp_f32 v234, v234\n\t"
        "v_exp_f32 v235, v235\n\t"
        "v_cvt_pk_bf16_f32 v228, v228, v229\n\t"
        "v_cvt_pk_bf16_f32 v229, v230, v231\n\t"
        "v_cvt_pk_bf16_f32 v230, v232, v233\n\t"
        "v_cvt_pk_bf16_f32 v231, v234, v235\n\t"
        "s_waitcnt lgkmcnt(3)\n\ts_nop 0\n\t"
        "v_mfma_f32_32x32x16_bf16 %[o], v[252:255], v[228:231], %[o]\n\t"
        "s_sub_u32 %[d], %[d], 1\n\ts_cmp_lg_u32 %[d], 0\n\ts_cbranch_scc1 Lloop_%=\n"
        "Ltail_%=:\n\t"
        "s_bitcmp1_b32 %[n], 0\n\ts_waitcnt lgkmcnt(0)\n\ts_cbranch_scc0 Lend_%=\n\t"
        "v_exp_f32 v204, v204\n\t"
        "v_exp_f32 v205, v205\n\t"
        "v_exp_f32 v206, v206\n\t"
        "v_exp_f32 v207, v207\n\t"
        "v_exp_f32 v208, v208\n\t"
        "v_exp_f32 v209, v209\n\t"
        "v_exp_f32 v210, v210\n\t"
        "v_exp_f32 v211, v211\n\t"
        "v_cvt_pk_bf16_f32 v204, v204, v205\n\t"
        "v_cvt_pk_bf16_f32 v205, v206, v207\n\t"
        "v_cvt_pk_bf16_f32 v206, v208, v209\n\t"
        "v_cvt_pk_bf16_f32 v207, v210, v211\n\t"
        "s_nop 1\n\t"
        "v_mfma_f32_32x32x16_bf16 %[o], v[240:243], v[204:207], %[o]\n\t"
        "v_exp_f32 v212, v212\n\t"
        "v_exp_f32 v213, v213\n\t"
        "v_exp_f32 v214, v214\n\t"
        "v_exp_f32 v215, v215\n\t"
        "v_exp_f32 v216, v216\n\t"
        "v_exp_f32 v217, v217\n\t"
        "v_exp_f32 v218, v218\n\t"
        "v_exp_f32 v219, v219\n\t"
        "v_cvt_pk_bf16_f32 v212, v212, v213\n\t"
        "v_cvt_pk_bf16_f32 v213, v214, v215\n\t"
        "v_cvt_pk_bf16_f32 v214, v216, v217\n\t"
        "v_cvt_pk_bf16_f32 v215, v218, v219\n\t"
        "s_nop 1\n\t"
        "v_mfma_f32_32x32x16_bf16 %[o], v[244:247], v[212:215], %[o]\n"
        "Lend_%=:\n\ts_nop 11"
        : [o] "+v"(o), [ka] "+v"(ka), [va] "+v"(va), [d] "=&s"(d)
        : [q] "v"(qf), [c0] "v"(c0), [ks] "s"(ks), [vs] "v"(vs), [n] "s"(n)
        : "memory", "scc", "v204", "v205", "v206", "v207", "v208", "v209", "v210", "v211", "v212", "v213", "v214", "v215", "v216", "v217", "v218", "v219", "v220", "v221", "v222", "v223", "v224", "v225", "v226", "v227", "v228", "v229", "v230", "v231", "v232", "v233", "v234", "v235", "v236", "v237", "v238", "v239", "v240", "v241", "v242", "v243", "v244", "v245", "v246", "v247", "v248", "v249", "v250", "v251", "v252", "v253", "v254", "v255");
}
constexpr float BOUND2 = 4096.f;
DI f32x16 attn_exact(LAS char* lds, int tw, int hd, int s0, bool first_masked, const bf16x8 qf, int lane) {
    const int h = lane >> 5, tok = lane & 31;
    const LAS char* kb = lds + L_KV + hd * 1024 + lane * 16;
    const bool isdata = ((lane >> 4) & 1) == hd;
    const LAS char* vb = isdata ? (lds + L_KV + 2048 + lane * 16) : (lds + L_ONES);
    const int vstep = isdata ? 4096 : 0;
    const f32x16 zero = {};
    f32x16 o = {};
    float mx = -INFINITY;
    for (int pass = 0; pass < 2; ++pass) {
        for (int sl = s0; sl <= 8 + tw; ++sl) {
            f32x16 s = mfma32(*(const LAS bf16x8*)(kb + sl * 4096), qf, zero);
            const bool diag = sl == 8 + tw, first = first_masked && sl == s0;
#pragma unroll
            for (int i = 0; i < 16; ++i) { const int kk = (i & 3) + 8 * (i >> 2) + 4 * h; if ((diag && kk > tok) || (first && kk <= tok)) s[i] = -INFINITY; }
            if (pass == 0) mx = max16(mx, s);
            else {
#pragma unroll
                for (int i = 0; i < 16; ++i) s[i] -= mx;
                u32x4 p0, p1; exp_pack(s, p0, p1);
                o = mfma32(*(const LAS bf16x8*)(vb + sl * vstep), asfrag(p0), o);
                o = mfma32(*(const LAS bf16x8*)(vb + sl * vstep + 1024), asfrag(p1), o);
            }
        }
        if (pass == 0) mx = max_xor32(mx);
    }
    return o;
}
DI void attn_phase(const Params& p, LAS char* lds, rsrc_t rs, int w, int g, int r, int lyr, int lane, unsigned tag TS_PARAM) {
    const int h = lane >> 5, tok = lane & 31;
    const int hd = w >> 2, tw = hd ? 3 - (w & 3) : (w & 3), T = 4 * g + tw;
    const bf16x8 qf = *(const LAS bf16x8*)(lds + L_Q + tw * 2048 + hd * 1024 + lane * 16);
    const unsigned kb = (unsigned)(uintptr_t)(lds + L_KV + hd * 1024 + lane * 16);
    const bool isdata = ((lane >> 4) & 1) == hd;
    const unsigned vb = (unsigned)(uintptr_t)(isdata ? (lds + L_KV + 2048 + lane * 16) : (lds + L_ONES));
    const int vstep = isdata ? 4096 : 0;
    const LAS float* kn = (const LAS float*)(lds + L_KN);
    const float qn = ((const LAS float*)(lds + L_QN))[tw * 2 + hd] * 1.05f;
    const float kown = fmaxf(fmaxf(kn[16 + hd], kn[18 + hd]), fmaxf(kn[20 + hd], kn[22 + hd]));
    bool fast = __builtin_amdgcn_readfirstlane(qn * kown <= BOUND2);
    f32x16 t, o = {};
#pragma unroll
    for (int i = 0; i < 16; ++i) t[i] = __builtin_fmaf((float)(tok - 4 * h), 1e30f, (0.5f - (float)((i & 3) + 8 * (i >> 2))) * 1e30f);
    const int s4 = 4 * hd, tile0 = 4 * g - 8 + s4;
    const bool loader = tw == 0 && tile0 >= 0;
    u32x4 hv[16]; f32x2 hn;
    if (loader) {
        wait_word(p, flagp(p, 1 + 5 * lyr, g - 2 + hd, r), tag);
        TS(tb + 1);
        const int kvo = KV_OFF + lyr * 4 * MB + (tile0 * 16 + r) * 4096;
#pragma unroll
        for (int i = 0; i < 16; ++i) hv[i] = ldw(rs, lane * 16 + (i & 3) * 1024, kvo + (i >> 2) * 65536);
        hn = ldst(rs, (lane & 3) * 128, KN_OFF + ((lyr * 64 + tile0) * 16 + r) * 8);
        SB();
    }
    if (fast) {
        f32x16 m;
#pragma unroll
        for (int i = 0; i < 16; ++i) m[i] = fminf(t[i], 0.f);
        attn_seg(kb + (8 + tw) * 4096, vb + (8 + tw) * vstep, -4096, -vstep, tw + 1, qf, m, o);
    }
    TS(tb);
    if (loader) {
        LAS char* kvl = lds + L_KV + s4 * 4096 + lane * 16;
#pragma unroll
        for (int i = 0; i < 16; ++i) *(LAS u32x4*)(kvl + i * 1024) = hv[i];
        if (lane < 4) *(LAS f32x2*)(lds + L_KN + (s4 + lane) * 8) = hn;
    }
    BAR();
    TS(tb + 2);
    const int s0 = T >= 8 ? tw : 8 - 4 * g;
    if (s0 < 8) {
        float kh = 0.f;
#pragma unroll
        for (int j = 0; j < 8; ++j) kh = fmaxf(kh, j >= 8 - 4 * g ? kn[2 * j + hd] : 0.f);
        fast = fast && __builtin_amdgcn_readfirstlane(qn * kh <= BOUND2);
        if (fast) {
            f32x16 m;
#pragma unroll
            for (int i = 0; i < 16; ++i) m[i] = T >= 8 ? fminf(-t[i], 0.f) : 0.f;
            attn_seg(kb + s0 * 4096, vb + s0 * vstep, 4096, vstep, 8 - s0, qf, m, o);
        }
    }
    if (!fast) o = attn_exact(lds, tw, hd, s0, T >= 8, qf, lane);
    attn_finish(rs, o, hd, T, r, lyr, lane);
}
DI void ln_stats(const f32x2 (&sv)[8], float& mean, float& rstd) {
    float S = 0.f, SS = 0.f;
#pragma unroll
    for (int i = 0; i < 8; ++i) { S += sv[i].x; SS += sv[i].y; }
    S = sum_xor32(S); SS = sum_xor32(SS);
    mean = S * (1.f / DM); const float var = fmaxf(SS * (1.f / DM) - mean * mean, 0.f); rstd = __builtin_amdgcn_rsqf(var + EPS);
}
DI unsigned read_tag(const Params& p) {
    return (unsigned)__builtin_amdgcn_readfirstlane(__hip_atomic_load((gu32*)(p.ws + EPOCH_OFF), __ATOMIC_RELAXED, __HIP_MEMORY_SCOPE_AGENT)) + 1u;
}
#define LDA(arr, s, base) arr[s] = ldw(rs, lane * 16 + ((s) & 3) * 1024, (base) + ((s) >> 2) * 4096)

#define WODELAY 36
#define PD0 4
#define PDV 12
constexpr int a_issued(int s, int total) { int v = PD0 + 2 * (s + 1); const int cap = s + 1 + PDV; v = v < cap ? v : cap; return v < total ? v : total; }

DI void main_path(const Params& p, LAS char* lds, rsrc_t rs, const int w, const int g_, const int r_) {
    unsigned tag = 0;
    TS_DECL();
    TS(0);
    {
        const int layer = 0; PHASE_VARS();
        const int T = 4 * g + (r & 3), u = (r >> 2) * 4 + w;
        const uint16_t* row = p.x + (size_t)(T * 32 + tok) * DM + 32 * u + 4 * h;
        u32x2 a0 = *(const GAS u32x2*)(row), a1 = *(const GAS u32x2*)(row + 8), a2 = *(const GAS u32x2*)(row + 16), a3 = *(const GAS u32x2*)(row + 24);
        tag = read_tag(p); asm volatile("" :: "s"(tag) : "memory");
        stw(rs, lane * 16, XF_OFF + (T * 32 + 2 * u) * 1024, u32x4{a0.x, a0.y, a1.x, a1.y});
        stw(rs, lane * 16, XF_OFF + (T * 32 + 2 * u + 1) * 1024, u32x4{a2.x, a2.y, a3.x, a3.y});
        const unsigned c[8] = {a0.x, a0.y, a1.x, a1.y, a2.x, a2.y, a3.x, a3.y};
        stats_store(rs, 0, T, u, lane, c);
        DRAIN();
        BAR();
        if (w == 0 && lane == 0) { set_flag(p, 0, g, r, tag); __hip_atomic_store((gu32*)(p.ws + ACK_OFF) + blockIdx.x * 32, tag, __ATOMIC_RELAXED, __HIP_MEMORY_SCOPE_AGENT); }
    }
    TS(1);
    for (int layer = 0; layer < 2; ++layer) {
        const int tb = 2 + layer * 24; (void)tb;
        {
            PHASE_VARS();
            if (w == 0) {
                if (lyr == 0) {
                    const int j = lane & 15;
                    wait_word(p, (lane & 16) ? flagp(p, 0, j, r) : flagp(p, 0, g, j), tag);
                } else wait_group(p, 5, g, tag, lane);
            }
            BAR();
            TS(tb + 0);
            const int T = 4 * g + w;
            const LAS char* wb = lds + L_WQ + lane * 16;
            LAS float* csq = (LAS float*)(lds + L_CSQ);
            LAS float* stt = (LAS float*)(lds + L_STT);
            f32x16 aq = {}, ak = {}, av = {};
            u32x4 xa[32];
            f32x2 sv[8];
            const int sto = ST_OFF + 2 * lyr * ST_BUF + T * 4096, xo = XF_OFF + lyr * 2 * MB + T * 32768;
#pragma unroll
            for (int s = 0; s < PD0; ++s) { LDA(xa, s, xo); SB(); }
            if (lyr == 0) BAR();
            bf16x8 wf[3][3];
#pragma unroll
            for (int s = 0; s < 2; ++s)
#pragma unroll
                for (int t = 0; t < 3; ++t) wf[s][t] = ldsfrag(wb + (s * 3 + t) * 1024);
#pragma unroll
            for (int s = 0; s < 32; ++s) {
                if ((s == 6 || s == 14 || s == 22) && lyr == 0) BAR();
#pragma unroll
                for (int j = (s == 0 ? PD0 : a_issued(s - 1, 32)); j < a_issued(s, 32); ++j) LDA(xa, j, xo);
                if (s >= 16 && s < 24) sv[s - 16] = ldst(rs, (8 * h * 32 + tok) * 8 + (s - 16) * 256, sto);
                if (s + 2 < 32) {
#pragma unroll
                    for (int t = 0; t < 3; ++t) wf[(s + 2) % 3][t] = ldsfrag(wb + ((s + 2) * 3 + t) * 1024);
                }
                const bf16x8 x = asfrag(xa[s]);
                aq = mfma32(wf[s % 3][0], x, aq); ak = mfma32(wf[s % 3][1], x, ak); av = mfma32(x, wf[s % 3][2], av);
                SGB(0x100, 1); SGB(0x8, 1); SGB(0x20, 1); SGB(0x100, 1); SGB(0x8, 1); SGB(0x20, 1); SGB(0x100, 1); SGB(0x8, 1);
                SB();
            }
            float mean, rstd; ln_stats(sv, mean, rstd);
            if (h == 0) { stt[w * 64 + tok] = rstd; stt[w * 64 + 32 + tok] = mean * rstd; }
            TS(tb + 1);
            BAR();
            TS(tb + 2);
            const float mr = mean * rstd;
            float q[16], k[16], v[16];
            const float cv = csq[64 + tok];
#pragma unroll
            for (int g4 = 0; g4 < 4; ++g4) {
                f32x4 cq = *(LAS f32x4*)(csq + 8 * g4 + 4 * h), ck = *(LAS f32x4*)(csq + 32 + 8 * g4 + 4 * h);
                f32x4 rsv = *(LAS f32x4*)(stt + w * 64 + 8 * g4 + 4 * h), ms = *(LAS f32x4*)(stt + w * 64 + 32 + 8 * g4 + 4 * h);
#pragma unroll
                for (int i = 0; i < 4; ++i) {
                    k[4 * g4 + i] = ak[4 * g4 + i] * rstd - mr * ck[i];
                    v[4 * g4 + i] = av[4 * g4 + i] * rsv[i] - ms[i] * cv;
                    q[4 * g4 + i] = (aq[4 * g4 + i] * rstd - mr * cq[i]) * QSCALE;
                }
            }
            const u32x4 k0 = {pk(k[0], k[1]), pk(k[2], k[3]), pk(k[4], k[5]), pk(k[6], k[7])};
            const u32x4 k1 = {pk(k[8], k[9]), pk(k[10], k[11]), pk(k[12], k[13]), pk(k[14], k[15])};
            const u32x4 v0 = {pk(v[0], v[1]), pk(v[2], v[3]), pk(v[4], v[5]), pk(v[6], v[7])};
            const u32x4 v1 = {pk(v[8], v[9]), pk(v[10], v[11]), pk(v[12], v[13]), pk(v[14], v[15])};
            const int kvo = KV_OFF + lyr * 4 * MB + (T * 16 + r) * 4096;
            stw(rs, lane * 16, kvo, k0); stw(rs, lane * 16 + 1024, kvo, k1); stw(rs, lane * 16 + 2048, kvo, v0); stw(rs, lane * 16 + 3072, kvo, v1);
            {
                float na = 0.f, nb = 0.f;
#pragma unroll
                for (int i = 0; i < 8; ++i) { na = __builtin_fmaf(k[i], k[i], na); nb = __builtin_fmaf(k[8 + i], k[8 + i], nb); }
                const f32x2 nn = {wave32_max(sum_xor32(na)), wave32_max(sum_xor32(nb))};
                if (lane == 0) { stst(rs, 0, KN_OFF + ((lyr * 64 + T) * 16 + r) * 8, nn); *(LAS f32x2*)(lds + L_KN + (8 + w) * 8) = nn; }
            }
            LAS char* kvl = lds + L_KV + (8 + w) * 4096 + lane * 16;
            *(LAS u32x4*)(kvl) = k0; *(LAS u32x4*)(kvl + 1024) = k1; *(LAS u32x4*)(kvl + 2048) = v0; *(LAS u32x4*)(kvl + 3072) = v1;
            LAS char* ql = lds + L_Q + w * 2048 + lane * 16;
            *(LAS u32x4*)(ql) = u32x4{pk(q[0], q[1]), pk(q[2], q[3]), pk(q[4], q[5]), pk(q[6], q[7])};
            *(LAS u32x4*)(ql + 1024) = u32x4{pk(q[8], q[9]), pk(q[10], q[11]), pk(q[12], q[13]), pk(q[14], q[15])};
            {
                float na = 0.f, nb = 0.f;
#pragma unroll
                for (int i = 0; i < 8; ++i) { na = __builtin_fmaf(q[i], q[i], na); nb = __builtin_fmaf(q[8 + i], q[8 + i], nb); }
                const f32x2 nn = {wave32_max(sum_xor32(na)), wave32_max(sum_xor32(nb))};
                if (lane == 0) *(LAS f32x2*)(lds + L_QN + w * 8) = nn;
            }
            DRAIN();
            BAR();
            if (w == 0 && lane == 0) set_flag(p, 1 + 5 * lyr, g, r, tag);
            TS(tb + 3);
        }
        {
            PHASE_VARS();
            attn_phase(p, lds, rs, w, g, r, lyr, lane, tag TS_ARG(layer == 0 ? 22 : 48));
            TS(tb + 4); TS(tb + 5); TS(tb + 6);
            DRAIN();
            BAR();
            if (w == 0 && lane == 0) set_flag(p, 2 + 5 * lyr, g, r, tag);
            TS(tb + 7);
        }
        {
            PHASE_VARS();
            const int mt = w & 1, kh = (w >> 1) & 1, T = 4 * g + 2 * m + mt, u = 2 * n + kh;
            const int xo = XF_OFF + lyr * 2 * MB + (T * 32 + 2 * u) * 1024;
            const u32x4 r0 = ldw(rs, lane * 16, xo), r1 = ldw(rs, lane * 16 + 1024, xo);
            if (w == 0) wait_group(p, 2 + 5 * lyr, g, tag, lane);
            BAR();
            TS(tb + 8);
            f32x16 acc[2] = {};
            u32x4 xa[16];
            const int oo = O_OFF + lyr * 2 * MB + (T * 32 + 16 * kh) * 1024;
#pragma unroll
            for (int s = 0; s < PD0; ++s) { LDA(xa, s, oo); SB(); }
            const LAS char* wb = lds + 96 * KB + kh * 32768 + lane * 16;
            const LAS char* wlast = lds + (kh ? 94 * KB : 96 * KB + 15 * 2048) + lane * 16;
            bf16x8 wf[4][2];
#pragma unroll
            for (int s = 0; s < 3; ++s) { wf[s][0] = ldsfrag(wb + s * 2048); wf[s][1] = ldsfrag(wb + s * 2048 + 1024); }
#pragma unroll
            for (int s = 0; s < 16; ++s) {
#pragma unroll
                for (int j = (s == 0 ? PD0 : a_issued(s - 1, 16)); j < a_issued(s, 16); ++j) LDA(xa, j, oo);
                if (s + 3 < 15) { wf[(s + 3) & 3][0] = ldsfrag(wb + (s + 3) * 2048); wf[(s + 3) & 3][1] = ldsfrag(wb + (s + 3) * 2048 + 1024); }
                if (s + 3 == 15) { wf[3][0] = ldsfrag(wlast); wf[3][1] = ldsfrag(wlast + 1024); }
                const bf16x8 x = asfrag(xa[s]);
                acc[0] = mfma32(wf[s & 3][0], x, acc[0]); acc[1] = mfma32(wf[s & 3][1], x, acc[1]);
                SGB(0x100, 1); SGB(0x8, 1); SGB(0x20, 2); SGB(0x100, 1); SGB(0x8, 1);
                SB();
            }
            const f32x16 snd = kh ? acc[0] : acc[1];
#pragma unroll
            for (int g4 = 0; g4 < 4; ++g4) *(LAS f32x4*)(lds + L_RED2 + ((w * 4 + g4) * 64 + lane) * 16) = f32x4{snd[4 * g4], snd[4 * g4 + 1], snd[4 * g4 + 2], snd[4 * g4 + 3]};
            TS(tb + 9);
            BAR();
            const f32x16 own = kh ? acc[1] : acc[0];
            const int pw = w ^ 2;
            float v[16];
#pragma unroll
            for (int g4 = 0; g4 < 4; ++g4) { f32x4 t = *(LAS f32x4*)(lds + L_RED2 + ((pw * 4 + g4) * 64 + lane) * 16);
#pragma unroll
                for (int i = 0; i < 4; ++i) v[4 * g4 + i] = own[4 * g4 + i] + t[i]; }
            const unsigned rr[8] = {r0.x, r0.y, r0.z, r0.w, r1.x, r1.y, r1.z, r1.w};
            unsigned c[8];
#pragma unroll
            for (int i = 0; i < 8; ++i) c[i] = pk(v[2 * i] + bflo(rr[i]), v[2 * i + 1] + bfhi(rr[i]));
            const int yo = X2_OFF + lyr * 2 * MB + (T * 32 + 2 * u) * 1024;
            stw(rs, lane * 16, yo, u32x4{c[0], c[1], c[2], c[3]});
            stw(rs, lane * 16 + 1024, yo, u32x4{c[4], c[5], c[6], c[7]});
            stats_store(rs, 2 * lyr + 1, T, u, lane, c);
            DRAIN();
            BAR();
            if (w == 0 && lane == 0) set_flag(p, 3 + 5 * lyr, g, r, tag);
            TS(tb + 10);
        }
        {
            PHASE_VARS();
            if (w == 0) wait_group(p, 3 + 5 * lyr, g, tag, lane);
            BAR();
            TS(tb + 11);
            const int T = 4 * g + w;
            const LAS char* wbA = lds + lane * 16;
            const LAS char* wbB = lds + 3072 + (h * 16 + (lane & 15)) * 16;
            LAS float* cs1 = (LAS float*)(lds + L_CS1);
            f32x16 acc[4] = {};
            u32x4 xa[32];
            f32x2 sv[8];
            const int sto = ST_OFF + (2 * lyr + 1) * ST_BUF + T * 4096, xo = X2_OFF + lyr * 2 * MB + T * 32768;
#pragma unroll
            for (int s = 0; s < PD0; ++s) { LDA(xa, s, xo); SB(); }
            auto ldW = [&](int s, int t) { return t < 3 ? ldsfrag(wbA + w1_off(s) + t * 1024) : ldsfrag(wbB + w1_off(s)); };
            bf16x8 wf[2][4];
#pragma unroll
            for (int t = 0; t < 4; ++t) wf[0][t] = ldW(0, t);
#pragma unroll
            for (int s = 0; s < 32; ++s) {
                if (s == 20) BAR();
#pragma unroll
                for (int j = (s == 0 ? PD0 : a_issued(s - 1, 32)); j < a_issued(s, 32); ++j) LDA(xa, j, xo);
                if (s >= 16 && s < 24) sv[s - 16] = ldst(rs, (8 * h * 32 + tok) * 8 + (s - 16) * 256, sto);
                const bf16x8 x = asfrag(xa[s]);
#pragma unroll
                for (int t = 0; t < 4; ++t) {
                    if (s + 1 < 32) wf[(s + 1) & 1][t] = ldW(s + 1, t);
                    acc[t] = mfma32(wf[s & 1][t], x, acc[t]);
                }
                SGB(0x100, 1); SGB(0x8, 1); SGB(0x20, 1); SGB(0x100, 1); SGB(0x8, 1); SGB(0x20, 1); SGB(0x100, 1); SGB(0x8, 1); SGB(0x100, 1); SGB(0x8, 1);
                SB();
            }
            float mean, rstd; ln_stats(sv, mean, rstd);
            const float mr = mean * rstd;
            TS(tb + 12);
            BAR();
            TS(tb + 13);
            const int ho = H_OFF + lyr * 8 * MB + (T * 112 + 7 * r) * 1024;
#pragma unroll
            for (int t = 0; t < 4; ++t) {
#pragma unroll
                for (int sp = 0; sp < (t < 3 ? 2 : 1); ++sp) {
                    float hv[8];
#pragma unroll
                    for (int g2 = 0; g2 < 2; ++g2) {
                        const f32x4 c4 = *(LAS f32x4*)(cs1 + t * 32 + 16 * sp + 8 * g2 + 4 * h);
#pragma unroll
                        for (int i = 0; i < 4; ++i) {
                            const float a = acc[t][8 * sp + 4 * g2 + i];
                            const float z = a * rstd - mr * c4[i];
                            hv[4 * g2 + i] = z * __builtin_amdgcn_rcpf(1.f + __builtin_amdgcn_exp2f(-LOG2E * z));
                        }
                    }
                    stw(rs, lane * 16 + (2 * t + sp) * 1024, ho, u32x4{pk(hv[0], hv[1]), pk(hv[2], hv[3]), pk(hv[4], hv[5]), pk(hv[6], hv[7])});
                }
            }
            DRAIN();
            BAR();
            if (w == 0 && lane == 0) set_flag(p, 4 + 5 * lyr, g, r, tag);
            TS(tb + 14);
        }
        {
            PHASE_VARS();
            const int mt = w & 1, kh = (w >> 1) & 1, T = 4 * g + 2 * m + mt, u = 2 * n + kh;
            const int yo = X2_OFF + lyr * 2 * MB + (T * 32 + 2 * u) * 1024;
            const u32x4 r0 = ldw(rs, lane * 16, yo), r1 = ldw(rs, lane * 16 + 1024, yo);
            if (w == 0) wait_group(p, 4 + 5 * lyr, g, tag, lane);
            BAR();
            TS(tb + 15);
            const LAS char* wb = lds + kh * 2048 + lane * 16;
            f32x16 acc[2] = {};
            const int ho = H_OFF + lyr * 8 * MB + (T * 112 + 56 * kh) * 1024;
            u32x4 xa[56];
#pragma unroll
            for (int s = 0; s < PD0; ++s) { LDA(xa, s, ho); SB(); }
            auto woff = [](int s) { return w2_slot(s); };
            bf16x8 wf[4][2];
#pragma unroll
            for (int s = 0; s < 3; ++s) { wf[s][0] = ldsfrag(wb + woff(s)); wf[s][1] = ldsfrag(wb + woff(s) + 1024); }
#pragma unroll
            for (int s = 0; s < 56; ++s) {
                if (s == 18) { BAR(); TS(tb + 16); }
                if (s == 35) { BAR(); TS(tb + 17); }
#pragma unroll
                for (int j = (s == 0 ? PD0 : a_issued(s - 1, 56)); j < a_issued(s, 56); ++j) LDA(xa, j, ho);
                if (s + 3 < 56) { wf[(s + 3) & 3][0] = ldsfrag(wb + woff(s + 3)); wf[(s + 3) & 3][1] = ldsfrag(wb + woff(s + 3) + 1024); }
                const bf16x8 x = asfrag(xa[s]);
                acc[0] = mfma32(wf[s & 3][0], x, acc[0]); acc[1] = mfma32(wf[s & 3][1], x, acc[1]);
                SGB(0x100, 1); SGB(0x8, 1); SGB(0x20, 2); SGB(0x100, 1); SGB(0x8, 1);
                SB();
            }
            const f32x16 snd = kh ? acc[0] : acc[1];
#pragma unroll
            for (int g4 = 0; g4 < 4; ++g4) *(LAS f32x4*)(lds + L_RED4 + ((w * 4 + g4) * 64 + lane) * 16) = f32x4{snd[4 * g4], snd[4 * g4 + 1], snd[4 * g4 + 2], snd[4 * g4 + 3]};
            TS(tb + 18);
            BAR();
            const f32x16 own = kh ? acc[1] : acc[0];
            const int pw = w ^ 2;
            float v[16];
#pragma unroll
            for (int g4 = 0; g4 < 4; ++g4) { f32x4 t = *(LAS f32x4*)(lds + L_RED4 + ((pw * 4 + g4) * 64 + lane) * 16);
#pragma unroll
                for (int i = 0; i < 4; ++i) v[4 * g4 + i] = own[4 * g4 + i] + t[i]; }
            const unsigned rr[8] = {r0.x, r0.y, r0.z, r0.w, r1.x, r1.y, r1.z, r1.w};
            unsigned c[8];
#pragma unroll
            for (int i = 0; i < 8; ++i) c[i] = pk(v[2 * i] + bflo(rr[i]), v[2 * i + 1] + bfhi(rr[i]));
            if (lyr == 0) {
                stw(rs, lane * 16, XF_OFF + 2 * MB + (T * 32 + 2 * u) * 1024, u32x4{c[0], c[1], c[2], c[3]});
                stw(rs, lane * 16 + 1024, XF_OFF + 2 * MB + (T * 32 + 2 * u) * 1024, u32x4{c[4], c[5], c[6], c[7]});
                stats_store(rs, 2, T, u, lane, c);
                DRAIN();
                BAR();
                if (w == 0 && lane == 0) set_flag(p, 5, g, r, tag);
            } else {
                auto s0 = __builtin_amdgcn_permlane32_swap(c[0], c[2], false, false);
                auto s1 = __builtin_amdgcn_permlane32_swap(c[1], c[3], false, false);
                auto s2 = __builtin_amdgcn_permlane32_swap(c[4], c[6], false, false);
                auto s3 = __builtin_amdgcn_permlane32_swap(c[5], c[7], false, false);
                uint16_t* orow = p.out + (size_t)(T * 32 + (lane & 31)) * DM + 32 * u + 8 * h;
                *(GAS u32x4*)(orow) = u32x4{s0[0], s1[0], s0[1], s1[1]};
                *(GAS u32x4*)(orow + 16) = u32x4{s2[0], s3[0], s2[1], s3[1]};
            }
            TS(tb + 19);
        }
    }
    asm volatile("s_waitcnt lgkmcnt(0)" ::: "memory");
    if (w == 0) TS_DUMP();
}

template <int PART> DI void pretile(const Params& p, LAS char* lds, unsigned lds0, rsrc_t rs, int hw, int g, int r, int lane) {
    const int h = lane >> 5, a = (lane & 15) >> 2, pp = lane & 3, ch = (lane >> 4) & 1;
    const int n = r >> 1, j = 2 * g + (r & 1);
    const unsigned scr0 = lds0 + L_SCR + hw * 8192;
    const LAS char* scr = lds + L_SCR + hw * 8192;
    if (hw == 0) {
        const char* base = (const char*)(p.wqkv + (size_t)32 * g * 1536 + 32 * r);
        if (PART == 0) {
#pragma unroll
            for (int i = 0; i < 6; ++i) { const int L = 64 * i + lane, k = L / 12, c = L % 12; glds16s<false>(base, (unsigned)(k * 1536 + (c >> 2) * 512 + (c & 3) * 8) * 2, scr0 + i * 1024); }
            return;
        }
        DRAIN();
        const LAS char* wb = scr + (4 * h + a) * 192 + ch * 32 + pp * 8;
#pragma unroll
        for (int s = 0; s < 2; ++s)
#pragma unroll
            for (int t = 0; t < 3; ++t) stw(rs, lane * 16, WQT_OFF + ((r * 32 + 2 * g + s) * 3 + t) * 1024, wfrag_tr(wb + s * 16 * 192 + t * 64, 8 * 192));
    } else if (hw == 1) {
        const char* base = (const char*)(p.w1 + (size_t)32 * g * FF + 112 * r);
        if (PART == 0) {
#pragma unroll
            for (int b = 0; b < 7; ++b) {
                const int L = 64 * b + lane, G = L / 56, x = L - G * 56;
                const int aa = x < 48 ? ((x >> 2) & 3) : ((x - 48) >> 1);
                const int c = x < 48 ? ((x >> 4) * 4 + (x & 3)) : (12 + (x & 1));
                glds16s<false>(base, (unsigned)((4 * G + aa) * FF + c * 8) * 2, scr0 + b * 1024);
            }
            return;
        }
        DRAIN();
        const LAS char* wbA = scr + h * 896 + a * 64 + ch * 32 + pp * 8;
        const LAS char* wbB = scr + h * 896 + 768 + a * 32 + pp * 8;
#pragma unroll
        for (int s = 0; s < 2; ++s) {
            const int dst = W1T_OFF + r * 114688 + (2 * g + s) * 3584;
#pragma unroll
            for (int t = 0; t < 3; ++t) stw(rs, lane * 16, dst + t * 1024, wfrag_tr(wbA + s * 3584 + t * 256, 1792));
            const u32x4 f3 = wfrag_tr(wbB + s * 3584, 1792);
            if (ch == 0) stw(rs, (h * 16 + (lane & 15)) * 16, dst + 3072, f3);
        }
    } else {
        const int rr = lane >> 3, slot = lane & 7, chunk = slot ^ (((rr >> 1) & 1) << 2);
        const unsigned vo = (unsigned)(rr * DM + chunk * 8) * 2;
        const int s0 = hw == 3 ? j : (j < 16 ? 4 * j : 64 + 3 * (j - 16)), ns = hw == 3 ? 1 : (j < 16 ? 4 : 3);
        const char* base = (const char*)((hw == 3 ? p.wo : p.w2) + (size_t)16 * s0 * DM + 64 * n);
        if (PART == 0) {
            for (int i = 0; i < 2 * ns; ++i) glds16s<false>(base + (size_t)i * 8 * DM * 2, vo, scr0 + i * 1024);
            return;
        }
        DRAIN();
        const int sw = ((a >> 1) & 1) << 6;
        const LAS char* wb0 = scr + (4 * h + a) * 128 + (0 ^ sw) + ch * 32 + pp * 8;
        const LAS char* wb1 = scr + (4 * h + a) * 128 + (64 ^ sw) + ch * 32 + pp * 8;
        for (int i = 0; i < ns; ++i) {
            const int s = s0 + i;
            const int kh = s >= 56 ? 1 : 0, sl = s - 56 * kh;
            const int dst = hw == 3 ? WOT_OFF + n * 65536 + s * 2048 : W2T_OFF + (n * 56 + sl) * 4096 + kh * 2048;
            stw(rs, lane * 16, dst, wfrag_tr(wb0 + i * 2048, 1024));
            stw(rs, lane * 16, dst + 1024, wfrag_tr(wb1 + i * 2048, 1024));
        }
    }
    DRAIN();
}

constexpr int w2_regsp(int j) { return j < 10 ? j : j < 30 ? j + 4 : j + 8; }
DI void helper_path(const Params& p, LAS char* lds, unsigned lds0_, rsrc_t rs, const int w, const int g_, const int r_) {
    unsigned tag = 0;
    TS_DECL();
    {   const int layer = 0; PHASE_VARS(); HELPER_VARS();
        tag = read_tag(p); asm volatile("" :: "s"(tag) : "memory");
        pretile<0>(p, lds, lds0, rs, hw, g, r, lane);
        if (hw == 0) { pretile<1>(p, lds, lds0, rs, hw, g, r, lane); BAR(); }
        else { BAR(); pretile<1>(p, lds, lds0, rs, hw, g, r, lane); }
    }
    for (int layer = 0; layer < 2; ++layer) {
        {
            PHASE_VARS(); HELPER_VARS();
            HTS(layer * 20 + 0);
            BAR();
            if (lyr == 0) {
                if (w == 6 && lane == 0) set_flag(p, 10, g, r, tag);
                stage_lin(wsb, lds0, WQT_OFF + r * 98304, L_WQ, 96, hw, lane);
                float acc = 0.f;
                const LAS char* wbq = lds + L_WQ + (hw < 3 ? hw : 0) * 1024 + lane * 16;
                WAITV(18); BAR();
                acc = colsum_frags<8>(wbq, [](int i) { return (0 + i) * 3072; }, acc);
                WAITV(12); BAR();
                acc = colsum_frags<8>(wbq, [](int i) { return (8 + i) * 3072; }, acc);
                WAITV(6); BAR();
                acc = colsum_frags<8>(wbq, [](int i) { return (16 + i) * 3072; }, acc);
                DRAIN(); BAR();
                acc = colsum_frags<8>(wbq, [](int i) { return (24 + i) * 3072; }, acc);
                acc = sum_xor32(acc);
                if (h == 0 && hw < 3) ((LAS float*)(lds + L_CSQ))[hw * 32 + tok] = acc;
                wait_word(p, flagp(p, 10, lane & 15, (lane & 16) ? (r ^ 1) : r), tag);
            } else {
                __builtin_amdgcn_s_sleep(WODELAY);
                for (int i = hw; i < 62; i += 4) glds16s<true>(wsb + WOT_OFF + n * 65536 + i * 1024, lane * 16, lds0 + 96 * KB + i * 1024);
            }
            BAR();
            if (w == 7 && lane < 2) *(LAS u32x4*)(lds + L_ONES + lane * 1024) = u32x4{0x3f803f80u, 0x3f803f80u, 0x3f803f80u, 0x3f803f80u};
            HTS(layer * 20 + 2);
            stage_lin(wsb, lds0, WOT_OFF + n * 65536 + 62 * KB, 94 * KB, 2, hw, lane);
            stage_lin(wsb, lds0, W1T_OFF + r * 114688, 56 * KB, 35, hw, lane);
            HTS(layer * 20 + 3);
            BAR();
            if (lyr == 0) stage_lin(wsb, lds0, WOT_OFF + n * 65536, 96 * KB, 62, hw, lane);
        }
        u32x4 buf[48];
        {   PHASE_VARS(); HELPER_VARS();

            attn_phase(p, lds, rs, w, g, r, lyr, lane, tag TS_ARG(layer == 0 ? 40 : 44));
            HTS(layer * 20 + 4);
            DRAIN();
            HTS(layer * 20 + 5);
            BAR();
            stage_lin(wsb, lds0, W1T_OFF + r * 114688 + 35 * KB, 0, 39, hw, lane);
            HTS(layer * 20 + 6);
        }
        {
            PHASE_VARS(); HELPER_VARS();
            const int wsrc = W2T_OFF + n * 56 * 4096 + hw * 1024;
            BAR();
#pragma unroll
            for (int j = 0; j < 12; ++j) { buf[j] = ldw(rs, lane * 16, wsrc + w2_regsp(j) * 4096); SB(); }
            BAR();
#pragma unroll
            for (int j = 12; j < 22; ++j) { buf[j] = ldw(rs, lane * 16, wsrc + w2_regsp(j) * 4096); SB(); }
            BAR();
            stage_lin(wsb, lds0, W1T_OFF + r * 114688 + 75264, 94 * KB, 39, hw, lane);
        }
        {
            PHASE_VARS(); HELPER_VARS();
            HTS(layer * 20 + 8);
            WAITV(31);
            BAR();
            const int wsrc = W2T_OFF + n * 56 * 4096 + hw * 1024;
            stage_lin(wsb, lds0, W2T_OFF + (n * 56 + 10) * 4096, 40 * KB, 16, hw, lane);
            stage_lin(wsb, lds0, W2T_OFF + (n * 56 + 34) * 4096, 136 * KB, 16, hw, lane);
#pragma unroll
            for (int j = 22; j < 34; ++j) { buf[j] = ldw(rs, lane * 16, wsrc + w2_regsp(j) * 4096); SB(); }
            WAITV(20);
            HTS(layer * 20 + 10);
            BAR();
#pragma unroll
            for (int j = 34; j < 48; ++j) { buf[j] = ldw(rs, lane * 16, wsrc + w2_regsp(j) * 4096); SB(); }
            if (lyr == 0) {
                LAS float* cs1 = (LAS float*)(lds + L_CS1);
                const LAS char* wb = hw < 3 ? lds + hw * 1024 + lane * 16 : lds + 3072 + (h * 16 + (lane & 15)) * 16;
                float cs_part = colsum_frags<11>(wb, [](int i) { return w1_off(i); }, 0.f);
                cs_part = colsum_frags<11>(wb, [](int i) { return w1_off(11 + i); }, cs_part);
                cs_part = colsum_frags<10>(wb, [](int i) { return w1_off(22 + i); }, cs_part);
                const float s = sum_xor32(cs_part);
                if (h == 0 && (hw < 3 || tok < 16)) cs1[hw * 32 + tok] = s;
            }
            WAITV(14);
            BAR();
            HTS(layer * 20 + 11);
#pragma unroll
            for (int j = 0; j < 30; ++j) *(LAS u32x4*)(lds + w2_slot(w2_regsp(j)) + hw * 1024 + lane * 16) = buf[j];
            if (lyr == 0) {
#pragma unroll
                for (int j = 0; j < 24; ++j) { buf[j] = ldw(rs, lane * 16, WQT_OFF + r * 98304 + (4 * j + hw) * 1024); SB(); }
            }
            HTS(layer * 20 + 12);
            BAR();
        }
        {
            PHASE_VARS(); HELPER_VARS();
            BAR();
            BAR();
#pragma unroll
            for (int j = 30; j < 48; ++j) *(LAS u32x4*)(lds + w2_slot(w2_regsp(j)) + hw * 1024 + lane * 16) = buf[j];
            if (lyr == 1 && blockIdx.x == 0 && w == 7) {
                bool ok = false;
                for (unsigned spins = 0; spins < (1u << 16) && !ok; ++spins) {
                    bool mine = true;
#pragma unroll
                    for (int j = 0; j < 4; ++j) mine &= __hip_atomic_load((gu32*)(p.ws + ACK_OFF) + (lane + 64 * j) * 32, __ATOMIC_RELAXED, __HIP_MEMORY_SCOPE_AGENT) == tag;
                    ok = __all(mine);
                }
                if (lane == 0) __hip_atomic_store((gu32*)(p.ws + EPOCH_OFF), tag, __ATOMIC_RELAXED, __HIP_MEMORY_SCOPE_AGENT);
            }
            HTS(layer * 20 + 15);
            BAR();
            BAR();
            if (lyr == 0) {
#pragma unroll
                for (int j = 0; j < 24; ++j) *(LAS u32x4*)(lds + L_WQ + (4 * j + hw) * 1024 + lane * 16) = buf[j];
                HTS(17);
                BAR();
            }
        }
    }
    asm volatile("s_waitcnt lgkmcnt(0)" ::: "memory");
    if (w == 4) HTS_DUMP();
}

__global__ __launch_bounds__(512, 2) void k_mega(Params p) {
    extern __shared__ __attribute__((aligned(16))) char smem_[];
    LAS char* lds = (LAS char*)smem_;
    const unsigned lds0 = (unsigned)(uintptr_t)smem_;
    const int b = blockIdx.x, idx = b >> 3;
    const int g_ = ((b & 7) >> 1) * 4 + (idx >> 3), r_ = (b & 1) * 8 + (idx & 7);
    const int w = __builtin_amdgcn_readfirstlane(threadIdx.x >> 6);
    const rsrc_t rs = __builtin_amdgcn_make_buffer_rsrc((void*)p.ws, 0, WS_USED, 0x00020000);
    if (w < 4) main_path(p, lds, rs, w, g_, r_);
    else helper_path(p, lds, lds0, rs, w, g_, r_);
}
}

extern "C" void kernel_launch(void* const* d_in, const int* in_sizes, int n_in, void* d_out, int out_size, void* d_ws, size_t ws_size, hipStream_t stream) {
    Params p;
    p.x = (const uint16_t*)d_in[0]; p.wqkv = (const uint16_t*)d_in[1]; p.wo = (const uint16_t*)d_in[2];
    p.w1 = (const uint16_t*)d_in[4]; p.w2 = (const uint16_t*)d_in[5];
    p.out = (uint16_t*)d_out; p.ws = (char*)d_ws;
    (void)hipFuncSetAttribute((const void*)k_mega, hipFuncAttributeMaxDynamicSharedMemorySize, LDS_BYTES);
    k_mega<<<256, 512, LDS_BYTES, stream>>>(p);
}
```

```cpp
#include <hip/hip_runtime.h>
#include <hip/hip_bf16.h>
#include <stdint.h>

#define GAS __attribute__((address_space(1)))
#define LAS __attribute__((address_space(3)))
#define DI __device__ __forceinline__

typedef __bf16 bf16x8 __attribute__((ext_vector_type(8)));
typedef __bf16 bf16x2 __attribute__((ext_vector_type(2)));
typedef short s16x4 __attribute__((ext_vector_type(4)));
typedef float f32x16 __attribute__((ext_vector_type(16)));
typedef float f32x4 __attribute__((ext_vector_type(4)));
typedef float f32x2 __attribute__((ext_vector_type(2)));
typedef unsigned u32x4 __attribute__((ext_vector_type(4)));
typedef unsigned u32x2 __attribute__((ext_vector_type(2)));
typedef GAS unsigned gu32;

#define TS_DECL()
#define TS(i)
#define TS_DUMP()
#define HTS(i)
#define HTS_DUMP()
#define MAPMODE 1
namespace {
constexpr int DM = 512, FF = 1792;
constexpr float EPS = 1e-5f;
constexpr float LOG2E = 1.4426950408889634f;
constexpr float QSCALE = 0.25f * LOG2E;

constexpr int MB = 1 << 20, KB = 1024;
constexpr int XF_OFF = 0;
constexpr int O_OFF = 4 * MB;
constexpr int X2_OFF = 8 * MB;
constexpr int H_OFF = 12 * MB;
constexpr int KV_OFF = 28 * MB;
constexpr int WQT_OFF = 36 * MB;
constexpr int WOT_OFF = WQT_OFF + 1536 * KB;
constexpr int W1T_OFF = 38 * MB;
constexpr int W2T_OFF = 40 * MB;
constexpr int ST_OFF = 42 * MB;
constexpr int ST_BUF = 262144;
constexpr int FLAG_OFF = 43 * MB;
constexpr int FLAG_BYTES = 11 * 256 * 128;
constexpr int TMO_OFF = FLAG_OFF + FLAG_BYTES;
constexpr int EPOCH_OFF = TMO_OFF + 256;
constexpr int ACK_OFF = EPOCH_OFF + 256;
constexpr int WS_USED = 44 * MB;

constexpr int L_WQ = 0;
constexpr int L_Q = 0, L_KV = 8 * KB, L_ONES = 91 * KB;
constexpr int L_WO = 94 * KB, L_RED2 = 40 * KB;
constexpr int L_RED4 = 96 * KB;
constexpr int L_SCR = 100 * KB;
constexpr int L_TAB = 158 * KB, L_CSQ = L_TAB, L_CS1 = L_TAB + 512, L_STT = L_TAB + 1024;
constexpr int LDS_BYTES = 160 * KB;
constexpr int w1_off(int s) { return s < 10 ? 56 * KB + s * 3584 : s < 21 ? (s - 10) * 3584 : 94 * KB + (s - 21) * 3584; }
constexpr int w2_slot(int sp) { return (sp < 38 ? sp : sp - 38) * 4096; }
constexpr int AUX_SC1 = 16;

struct Params {
    const uint16_t* x; const uint16_t* wqkv; const uint16_t* wo; const uint16_t* w1; const uint16_t* w2;
    uint16_t* out; char* ws;
};

DI f32x16 mfma32(bf16x8 a, bf16x8 b, f32x16 c) { return __builtin_amdgcn_mfma_f32_32x32x16_bf16(a, b, c, 0, 0, 0); }
DI unsigned pk(float a, float b) { bf16x2 v = {(__bf16)a, (__bf16)b}; return __builtin_bit_cast(unsigned, v); }
DI float bflo(unsigned u) { return __uint_as_float(u << 16); }
DI float bfhi(unsigned u) { return __uint_as_float(u & 0xffff0000u); }
DI bf16x8 asfrag(u32x4 v) { return __builtin_bit_cast(bf16x8, v); }
DI s16x4 trd(const LAS char* p) { return __builtin_amdgcn_ds_read_tr16_b64_v4i16((LAS s16x4*)p); }
DI u32x4 wfrag_tr(const LAS char* p, int off2) {
    const u32x2 lo = __builtin_bit_cast(u32x2, trd(p)), hi = __builtin_bit_cast(u32x2, trd(p + off2));
    return u32x4{lo.x, lo.y, hi.x, hi.y};
}
DI bf16x8 ldsfrag(const LAS char* p) { return *(const LAS bf16x8*)p; }
template <int N, class F> DI float colsum_frags(const LAS char* p, F off, float acc) {
    u32x4 f[N];
#pragma unroll
    for (int i = 0; i < N; ++i) f[i] = *(const LAS u32x4*)(p + off(i));
    float a1 = 0.f;
#pragma unroll
    for (int i = 0; i < N; ++i) {
        unsigned w0 = f[i].x, w1 = f[i].y, w2 = f[i].z, w3 = f[i].w;
        asm("v_dot2c_f32_bf16 %0, %2, %6\n\tv_dot2c_f32_bf16 %1, %3, %6\n\tv_dot2c_f32_bf16 %0, %4, %6\n\tv_dot2c_f32_bf16 %1, %5, %6"
            : "+v"(acc), "+v"(a1) : "v"(w0), "v"(w1), "v"(w2), "v"(w3), "v"(0x3f803f80u));
    }
    asm volatile("s_nop 2" : "+v"(acc), "+v"(a1));
    return acc + a1;
}
DI float sum_xor32(float v) { auto r = __builtin_amdgcn_permlane32_swap(__float_as_uint(v), __float_as_uint(v), false, false); return __uint_as_float(r[0]) + __uint_as_float(r[1]); }
DI float max_xor32(float v) { auto r = __builtin_amdgcn_permlane32_swap(__float_as_uint(v), __float_as_uint(v), false, false); return fmaxf(__uint_as_float(r[0]), __uint_as_float(r[1])); }
template <bool SC1> DI void glds16s(const void* sbase, unsigned voff, unsigned lds_addr) {
    unsigned keep;
    if (SC1) asm volatile("s_mov_b32 %0, m0\n\ts_mov_b32 m0, %3\n\ts_nop 0\n\tglobal_load_lds_dwordx4 %1, %2 sc1\n\ts_mov_b32 m0, %0" : "=&s"(keep) : "v"(voff), "s"(sbase), "s"(lds_addr) : "memory");
    else asm volatile("s_mov_b32 %0, m0\n\ts_mov_b32 m0, %3\n\ts_nop 0\n\tglobal_load_lds_dwordx4 %1, %2\n\ts_mov_b32 m0, %0" : "=&s"(keep) : "v"(voff), "s"(sbase), "s"(lds_addr) : "memory");
}
#define BAR() asm volatile("s_waitcnt lgkmcnt(0)\n\ts_barrier" ::: "memory")
#define DRAIN() asm volatile("s_waitcnt vmcnt(0)" ::: "memory")
#define WAITV(n) asm volatile("s_waitcnt vmcnt(" #n ")" ::: "memory")
#define SB() __builtin_amdgcn_sched_barrier(0)
#define SGB(mask, n) __builtin_amdgcn_sched_group_barrier(mask, n, 0)

typedef __amdgpu_buffer_rsrc_t rsrc_t;
DI u32x4 ldw(rsrc_t rs, int voff, int soff) { return __builtin_amdgcn_raw_buffer_load_b128(rs, voff, soff, AUX_SC1); }
DI void stw(rsrc_t rs, int voff, int soff, u32x4 v) { __builtin_amdgcn_raw_buffer_store_b128(v, rs, voff + soff, 0, AUX_SC1); }
DI f32x2 ldst(rsrc_t rs, int voff, int soff) { return __builtin_bit_cast(f32x2, __builtin_amdgcn_raw_buffer_load_b64(rs, voff, soff, AUX_SC1)); }
DI void stst(rsrc_t rs, int voff, int soff, f32x2 v) { __builtin_amdgcn_raw_buffer_store_b64(__builtin_bit_cast(u32x2, v), rs, voff, soff, AUX_SC1); }

DI gu32* flagp(const Params& p, int ph, int g, int r) { return (gu32*)(p.ws + FLAG_OFF) + ((ph * 16 + g) * 16 + r) * 32; }
DI void set_flag(const Params& p, int ph, int g, int r, unsigned tag) { __hip_atomic_store(flagp(p, ph, g, r), tag, __ATOMIC_RELAXED, __HIP_MEMORY_SCOPE_AGENT); }
DI void wait_word(const Params& p, gu32* a, unsigned tag) {
    for (unsigned spins = 0;; ++spins) {
        const unsigned v = __hip_atomic_load(a, __ATOMIC_RELAXED, __HIP_MEMORY_SCOPE_AGENT);
        if (__all(v == tag)) break;
        if (spins > (1u << 20)) { __hip_atomic_store((gu32*)(p.ws + TMO_OFF), 1u, __ATOMIC_RELAXED, __HIP_MEMORY_SCOPE_AGENT); break; }
        __builtin_amdgcn_s_sleep(1);
    }
    __builtin_amdgcn_fence(__ATOMIC_ACQUIRE, "wavefront");
}
DI void wait_group(const Params& p, int ph, int g, unsigned tag, int lane) { wait_word(p, flagp(p, ph, g, lane & 15), tag); }

DI void stats_store(rsrc_t rs, int buf, int T, int u, int lane, const unsigned (&c)[8]) {
    float s = 0.f, ss = 0.f;
#pragma unroll
    for (int i = 0; i < 8; ++i) { float lo = bflo(c[i]), hi = bfhi(c[i]); s += lo + hi; ss += lo * lo + hi * hi; }
    s = sum_xor32(s); ss = sum_xor32(ss);
    if (lane < 32) stst(rs, lane * 8, ST_OFF + buf * ST_BUF + (T * 16 + u) * 256, f32x2{s, ss});
}

DI void stage_lin(const Params& p, unsigned lds0, int src, int dst, int n, int hw, int lane, int nw = 4) {
    const unsigned vo = lane * 16;
    for (int i = hw; i < n; i += nw) glds16s<true>(p.ws + src + i * 1024, vo, lds0 + dst + i * 1024);
}

#define PHASE_VARS() \
    int lane = threadIdx.x & 63; asm volatile("" : "+v"(lane)); \
    int lyr = layer; asm volatile("" : "+s"(lyr)); \
    int g = g_, r = r_; asm volatile("" : "+s"(g), "+s"(r)); \
    const int h = lane >> 5, tok = lane & 31; const int m = r & 1, n = r >> 1; (void)h; (void)tok; (void)m; (void)n; (void)lyr;

DI void attn_finish(rsrc_t rs, const f32x16& o, int hd, int T, int r, int lyr, int lane) {
    float ov[8]; float l;
    if (hd == 0) { l = o[8];
#pragma unroll
        for (int i = 0; i < 8; ++i) ov[i] = o[i];
    } else { l = o[0];
#pragma unroll
        for (int i = 0; i < 8; ++i) ov[i] = o[8 + i];
    }
    const float inv = __builtin_amdgcn_rcpf(l);
    stw(rs, lane * 16, O_OFF + lyr * 2 * MB + (T * 32 + 2 * r + hd) * 1024,
        u32x4{pk(ov[0] * inv, ov[1] * inv), pk(ov[2] * inv, ov[3] * inv), pk(ov[4] * inv, ov[5] * inv), pk(ov[6] * inv, ov[7] * inv)});
}
DI float max16(float m, const f32x16& s) {
#pragma unroll
    for (int i = 0; i < 16; i += 2) m = fmaxf(fmaxf(m, s[i]), s[i + 1]);
    return m;
}
DI void max16x2(float& m0, float& m1, const f32x16& s) {
#pragma unroll
    for (int i = 0; i < 16; i += 4) { m0 = fmaxf(fmaxf(m0, s[i]), s[i + 1]); m1 = fmaxf(fmaxf(m1, s[i + 2]), s[i + 3]); }
}
DI void exp_pack(const f32x16& s, u32x4& p0, u32x4& p1) {
    unsigned pw[8];
#pragma unroll
    for (int i = 0; i < 8; ++i) pw[i] = pk(__builtin_amdgcn_exp2f(s[2 * i]), __builtin_amdgcn_exp2f(s[2 * i + 1]));
    p0 = u32x4{pw[0], pw[1], pw[2], pw[3]}; p1 = u32x4{pw[4], pw[5], pw[6], pw[7]};
}
DI void attn_unit(LAS char* lds, rsrc_t rs, int w, int g, int r, int lyr, int lane) {
    const int h = lane >> 5, tok = lane & 31;
    const int tw = w & 3, hd = w >> 2, T = 4 * g + tw;
    const bf16x8 qf = *(const LAS bf16x8*)(lds + L_Q + tw * 2048 + hd * 1024 + lane * 16);
    f32x16 mD, mF;
#pragma unroll
    for (int i = 0; i < 16; ++i) {
        const float t = __builtin_fmaf((float)(tok - 4 * h), 1e30f, (0.5f - (float)((i & 3) + 8 * (i >> 2))) * 1e30f);
        mD[i] = fminf(t, 0.f); mF[i] = fminf(-t, 0.f);
    }
    const LAS char* kb = lds + L_KV + tw * 4096 + hd * 1024 + lane * 16;
    const bool isdata = ((lane >> 4) & 1) == hd;
    const LAS char* vb = isdata ? (lds + L_KV + tw * 4096 + 2048 + lane * 16) : (lds + L_ONES);
    const int vstep = isdata ? 4096 : 0;
    const f32x16 zero = {};
    f32x16 o = {};
    if (T >= 8) {
        bf16x8 kf[9];
#pragma unroll
        for (int jj = 0; jj < 9; ++jj) kf[jj] = *(const LAS bf16x8*)(kb + jj * 4096);
        float mx = -INFINITY, mx1 = -INFINITY;
        f32x16 s[2];
        s[0] = mfma32(kf[0], qf, mF); s[1] = mfma32(kf[1], qf, zero);
#pragma unroll
        for (int jj = 0; jj < 9; ++jj) {
            const f32x16 cur = s[jj & 1];
            max16x2(mx, mx1, cur);
            if (jj + 2 < 9) s[jj & 1] = mfma32(kf[jj + 2], qf, jj + 2 == 8 ? mD : zero);
            SB();
        }
        mx = max_xor32(fmaxf(mx, mx1));
        f32x16 cm, cD, cF;
#pragma unroll
        for (int i = 0; i < 16; ++i) { cm[i] = -mx; cD[i] = mD[i] - mx; cF[i] = mF[i] - mx; }
        bf16x8 v0 = *(const LAS bf16x8*)(vb), v1 = *(const LAS bf16x8*)(vb + 1024);
        s[0] = mfma32(kf[0], qf, cF); s[1] = mfma32(kf[1], qf, cm);
#pragma unroll
        for (int jj = 0; jj < 9; ++jj) {
            u32x4 p0, p1;
            exp_pack(s[jj & 1], p0, p1);
            if (jj + 2 < 9) s[jj & 1] = mfma32(kf[jj + 2], qf, jj + 2 == 8 ? cD : cm);
            o = mfma32(v0, asfrag(p0), o);
            o = mfma32(v1, asfrag(p1), o);
            if (jj + 1 < 9) { const LAS char* va = vb + (jj + 1) * vstep; v0 = *(const LAS bf16x8*)(va); v1 = *(const LAS bf16x8*)(va + 1024); }
            SB();
        }
    } else {
        const int j0 = 8 - T;
        float mx = -INFINITY;
        for (int jj = j0; jj < 9; ++jj) {
            const bf16x8 kf = *(const LAS bf16x8*)(kb + jj * 4096);
            f32x16 s = mfma32(kf, qf, zero);
            if (jj == 8) {
#pragma unroll
                for (int i = 0; i < 16; ++i) s[i] += mD[i];
            }
            mx = max16(mx, s);
        }
        mx = max_xor32(mx);
        for (int jj = j0; jj < 9; ++jj) {
            const bf16x8 kf = *(const LAS bf16x8*)(kb + jj * 4096);
            const LAS char* va = vb + jj * vstep;
            const bf16x8 v0 = *(const LAS bf16x8*)(va), v1 = *(const LAS bf16x8*)(va + 1024);
            f32x16 s = mfma32(kf, qf, zero);
#pragma unroll
            for (int i = 0; i < 16; ++i) s[i] = (jj == 8 ? s[i] + mD[i] : s[i]) - mx;
            u32x4 p0, p1;
            exp_pack(s, p0, p1);
            o = mfma32(v0, asfrag(p0), o);
            o = mfma32(v1, asfrag(p1), o);
        }
    }
    attn_finish(rs, o, hd, T, r, lyr, lane);
}
DI void halo_load(LAS char* lds, rsrc_t rs, int w, int g, int r, int lyr, int lane) {
    const int tile = 4 * g - 8 + w;
    if (tile >= 0) {
        const int kvo = KV_OFF + lyr * 4 * MB + (tile * 16 + r) * 4096;
        u32x4 t0 = ldw(rs, lane * 16, kvo), t1 = ldw(rs, lane * 16 + 1024, kvo), t2 = ldw(rs, lane * 16 + 2048, kvo), t3 = ldw(rs, lane * 16 + 3072, kvo);
        LAS char* kvl = lds + L_KV + w * 4096 + lane * 16;
        *(LAS u32x4*)(kvl) = t0; *(LAS u32x4*)(kvl + 1024) = t1; *(LAS u32x4*)(kvl + 2048) = t2; *(LAS u32x4*)(kvl + 3072) = t3;
    }
}
DI void ln_stats(const f32x2 (&sv)[8], float& mean, float& rstd) {
    float S = 0.f, SS = 0.f;
#pragma unroll
    for (int i = 0; i < 8; ++i) { S += sv[i].x; SS += sv[i].y; }
    S = sum_xor32(S); SS = sum_xor32(SS);
    mean = S * (1.f / DM); const float var = fmaxf(SS * (1.f / DM) - mean * mean, 0.f); rstd = 1.f / sqrtf(var + EPS);
}
DI unsigned read_tag(const Params& p) {
    return (unsigned)__builtin_amdgcn_readfirstlane(__hip_atomic_load((gu32*)(p.ws + EPOCH_OFF), __ATOMIC_RELAXED, __HIP_MEMORY_SCOPE_AGENT)) + 1u;
}
#define LDA(arr, s, base) arr[s] = ldw(rs, lane * 16 + ((s) & 3) * 1024, (base) + ((s) >> 2) * 4096)

#define PD0 4
#define PDV 12
constexpr int a_issued(int s, int total) { int v = PD0 + 2 * (s + 1); const int cap = s + 1 + PDV; v = v < cap ? v : cap; return v < total ? v : total; }

DI void main_path(const Params& p, LAS char* lds, rsrc_t rs, const int w, const int g_, const int r_) {
    const int tid = threadIdx.x;
    unsigned tag = 0;
    TS_DECL();
    TS(0);
    {
        const int layer = 0; PHASE_VARS();
        const int T = 4 * g + (r & 3), u = (r >> 2) * 4 + w;
        const uint16_t* row = p.x + (size_t)(T * 32 + tok) * DM + 32 * u + 4 * h;
        u32x2 a0 = *(const GAS u32x2*)(row), a1 = *(const GAS u32x2*)(row + 8), a2 = *(const GAS u32x2*)(row + 16), a3 = *(const GAS u32x2*)(row + 24);
        if (w == 0) { tag = read_tag(p); asm volatile("" :: "s"(tag) : "memory"); }
        stw(rs, lane * 16, XF_OFF + (T * 32 + 2 * u) * 1024, u32x4{a0.x, a0.y, a1.x, a1.y});
        stw(rs, lane * 16, XF_OFF + (T * 32 + 2 * u + 1) * 1024, u32x4{a2.x, a2.y, a3.x, a3.y});
        const unsigned c[8] = {a0.x, a0.y, a1.x, a1.y, a2.x, a2.y, a3.x, a3.y};
        stats_store(rs, 0, T, u, lane, c);
        DRAIN();
        BAR();
        if (tid == 0) { set_flag(p, 0, g, r, tag); __hip_atomic_store((gu32*)(p.ws + ACK_OFF) + blockIdx.x * 32, tag, __ATOMIC_RELAXED, __HIP_MEMORY_SCOPE_AGENT); }
    }
    TS(1);
    for (int layer = 0; layer < 2; ++layer) {
        const int tb = 2 + layer * 24; (void)tb;
        {
            PHASE_VARS();
            if (w == 0) {
                if (lyr == 0) {
                    const int j = lane & 15;
                    wait_word(p, (lane & 16) ? flagp(p, 0, j, r) : flagp(p, 0, g, j), tag);
                } else wait_group(p, 5, g, tag, lane);
            }
            BAR();
            TS(tb + 0);
            const int T = 4 * g + w;
            const LAS char* wb = lds + L_WQ + lane * 16;
            LAS float* csq = (LAS float*)(lds + L_CSQ);
            LAS float* stt = (LAS float*)(lds + L_STT);
            f32x16 aq = {}, ak = {}, av = {};
            u32x4 xa[32];
            f32x2 sv[8];
            const int sto = ST_OFF + 2 * lyr * ST_BUF + T * 4096, xo = XF_OFF + lyr * 2 * MB + T * 32768;
#pragma unroll
            for (int s = 0; s < PD0; ++s) { LDA(xa, s, xo); SB(); }
            if (lyr == 0) BAR();
            bf16x8 wf[3][3];
#pragma unroll
            for (int s = 0; s < 2; ++s)
#pragma unroll
                for (int t = 0; t < 3; ++t) wf[s][t] = ldsfrag(wb + (s * 3 + t) * 1024);
#pragma unroll
            for (int s = 0; s < 32; ++s) {
                if ((s == 6 || s == 14 || s == 22) && lyr == 0) BAR();
#pragma unroll
                for (int j = (s == 0 ? PD0 : a_issued(s - 1, 32)); j < a_issued(s, 32); ++j) LDA(xa, j, xo);
                if (s >= 16 && s < 24) sv[s - 16] = ldst(rs, (8 * h * 32 + tok) * 8 + (s - 16) * 256, sto);
                if (s + 2 < 32) {
#pragma unroll
                    for (int t = 0; t < 3; ++t) wf[(s + 2) % 3][t] = ldsfrag(wb + ((s + 2) * 3 + t) * 1024);
                }
                const bf16x8 x = asfrag(xa[s]);
                aq = mfma32(wf[s % 3][0], x, aq); ak = mfma32(wf[s % 3][1], x, ak); av = mfma32(x, wf[s % 3][2], av);
                SGB(0x100, 1); SGB(0x8, 1); SGB(0x20, 1); SGB(0x100, 1); SGB(0x8, 1); SGB(0x20, 1); SGB(0x100, 1); SGB(0x8, 1);
                SB();
            }
            float mean, rstd; ln_stats(sv, mean, rstd);
            if (h == 0) { stt[w * 64 + tok] = rstd; stt[w * 64 + 32 + tok] = mean * rstd; }
            TS(tb + 1);
            BAR();
            TS(tb + 2);
            const float mr = mean * rstd;
            float q[16], k[16], v[16];
            const float cv = csq[64 + tok];
#pragma unroll
            for (int g4 = 0; g4 < 4; ++g4) {
                f32x4 cq = *(LAS f32x4*)(csq + 8 * g4 + 4 * h), ck = *(LAS f32x4*)(csq + 32 + 8 * g4 + 4 * h);
                f32x4 rsv = *(LAS f32x4*)(stt + w * 64 + 8 * g4 + 4 * h), ms = *(LAS f32x4*)(stt + w * 64 + 32 + 8 * g4 + 4 * h);
#pragma unroll
                for (int i = 0; i < 4; ++i) {
                    k[4 * g4 + i] = ak[4 * g4 + i] * rstd - mr * ck[i];
                    v[4 * g4 + i] = av[4 * g4 + i] * rsv[i] - ms[i] * cv;
                    q[4 * g4 + i] = (aq[4 * g4 + i] * rstd - mr * cq[i]) * QSCALE;
                }
            }
            const u32x4 k0 = {pk(k[0], k[1]), pk(k[2], k[3]), pk(k[4], k[5]), pk(k[6], k[7])};
            const u32x4 k1 = {pk(k[8], k[9]), pk(k[10], k[11]), pk(k[12], k[13]), pk(k[14], k[15])};
            const u32x4 v0 = {pk(v[0], v[1]), pk(v[2], v[3]), pk(v[4], v[5]), pk(v[6], v[7])};
            const u32x4 v1 = {pk(v[8], v[9]), pk(v[10], v[11]), pk(v[12], v[13]), pk(v[14], v[15])};
            const int kvo = KV_OFF + lyr * 4 * MB + (T * 16 + r) * 4096;
            stw(rs, lane * 16, kvo, k0); stw(rs, lane * 16 + 1024, kvo, k1); stw(rs, lane * 16 + 2048, kvo, v0); stw(rs, lane * 16 + 3072, kvo, v1);
            LAS char* kvl = lds + L_KV + (8 + w) * 4096 + lane * 16;
            *(LAS u32x4*)(kvl) = k0; *(LAS u32x4*)(kvl + 1024) = k1; *(LAS u32x4*)(kvl + 2048) = v0; *(LAS u32x4*)(kvl + 3072) = v1;
            LAS char* ql = lds + L_Q + w * 2048 + lane * 16;
            *(LAS u32x4*)(ql) = u32x4{pk(q[0], q[1]), pk(q[2], q[3]), pk(q[4], q[5]), pk(q[6], q[7])};
            *(LAS u32x4*)(ql + 1024) = u32x4{pk(q[8], q[9]), pk(q[10], q[11]), pk(q[12], q[13]), pk(q[14], q[15])};
            DRAIN();
            BAR();
            if (tid == 0) set_flag(p, 1 + 5 * lyr, g, r, tag);
            TS(tb + 3);
            BAR();
            TS(tb + 4);
            halo_load(lds, rs, w, g, r, lyr, lane);
            BAR();
            TS(tb + 5);
        }
        {
            PHASE_VARS();
            attn_unit(lds, rs, w, g, r, lyr, lane);
            TS(tb + 6);
            DRAIN();
            BAR();
            if (tid == 0) set_flag(p, 2 + 5 * lyr, g, r, tag);
            TS(tb + 7);
        }
        {
            PHASE_VARS();
            const int mt = w & 1, kh = (w >> 1) & 1, T = 4 * g + 2 * m + mt, u = 2 * n + kh;
            const int xo = XF_OFF + lyr * 2 * MB + (T * 32 + 2 * u) * 1024;
            const u32x4 r0 = ldw(rs, lane * 16, xo), r1 = ldw(rs, lane * 16 + 1024, xo);
            if (w == 0) wait_group(p, 2 + 5 * lyr, g, tag, lane);
            BAR();
            TS(tb + 8);
            f32x16 acc[2] = {};
            u32x4 xa[16];
            const int oo = O_OFF + lyr * 2 * MB + (T * 32 + 16 * kh) * 1024;
#pragma unroll
            for (int s = 0; s < PD0; ++s) { LDA(xa, s, oo); SB(); }
            const LAS char* wb = lds + L_WO + kh * 32768 + lane * 16;
            bf16x8 wf[4][2];
#pragma unroll
            for (int s = 0; s < 3; ++s) { wf[s][0] = ldsfrag(wb + s * 2048); wf[s][1] = ldsfrag(wb + s * 2048 + 1024); }
#pragma unroll
            for (int s = 0; s < 16; ++s) {
#pragma unroll
                for (int j = (s == 0 ? PD0 : a_issued(s - 1, 16)); j < a_issued(s, 16); ++j) LDA(xa, j, oo);
                if (s + 3 < 16) { wf[(s + 3) & 3][0] = ldsfrag(wb + (s + 3) * 2048); wf[(s + 3) & 3][1] = ldsfrag(wb + (s + 3) * 2048 + 1024); }
                const bf16x8 x = asfrag(xa[s]);
                acc[0] = mfma32(wf[s & 3][0], x, acc[0]); acc[1] = mfma32(wf[s & 3][1], x, acc[1]);
                SGB(0x100, 1); SGB(0x8, 1); SGB(0x20, 2); SGB(0x100, 1); SGB(0x8, 1);
                SB();
            }
            const f32x16 snd = kh ? acc[0] : acc[1];
#pragma unroll
            for (int g4 = 0; g4 < 4; ++g4) *(LAS f32x4*)(lds + L_RED2 + ((w * 4 + g4) * 64 + lane) * 16) = f32x4{snd[4 * g4], snd[4 * g4 + 1], snd[4 * g4 + 2], snd[4 * g4 + 3]};
            TS(tb + 9);
            BAR();
            const f32x16 own = kh ? acc[1] : acc[0];
            const int pw = w ^ 2;
            float v[16];
#pragma unroll
            for (int g4 = 0; g4 < 4; ++g4) { f32x4 t = *(LAS f32x4*)(lds + L_RED2 + ((pw * 4 + g4) * 64 + lane) * 16);
#pragma unroll
                for (int i = 0; i < 4; ++i) v[4 * g4 + i] = own[4 * g4 + i] + t[i]; }
            const unsigned rr[8] = {r0.x, r0.y, r0.z, r0.w, r1.x, r1.y, r1.z, r1.w};
            unsigned c[8];
#pragma unroll
            for (int i = 0; i < 8; ++i) c[i] = pk(v[2 * i] + bflo(rr[i]), v[2 * i + 1] + bfhi(rr[i]));
            const int yo = X2_OFF + lyr * 2 * MB + (T * 32 + 2 * u) * 1024;
            stw(rs, lane * 16, yo, u32x4{c[0], c[1], c[2], c[3]});
            stw(rs, lane * 16 + 1024, yo, u32x4{c[4], c[5], c[6], c[7]});
            stats_store(rs, 2 * lyr + 1, T, u, lane, c);
            DRAIN();
            BAR();
            if (tid == 0) set_flag(p, 3 + 5 * lyr, g, r, tag);
            TS(tb + 10);
        }
        {
            PHASE_VARS();
            if (w == 0) wait_group(p, 3 + 5 * lyr, g, tag, lane);
            BAR();
            TS(tb + 11);
            const int T = 4 * g + w;
            const LAS char* wbA = lds + lane * 16;
            const LAS char* wbB = lds + 3072 + (h * 16 + (lane & 15)) * 16;
            LAS float* cs1 = (LAS float*)(lds + L_CS1);
            f32x16 acc[4] = {};
            u32x4 xa[32];
            f32x2 sv[8];
            const int sto = ST_OFF + (2 * lyr + 1) * ST_BUF + T * 4096, xo = X2_OFF + lyr * 2 * MB + T * 32768;
#pragma unroll
            for (int s = 0; s < PD0; ++s) { LDA(xa, s, xo); SB(); }
            auto ldW = [&](int s, int t) { return t < 3 ? ldsfrag(wbA + w1_off(s) + t * 1024) : ldsfrag(wbB + w1_off(s)); };
            bf16x8 wf[2][4];
#pragma unroll
            for (int t = 0; t < 4; ++t) wf[0][t] = ldW(0, t);
#pragma unroll
            for (int s = 0; s < 32; ++s) {
                if (s == 20) BAR();
#pragma unroll
                for (int j = (s == 0 ? PD0 : a_issued(s - 1, 32)); j < a_issued(s, 32); ++j) LDA(xa, j, xo);
                if (s >= 16 && s < 24) sv[s - 16] = ldst(rs, (8 * h * 32 + tok) * 8 + (s - 16) * 256, sto);
                const bf16x8 x = asfrag(xa[s]);
#pragma unroll
                for (int t = 0; t < 4; ++t) {
                    if (s + 1 < 32) wf[(s + 1) & 1][t] = ldW(s + 1, t);
                    acc[t] = mfma32(wf[s & 1][t], x, acc[t]);
                }
                SGB(0x100, 1); SGB(0x8, 1); SGB(0x20, 1); SGB(0x100, 1); SGB(0x8, 1); SGB(0x20, 1); SGB(0x100, 1); SGB(0x8, 1); SGB(0x100, 1); SGB(0x8, 1);
                SB();
            }
            float mean, rstd; ln_stats(sv, mean, rstd);
            const float mr = mean * rstd;
            TS(tb + 12);
            BAR();
            TS(tb + 13);
            const int ho = H_OFF + lyr * 8 * MB + (T * 112 + 7 * r) * 1024;
#pragma unroll
            for (int t = 0; t < 4; ++t) {
#pragma unroll
                for (int sp = 0; sp < (t < 3 ? 2 : 1); ++sp) {
                    float hv[8];
#pragma unroll
                    for (int g2 = 0; g2 < 2; ++g2) {
                        const f32x4 c4 = *(LAS f32x4*)(cs1 + t * 32 + 16 * sp + 8 * g2 + 4 * h);
#pragma unroll
                        for (int i = 0; i < 4; ++i) {
                            const float z = acc[t][8 * sp + 4 * g2 + i] * rstd - mr * c4[i];
                            hv[4 * g2 + i] = z * __builtin_amdgcn_rcpf(1.f + __builtin_amdgcn_exp2f(-LOG2E * z));
                        }
                    }
                    stw(rs, lane * 16 + (2 * t + sp) * 1024, ho, u32x4{pk(hv[0], hv[1]), pk(hv[2], hv[3]), pk(hv[4], hv[5]), pk(hv[6], hv[7])});
                }
            }
            DRAIN();
            BAR();
            if (tid == 0) set_flag(p, 4 + 5 * lyr, g, r, tag);
            TS(tb + 14);
        }
        {
            PHASE_VARS();
            const int mt = w & 1, kh = (w >> 1) & 1, T = 4 * g + 2 * m + mt, u = 2 * n + kh;
            const int yo = X2_OFF + lyr * 2 * MB + (T * 32 + 2 * u) * 1024;
            const u32x4 r0 = ldw(rs, lane * 16, yo), r1 = ldw(rs, lane * 16 + 1024, yo);
            if (w == 0) wait_group(p, 4 + 5 * lyr, g, tag, lane);
            BAR();
            TS(tb + 15);
            const LAS char* wb = lds + kh * 2048 + lane * 16;
            f32x16 acc[2] = {};
            const int ho = H_OFF + lyr * 8 * MB + (T * 112 + 56 * kh) * 1024;
            u32x4 xa[56];
#pragma unroll
            for (int s = 0; s < PD0; ++s) { LDA(xa, s, ho); SB(); }
            auto woff = [](int s) { return w2_slot(s); };
            bf16x8 wf[4][2];
#pragma unroll
            for (int s = 0; s < 3; ++s) { wf[s][0] = ldsfrag(wb + woff(s)); wf[s][1] = ldsfrag(wb + woff(s) + 1024); }
#pragma unroll
            for (int s = 0; s < 56; ++s) {
                if (s == 18) { BAR(); TS(tb + 16); }
                if (s == 35) { BAR(); TS(tb + 17); }
#pragma unroll
                for (int j = (s == 0 ? PD0 : a_issued(s - 1, 56)); j < a_issued(s, 56); ++j) LDA(xa, j, ho);
                if (s + 3 < 56) { wf[(s + 3) & 3][0] = ldsfrag(wb + woff(s + 3)); wf[(s + 3) & 3][1] = ldsfrag(wb + woff(s + 3) + 1024); }
                const bf16x8 x = asfrag(xa[s]);
                acc[0] = mfma32(wf[s & 3][0], x, acc[0]); acc[1] = mfma32(wf[s & 3][1], x, acc[1]);
                SGB(0x100, 1); SGB(0x8, 1); SGB(0x20, 2); SGB(0x100, 1); SGB(0x8, 1);
                SB();
            }
            const f32x16 snd = kh ? acc[0] : acc[1];
#pragma unroll
            for (int g4 = 0; g4 < 4; ++g4) *(LAS f32x4*)(lds + L_RED4 + ((w * 4 + g4) * 64 + lane) * 16) = f32x4{snd[4 * g4], snd[4 * g4 + 1], snd[4 * g4 + 2], snd[4 * g4 + 3]};
            TS(tb + 18);
            BAR();
            const f32x16 own = kh ? acc[1] : acc[0];
            const int pw = w ^ 2;
            float v[16];
#pragma unroll
            for (int g4 = 0; g4 < 4; ++g4) { f32x4 t = *(LAS f32x4*)(lds + L_RED4 + ((pw * 4 + g4) * 64 + lane) * 16);
#pragma unroll
                for (int i = 0; i < 4; ++i) v[4 * g4 + i] = own[4 * g4 + i] + t[i]; }
            const unsigned rr[8] = {r0.x, r0.y, r0.z, r0.w, r1.x, r1.y, r1.z, r1.w};
            unsigned c[8];
#pragma unroll
            for (int i = 0; i < 8; ++i) c[i] = pk(v[2 * i] + bflo(rr[i]), v[2 * i + 1] + bfhi(rr[i]));
            if (lyr == 0) {
                stw(rs, lane * 16, XF_OFF + 2 * MB + (T * 32 + 2 * u) * 1024, u32x4{c[0], c[1], c[2], c[3]});
                stw(rs, lane * 16 + 1024, XF_OFF + 2 * MB + (T * 32 + 2 * u) * 1024, u32x4{c[4], c[5], c[6], c[7]});
                stats_store(rs, 2, T, u, lane, c);
                DRAIN();
                BAR();
                if (tid == 0) set_flag(p, 5, g, r, tag);
            } else {
                auto s0 = __builtin_amdgcn_permlane32_swap(c[0], c[2], false, false);
                auto s1 = __builtin_amdgcn_permlane32_swap(c[1], c[3], false, false);
                auto s2 = __builtin_amdgcn_permlane32_swap(c[4], c[6], false, false);
                auto s3 = __builtin_amdgcn_permlane32_swap(c[5], c[7], false, false);
                uint16_t* orow = p.out + (size_t)(T * 32 + (lane & 31)) * DM + 32 * u + 8 * h;
                *(GAS u32x4*)(orow) = u32x4{s0[0], s1[0], s0[1], s1[1]};
                *(GAS u32x4*)(orow + 16) = u32x4{s2[0], s3[0], s2[1], s3[1]};
            }
            TS(tb + 19);
        }
    }
    asm volatile("s_waitcnt lgkmcnt(0)" ::: "memory");
    if (w == 0) TS_DUMP();
}

template <int PART> DI void pretile(const Params& p, LAS char* lds, unsigned lds0, rsrc_t rs, int hw, int g, int r, int lane) {
    const int h = lane >> 5, a = (lane & 15) >> 2, pp = lane & 3, ch = (lane >> 4) & 1;
    const int n = r >> 1, j = 2 * g + (r & 1);
    const unsigned scr0 = lds0 + L_SCR + hw * 8192;
    const LAS char* scr = lds + L_SCR + hw * 8192;
    if (hw == 0) {
        const char* base = (const char*)(p.wqkv + (size_t)32 * g * 1536 + 32 * r);
        if (PART == 0) {
#pragma unroll
            for (int i = 0; i < 6; ++i) { const int L = 64 * i + lane, k = L / 12, c = L % 12; glds16s<false>(base, (unsigned)(k * 1536 + (c >> 2) * 512 + (c & 3) * 8) * 2, scr0 + i * 1024); }
            return;
        }
        DRAIN();
        const LAS char* wb = scr + (4 * h + a) * 192 + ch * 32 + pp * 8;
#pragma unroll
        for (int s = 0; s < 2; ++s)
#pragma unroll
            for (int t = 0; t < 3; ++t) stw(rs, lane * 16, WQT_OFF + ((r * 32 + 2 * g + s) * 3 + t) * 1024, wfrag_tr(wb + s * 16 * 192 + t * 64, 8 * 192));
    } else if (hw == 1) {
        const char* base = (const char*)(p.w1 + (size_t)32 * g * FF + 112 * r);
        if (PART == 0) {
#pragma unroll
            for (int b = 0; b < 7; ++b) {
                const int L = 64 * b + lane, G = L / 56, x = L - G * 56;
                const int aa = x < 48 ? ((x >> 2) & 3) : ((x - 48) >> 1);
                const int c = x < 48 ? ((x >> 4) * 4 + (x & 3)) : (12 + (x & 1));
                glds16s<false>(base, (unsigned)((4 * G + aa) * FF + c * 8) * 2, scr0 + b * 1024);
            }
            return;
        }
        DRAIN();
        const LAS char* wbA = scr + h * 896 + a * 64 + ch * 32 + pp * 8;
        const LAS char* wbB = scr + h * 896 + 768 + a * 32 + pp * 8;
#pragma unroll
        for (int s = 0; s < 2; ++s) {
            const int dst = W1T_OFF + r * 114688 + (2 * g + s) * 3584;
#pragma unroll
            for (int t = 0; t < 3; ++t) stw(rs, lane * 16, dst + t * 1024, wfrag_tr(wbA + s * 3584 + t * 256, 1792));
            const u32x4 f3 = wfrag_tr(wbB + s * 3584, 1792);
            if (ch == 0) stw(rs, (h * 16 + (lane & 15)) * 16, dst + 3072, f3);
        }
    } else {
        const int rr = lane >> 3, slot = lane & 7, chunk = slot ^ (((rr >> 1) & 1) << 2);
        const unsigned vo = (unsigned)(rr * DM + chunk * 8) * 2;
        const int s0 = hw == 3 ? j : (j < 16 ? 4 * j : 64 + 3 * (j - 16)), ns = hw == 3 ? 1 : (j < 16 ? 4 : 3);
        const char* base = (const char*)((hw == 3 ? p.wo : p.w2) + (size_t)16 * s0 * DM + 64 * n);
        if (PART == 0) {
            for (int i = 0; i < 2 * ns; ++i) glds16s<false>(base + (size_t)i * 8 * DM * 2, vo, scr0 + i * 1024);
            return;
        }
        DRAIN();
        const int sw = ((a >> 1) & 1) << 6;
        const LAS char* wb0 = scr + (4 * h + a) * 128 + (0 ^ sw) + ch * 32 + pp * 8;
        const LAS char* wb1 = scr + (4 * h + a) * 128 + (64 ^ sw) + ch * 32 + pp * 8;
        for (int i = 0; i < ns; ++i) {
            const int s = s0 + i;
            const int kh = s >= 56 ? 1 : 0, sl = s - 56 * kh;
            const int dst = hw == 3 ? WOT_OFF + n * 65536 + s * 2048 : W2T_OFF + (n * 56 + sl) * 4096 + kh * 2048;
            stw(rs, lane * 16, dst, wfrag_tr(wb0 + i * 2048, 1024));
            stw(rs, lane * 16, dst + 1024, wfrag_tr(wb1 + i * 2048, 1024));
        }
    }
    DRAIN();
}

constexpr int w2_regsp(int j) { return j < 10 ? j : j < 30 ? j + 4 : j + 8; }
DI void helper_path(const Params& p, LAS char* lds, unsigned lds0, rsrc_t rs, const int w, const int g_, const int r_) {
    const int hw = w - 4;
    unsigned tag = 0;
    TS_DECL();
    {   const int layer = 0; PHASE_VARS();
        tag = read_tag(p); asm volatile("" :: "s"(tag) : "memory");
        pretile<0>(p, lds, lds0, rs, hw, g, r, lane);
        if (hw == 0) { pretile<1>(p, lds, lds0, rs, hw, g, r, lane); BAR(); }
        else { BAR(); pretile<1>(p, lds, lds0, rs, hw, g, r, lane); }
    }
    for (int layer = 0; layer < 2; ++layer) {
        {
            PHASE_VARS();
            HTS(layer * 20 + 0);
            BAR();
            if (lyr == 0) {
                if (w == 6 && lane == 0) set_flag(p, 10, g, r, tag);
                stage_lin(p, lds0, WQT_OFF + r * 98304, L_WQ, 96, hw, lane);
                float acc = 0.f;
                const LAS char* wbq = lds + L_WQ + (hw < 3 ? hw : 0) * 1024 + lane * 16;
                WAITV(18); BAR();
                acc = colsum_frags<8>(wbq, [](int i) { return (0 + i) * 3072; }, acc);
                WAITV(12); BAR();
                acc = colsum_frags<8>(wbq, [](int i) { return (8 + i) * 3072; }, acc);
                WAITV(6); BAR();
                acc = colsum_frags<8>(wbq, [](int i) { return (16 + i) * 3072; }, acc);
                DRAIN(); BAR();
                acc = colsum_frags<8>(wbq, [](int i) { return (24 + i) * 3072; }, acc);
                acc = sum_xor32(acc);
                if (h == 0 && hw < 3) ((LAS float*)(lds + L_CSQ))[hw * 32 + tok] = acc;
            }
            BAR();
            if (w == 7 && lane < 2) *(LAS u32x4*)(lds + L_ONES + lane * 1024) = u32x4{0x3f803f80u, 0x3f803f80u, 0x3f803f80u, 0x3f803f80u};
            if (lyr == 0) wait_word(p, flagp(p, 10, lane & 15, (lane & 16) ? (r ^ 1) : r), tag);
            HTS(layer * 20 + 2);
            stage_lin(p, lds0, WOT_OFF + n * 65536, L_WO, 40, hw, lane);
            BAR();
            if (w == 7) { if (g > 0) wait_word(p, flagp(p, 1 + 5 * lyr, (g >= 2 && (lane & 1)) ? g - 2 : g - 1, r), tag); }
            else {
                stage_lin(p, lds0, WOT_OFF + n * 65536 + 40 * KB, L_WO + 40 * KB, 24, hw, lane, 3);
                stage_lin(p, lds0, W1T_OFF + r * 114688, 56 * KB, 35, hw, lane, 3);
            }
            HTS(layer * 20 + 3);
            BAR();
            halo_load(lds, rs, w, g, r, lyr, lane);
            BAR();
        }
        {   PHASE_VARS();
            attn_unit(lds, rs, w, g, r, lyr, lane);
            HTS(layer * 20 + 4);
            DRAIN();
            HTS(layer * 20 + 5);
            BAR();
            stage_lin(p, lds0, W1T_OFF + r * 114688 + 35 * KB, 0, 39, hw, lane);
            HTS(layer * 20 + 6);
            DRAIN();
            HTS(layer * 20 + 7);
        }
        float cs_part = 0.f;
        u32x4 buf[48];
        {
            PHASE_VARS();
            BAR();
            const int wsrc = W2T_OFF + n * 56 * 4096 + hw * 1024;
#pragma unroll
            for (int j = 0; j < 24; ++j) { buf[j] = ldw(rs, lane * 16, wsrc + w2_regsp(j) * 4096); SB(); }
            if (lyr == 0) {
                const LAS char* wb = hw < 3 ? lds + hw * 1024 + lane * 16 : lds + 3072 + (h * 16 + (lane & 15)) * 16;
                cs_part = colsum_frags<11>(wb, [](int i) { return w1_off(i); }, cs_part);
                cs_part = colsum_frags<10>(wb, [](int i) { return w1_off(11 + i); }, cs_part);
            }
            BAR();
            BAR();
            stage_lin(p, lds0, W1T_OFF + r * 114688 + 75264, 94 * KB, 39, hw, lane);
            stage_lin(p, lds0, W2T_OFF + (n * 56 + 10) * 4096, 40 * KB, 16, hw, lane);
            stage_lin(p, lds0, W2T_OFF + (n * 56 + 34) * 4096, 136 * KB, 16, hw, lane);
        }
        {
            PHASE_VARS();
            HTS(layer * 20 + 8);
            BAR();
            const int wsrc = W2T_OFF + n * 56 * 4096 + hw * 1024;
#pragma unroll
            for (int j = 24; j < 36; ++j) { buf[j] = ldw(rs, lane * 16, wsrc + w2_regsp(j) * 4096); SB(); __builtin_amdgcn_s_sleep(2); }
            WAITV(12);
            HTS(layer * 20 + 10);
            BAR();
#pragma unroll
            for (int j = 36; j < 48; ++j) { buf[j] = ldw(rs, lane * 16, wsrc + w2_regsp(j) * 4096); SB(); __builtin_amdgcn_s_sleep(1); }
            if (lyr == 0) {
                LAS float* cs1 = (LAS float*)(lds + L_CS1);
                const LAS char* wb = hw < 3 ? lds + hw * 1024 + lane * 16 : lds + 3072 + (h * 16 + (lane & 15)) * 16;
                cs_part = colsum_frags<11>(wb, [](int i) { return w1_off(21 + i); }, cs_part);
                const float s = sum_xor32(cs_part);
                if (h == 0 && (hw < 3 || tok < 16)) cs1[hw * 32 + tok] = s;
            }
            BAR();
            HTS(layer * 20 + 11);
#pragma unroll
            for (int j = 0; j < 30; ++j) *(LAS u32x4*)(lds + w2_slot(w2_regsp(j)) + hw * 1024 + lane * 16) = buf[j];
            if (lyr == 0) {
#pragma unroll
                for (int j = 0; j < 24; ++j) { buf[j] = ldw(rs, lane * 16, WQT_OFF + r * 98304 + (4 * j + hw) * 1024); SB(); }
            }
            HTS(layer * 20 + 12);
            BAR();
        }
        {
            PHASE_VARS();
            BAR();
            BAR();
#pragma unroll
            for (int j = 30; j < 48; ++j) *(LAS u32x4*)(lds + w2_slot(w2_regsp(j)) + hw * 1024 + lane * 16) = buf[j];
            if (lyr == 1 && blockIdx.x == 0 && w == 7) {
                bool ok = false;
                for (unsigned spins = 0; spins < (1u << 16) && !ok; ++spins) {
                    bool mine = true;
#pragma unroll
                    for (int j = 0; j < 4; ++j) mine &= __hip_atomic_load((gu32*)(p.ws + ACK_OFF) + (lane + 64 * j) * 32, __ATOMIC_RELAXED, __HIP_MEMORY_SCOPE_AGENT) == tag;
                    ok = __all(mine);
                }
                if (lane == 0) __hip_atomic_store((gu32*)(p.ws + EPOCH_OFF), tag, __ATOMIC_RELAXED, __HIP_MEMORY_SCOPE_AGENT);
            }
            HTS(layer * 20 + 15);
            BAR();
            BAR();
            if (lyr == 0) {
#pragma unroll
                for (int j = 0; j < 24; ++j) *(LAS u32x4*)(lds + L_WQ + (4 * j + hw) * 1024 + lane * 16) = buf[j];
                HTS(17);
                BAR();
            }
        }
    }
    asm volatile("s_waitcnt lgkmcnt(0)" ::: "memory");
    if (w == 4) HTS_DUMP();
}

__global__ __launch_bounds__(512, 2) void k_mega(Params p) {
    extern __shared__ __attribute__((aligned(16))) char smem_[];
    LAS char* lds = (LAS char*)smem_;
    const unsigned lds0 = (unsigned)(uintptr_t)smem_;
    const int b = blockIdx.x, idx = b >> 3;
    const int g_ = ((b & 7) >> 1) * 4 + (idx >> 3), r_ = (b & 1) * 8 + (idx & 7);
    const int w = __builtin_amdgcn_readfirstlane(threadIdx.x >> 6);
    const rsrc_t rs = __builtin_amdgcn_make_buffer_rsrc((void*)p.ws, 0, WS_USED, 0x00020000);
    if (w < 4) main_path(p, lds, rs, w, g_, r_);
    else helper_path(p, lds, lds0, rs, w, g_, r_);
}
}

extern "C" void kernel_launch(void* const* d_in, const int* in_sizes, int n_in, void* d_out, int out_size, void* d_ws, size_t ws_size, hipStream_t stream) {
    Params p;
    p.x = (const uint16_t*)d_in[0]; p.wqkv = (const uint16_t*)d_in[1]; p.wo = (const uint16_t*)d_in[2];
    p.w1 = (const uint16_t*)d_in[4]; p.w2 = (const uint16_t*)d_in[5];
    p.out = (uint16_t*)d_out; p.ws = (char*)d_ws;
    (void)hipFuncSetAttribute((const void*)k_mega, hipFuncAttributeMaxDynamicSharedMemorySize, LDS_BYTES);
    k_mega<<<256, 512, LDS_BYTES, stream>>>(p);
}
```

```cpp
#include <hip/hip_runtime.h>
#include <hip/hip_bf16.h>
#include <stdint.h>

#define GAS __attribute__((address_space(1)))
#define LAS __attribute__((address_space(3)))
#define DI __device__ __forceinline__

typedef __bf16 bf16x8 __attribute__((ext_vector_type(8)));
typedef __bf16 bf16x2 __attribute__((ext_vector_type(2)));
typedef short s16x4 __attribute__((ext_vector_type(4)));
typedef float f32x16 __attribute__((ext_vector_type(16)));
typedef float f32x4 __attribute__((ext_vector_type(4)));
typedef float f32x2 __attribute__((ext_vector_type(2)));
typedef unsigned u32x4 __attribute__((ext_vector_type(4)));
typedef unsigned u32x2 __attribute__((ext_vector_type(2)));
typedef GAS unsigned gu32;

#define TS_DECL()
#define TS_PARAM
#define TS_ARG(tb)
#define TS(i)
#define TS_DUMP()
#define HTS(i)
#define HTS_DUMP()
#define MAPMODE 1
namespace {
constexpr int DM = 512, FF = 1792;
constexpr float EPS = 1e-5f;
constexpr float LOG2E = 1.4426950408889634f;
constexpr float QSCALE = 0.25f * LOG2E;

constexpr int MB = 1 << 20, KB = 1024;
constexpr int XF_OFF = 0;
constexpr int O_OFF = 4 * MB;
constexpr int X2_OFF = 8 * MB;
constexpr int H_OFF = 12 * MB;
constexpr int KV_OFF = 28 * MB;
constexpr int WQT_OFF = 36 * MB;
constexpr int WOT_OFF = WQT_OFF + 1536 * KB;
constexpr int W1T_OFF = 38 * MB;
constexpr int W2T_OFF = 40 * MB;
constexpr int ST_OFF = 42 * MB;
constexpr int ST_BUF = 262144;
constexpr int KN_OFF = 43 * MB;
constexpr int FLAG_OFF = 43 * MB + 65536;
constexpr int FLAG_BYTES = 11 * 256 * 128;
constexpr int TMO_OFF = FLAG_OFF + FLAG_BYTES;
constexpr int EPOCH_OFF = TMO_OFF + 256;
constexpr int ACK_OFF = EPOCH_OFF + 256;
constexpr int WS_USED = 44 * MB;

constexpr int L_WQ = 0;
constexpr int L_Q = 0, L_KV = 8 * KB, L_ONES = 91 * KB;
constexpr int L_RED2 = 40 * KB;
constexpr int wo_off(int s) { return s < 31 ? 96 * KB + s * 2048 : 94 * KB; }
constexpr int L_RED4 = 96 * KB;
constexpr int L_SCR = 100 * KB;
constexpr int L_TAB = 158 * KB, L_CSQ = L_TAB, L_CS1 = L_TAB + 512, L_STT = L_TAB + 1024;
constexpr int L_KN = L_TAB + 384, L_QN = L_TAB + 384 + 96;
constexpr int LDS_BYTES = 160 * KB;
constexpr int w1_off(int s) { return s < 10 ? 56 * KB + s * 3584 : s < 21 ? (s - 10) * 3584 : 94 * KB + (s - 21) * 3584; }
constexpr int w2_slot(int sp) { return (sp < 38 ? sp : sp - 38) * 4096; }
constexpr int AUX_SC1 = 16;

struct Params {
    const uint16_t* x; const uint16_t* wqkv; const uint16_t* wo; const uint16_t* w1; const uint16_t* w2;
    uint16_t* out; char* ws;
};

DI int lane_id() { return (int)__builtin_amdgcn_mbcnt_hi(~0u, __builtin_amdgcn_mbcnt_lo(~0u, 0u)); }
DI f32x16 mfma32(bf16x8 a, bf16x8 b, f32x16 c) { return __builtin_amdgcn_mfma_f32_32x32x16_bf16(a, b, c, 0, 0, 0); }
DI unsigned pk(float a, float b) { bf16x2 v = {(__bf16)a, (__bf16)b}; return __builtin_bit_cast(unsigned, v); }
DI float bflo(unsigned u) { return __uint_as_float(u << 16); }
DI float bfhi(unsigned u) { return __uint_as_float(u & 0xffff0000u); }
DI bf16x8 asfrag(u32x4 v) { return __builtin_bit_cast(bf16x8, v); }
DI s16x4 trd(const LAS char* p) { return __builtin_amdgcn_ds_read_tr16_b64_v4i16((LAS s16x4*)p); }
DI u32x4 wfrag_tr(const LAS char* p, int off2) {
    const u32x2 lo = __builtin_bit_cast(u32x2, trd(p)), hi = __builtin_bit_cast(u32x2, trd(p + off2));
    return u32x4{lo.x, lo.y, hi.x, hi.y};
}
DI bf16x8 ldsfrag(const LAS char* p) { return *(const LAS bf16x8*)p; }
template <int N, class F> DI float colsum_frags(const LAS char* p, F off, float acc) {
    u32x4 f[N];
#pragma unroll
    for (int i = 0; i < N; ++i) f[i] = *(const LAS u32x4*)(p + off(i));
    float a1 = 0.f;
#pragma unroll
    for (int i = 0; i < N; ++i) {
        unsigned w0 = f[i].x, w1 = f[i].y, w2 = f[i].z, w3 = f[i].w;
        asm("v_dot2c_f32_bf16 %0, %2, %6\n\tv_dot2c_f32_bf16 %1, %3, %6\n\tv_dot2c_f32_bf16 %0, %4, %6\n\tv_dot2c_f32_bf16 %1, %5, %6"
            : "+v"(acc), "+v"(a1) : "v"(w0), "v"(w1), "v"(w2), "v"(w3), "v"(0x3f803f80u));
    }
    asm volatile("s_nop 2" : "+v"(acc), "+v"(a1));
    return acc + a1;
}
DI float sum_xor32(float v) { auto r = __builtin_amdgcn_permlane32_swap(__float_as_uint(v), __float_as_uint(v), false, false); return __uint_as_float(r[0]) + __uint_as_float(r[1]); }
DI float max_xor32(float v) { auto r = __builtin_amdgcn_permlane32_swap(__float_as_uint(v), __float_as_uint(v), false, false); return fmaxf(__uint_as_float(r[0]), __uint_as_float(r[1])); }
template <bool SC1> DI void glds16s(const void* sbase, unsigned voff, unsigned lds_addr) {
    unsigned keep;
    if (SC1) asm volatile("s_mov_b32 %0, m0\n\ts_mov_b32 m0, %3\n\ts_nop 0\n\tglobal_load_lds_dwordx4 %1, %2 sc1\n\ts_mov_b32 m0, %0" : "=&s"(keep) : "v"(voff), "s"(sbase), "s"(lds_addr) : "memory");
    else asm volatile("s_mov_b32 %0, m0\n\ts_mov_b32 m0, %3\n\ts_nop 0\n\tglobal_load_lds_dwordx4 %1, %2\n\ts_mov_b32 m0, %0" : "=&s"(keep) : "v"(voff), "s"(sbase), "s"(lds_addr) : "memory");
}
#define BAR() asm volatile("s_waitcnt lgkmcnt(0)\n\ts_barrier" ::: "memory")
#define DRAIN() asm volatile("s_waitcnt vmcnt(0)" ::: "memory")
#define WAITV(n) asm volatile("s_waitcnt vmcnt(" #n ")" ::: "memory")
#define SB() __builtin_amdgcn_sched_barrier(0)
#define SGB(mask, n) __builtin_amdgcn_sched_group_barrier(mask, n, 0)

typedef __amdgpu_buffer_rsrc_t rsrc_t;
DI u32x4 ldw(rsrc_t rs, int voff, int soff) { return __builtin_amdgcn_raw_buffer_load_b128(rs, voff, soff, AUX_SC1); }
DI void stw(rsrc_t rs, int voff, int soff, u32x4 v) { __builtin_amdgcn_raw_buffer_store_b128(v, rs, voff + soff, 0, AUX_SC1); }
DI f32x2 ldst(rsrc_t rs, int voff, int soff) { return __builtin_bit_cast(f32x2, __builtin_amdgcn_raw_buffer_load_b64(rs, voff, soff, AUX_SC1)); }
DI void stst(rsrc_t rs, int voff, int soff, f32x2 v) { __builtin_amdgcn_raw_buffer_store_b64(__builtin_bit_cast(u32x2, v), rs, voff, soff, AUX_SC1); }

DI gu32* flagp(const Params& p, int ph, int g, int r) { return (gu32*)(p.ws + FLAG_OFF) + ((ph * 16 + g) * 16 + r) * 32; }
DI void set_flag(const Params& p, int ph, int g, int r, unsigned tag) { __hip_atomic_store(flagp(p, ph, g, r), tag, __ATOMIC_RELAXED, __HIP_MEMORY_SCOPE_AGENT); }
DI void wait_word(const Params& p, gu32* a, unsigned tag) {
    for (unsigned spins = 0;; ++spins) {
        const unsigned v = __hip_atomic_load(a, __ATOMIC_RELAXED, __HIP_MEMORY_SCOPE_AGENT);
        if (__all(v == tag)) break;
        if (spins > (1u << 20)) { __hip_atomic_store((gu32*)(p.ws + TMO_OFF), 1u, __ATOMIC_RELAXED, __HIP_MEMORY_SCOPE_AGENT); break; }
        __builtin_amdgcn_s_sleep(1);
    }
    __builtin_amdgcn_fence(__ATOMIC_ACQUIRE, "wavefront");
}
DI void wait_group(const Params& p, int ph, int g, unsigned tag, int lane) { wait_word(p, flagp(p, ph, g, lane & 15), tag); }

DI void stats_store(rsrc_t rs, int buf, int T, int u, int lane, const unsigned (&c)[8]) {
    float s = 0.f, ss = 0.f;
#pragma unroll
    for (int i = 0; i < 8; ++i) { float lo = bflo(c[i]), hi = bfhi(c[i]); s += lo + hi; ss += lo * lo + hi * hi; }
    s = sum_xor32(s); ss = sum_xor32(ss);
    if (lane < 32) stst(rs, lane * 8, ST_OFF + buf * ST_BUF + (T * 16 + u) * 256, f32x2{s, ss});
}

DI void stage_lin(const char* wsb, unsigned lds0, int src, int dst, int n, int hw, int lane, int nw = 4) {
    const unsigned vo = lane * 16;
    for (int i = hw; i < n; i += nw) glds16s<true>(wsb + src + i * 1024, vo, lds0 + dst + i * 1024);
}

#define HELPER_VARS() \
    int hw = w - 4; asm volatile("" : "+s"(hw)); \
    const char* wsb = p.ws; asm volatile("" : "+s"(wsb)); \
    unsigned lds0 = lds0_; asm volatile("" : "+s"(lds0)); (void)hw; (void)wsb; (void)lds0;
#define PHASE_VARS() \
    int lane = lane_id(); asm volatile("" : "+v"(lane)); \
    int lyr = layer; asm volatile("" : "+s"(lyr)); \
    int g = g_, r = r_; asm volatile("" : "+s"(g), "+s"(r)); \
    const int h = lane >> 5, tok = lane & 31; const int m = r & 1, n = r >> 1; (void)h; (void)tok; (void)m; (void)n; (void)lyr;

DI void attn_finish(rsrc_t rs, const f32x16& o, int hd, int T, int r, int lyr, int lane) {
    float ov[8]; float l;
    if (hd == 0) { l = o[8];
#pragma unroll
        for (int i = 0; i < 8; ++i) ov[i] = o[i];
    } else { l = o[0];
#pragma unroll
        for (int i = 0; i < 8; ++i) ov[i] = o[8 + i];
    }
    const float inv = __builtin_amdgcn_rcpf(l);
    stw(rs, lane * 16, O_OFF + lyr * 2 * MB + (T * 32 + 2 * r + hd) * 1024,
        u32x4{pk(ov[0] * inv, ov[1] * inv), pk(ov[2] * inv, ov[3] * inv), pk(ov[4] * inv, ov[5] * inv), pk(ov[6] * inv, ov[7] * inv)});
}
DI float max16(float m, const f32x16& s) {
#pragma unroll
    for (int i = 0; i < 16; i += 2) m = fmaxf(fmaxf(m, s[i]), s[i + 1]);
    return m;
}
DI void max16x2(float& m0, float& m1, const f32x16& s) {
#pragma unroll
    for (int i = 0; i < 16; i += 4) { m0 = fmaxf(fmaxf(m0, s[i]), s[i + 1]); m1 = fmaxf(fmaxf(m1, s[i + 2]), s[i + 3]); }
}
DI void exp_pack(const f32x16& s, u32x4& p0, u32x4& p1) {
    unsigned pw[8];
#pragma unroll
    for (int i = 0; i < 8; ++i) pw[i] = pk(__builtin_amdgcn_exp2f(s[2 * i]), __builtin_amdgcn_exp2f(s[2 * i + 1]));
    p0 = u32x4{pw[0], pw[1], pw[2], pw[3]}; p1 = u32x4{pw[4], pw[5], pw[6], pw[7]};
}
DI float wave32_max(float v) {
    int x = __float_as_int(v);
    x = max(x, __builtin_amdgcn_update_dpp(x, x, 0x111, 0xf, 0xf, false));
    x = max(x, __builtin_amdgcn_update_dpp(x, x, 0x112, 0xf, 0xf, false));
    x = max(x, __builtin_amdgcn_update_dpp(x, x, 0x114, 0xf, 0xf, false));
    x = max(x, __builtin_amdgcn_update_dpp(x, x, 0x118, 0xf, 0xf, false));
    x = max(x, __builtin_amdgcn_update_dpp(x, x, 0x142, 0xa, 0xf, false));
    return __int_as_float(__builtin_amdgcn_readlane(x, 31));
}
DI void attn_seg(unsigned ka, unsigned va, int ks, int vs, int n, const bf16x8 qf, const f32x16 c0, f32x16& o) {
    int d;
    asm volatile(
        "ds_read_b128 v[248:251], %[ka]\n\tv_add_u32 %[ka], %[ks], %[ka]\n\tds_read_b128 v[236:239], %[ka]\n\tv_add_u32 %[ka], %[ks], %[ka]\n\t"
        "ds_read_b128 v[240:243], %[va]\n\tds_read_b128 v[244:247], %[va] offset:1024\n\tv_add_u32 %[va], %[vs], %[va]\n\t"
        "s_lshr_b32 %[d], %[n], 1\n\ts_waitcnt lgkmcnt(3)\n\t"
        "v_mfma_f32_32x32x16_bf16 v[204:219], v[248:251], %[q], %[c0]\n\t"
        "s_cmp_eq_u32 %[d], 0\n\ts_nop 10\n\ts_cbranch_scc1 Ltail_%=\n"
        "Lloop_%=:\n\t"
        "v_exp_f32 v204, v204\n\t"
        "v_exp_f32 v205, v205\n\t"
        "v_exp_f32 v206, v206\n\t"
        "v_exp_f32 v207, v207\n\t"
        "v_exp_f32 v208, v208\n\t"
        "v_exp_f32 v209, v209\n\t"
        "v_exp_f32 v210, v210\n\t"
        "v_exp_f32 v211, v211\n\t"
        "v_cvt_pk_bf16_f32 v204, v204, v205\n\t"
        "v_cvt_pk_bf16_f32 v205, v206, v207\n\t"
        "v_cvt_pk_bf16_f32 v206, v208, v209\n\t"
        "v_cvt_pk_bf16_f32 v207, v210, v211\n\t"
        "s_waitcnt lgkmcnt(1)\n\ts_nop 0\n\t"
        "v_mfma_f32_32x32x16_bf16 %[o], v[240:243], v[204:207], %[o]\n\t"
        "v_exp_f32 v212, v212\n\t"
        "v_exp_f32 v213, v213\n\t"
        "v_exp_f32 v214, v214\n\t"
        "v_exp_f32 v215, v215\n\t"
        "v_mfma_f32_32x32x16_bf16 v[220:235], v[236:239], %[q], 0\n\t"
        "ds_read_b128 v[236:239], %[ka]\n\tds_read_b128 v[248:251], %[va]\n\tds_read_b128 v[252:255], %[va] offset:1024\n\t"
        "v_add_u32 %[ka], %[ks], %[ka]\n\tv_add_u32 %[va], %[vs], %[va]\n\t"
        "v_exp_f32 v216, v216\n\t"
        "v_exp_f32 v217, v217\n\t"
        "v_exp_f32 v218, v218\n\t"
        "v_exp_f32 v219, v219\n\t"
        "v_cvt_pk_bf16_f32 v212, v212, v213\n\t"
        "v_cvt_pk_bf16_f32 v213, v214, v215\n\t"
        "v_cvt_pk_bf16_f32 v214, v216, v217\n\t"
        "v_cvt_pk_bf16_f32 v215, v218, v219\n\t"
        "s_waitcnt lgkmcnt(3)\n\ts_nop 0\n\t"
        "v_mfma_f32_32x32x16_bf16 %[o], v[244:247], v[212:215], %[o]\n\t"
        "v_exp_f32 v220, v220\n\t"
        "v_exp_f32 v221, v221\n\t"
        "v_exp_f32 v222, v222\n\t"
        "v_exp_f32 v223, v223\n\t"
        "v_exp_f32 v224, v224\n\t"
        "v_exp_f32 v225, v225\n\t"
        "v_exp_f32 v226, v226\n\t"
        "v_exp_f32 v227, v227\n\t"
        "v_cvt_pk_bf16_f32 v220, v220, v221\n\t"
        "v_cvt_pk_bf16_f32 v221, v222, v223\n\t"
        "v_cvt_pk_bf16_f32 v222, v224, v225\n\t"
        "v_cvt_pk_bf16_f32 v223, v226, v227\n\t"
        "s_waitcnt lgkmcnt(1)\n\ts_nop 0\n\t"
        "v_mfma_f32_32x32x16_bf16 %[o], v[248:251], v[220:223], %[o]\n\t"
        "v_exp_f32 v228, v228\n\t"
        "v_exp_f32 v229, v229\n\t"
        "v_exp_f32 v230, v230\n\t"
        "v_exp_f32 v231, v231\n\t"
        "v_mfma_f32_32x32x16_bf16 v[204:219], v[236:239], %[q], 0\n\t"
        "ds_read_b128 v[236:239], %[ka]\n\tds_read_b128 v[240:243], %[va]\n\tds_read_b128 v[244:247], %[va] offset:1024\n\t"
        "v_add_u32 %[ka], %[ks], %[ka]\n\tv_add_u32 %[va], %[vs], %[va]\n\t"
        "v_exp_f32 v232, v232\n\t"
        "v_exp_f32 v233, v233\n\t"
        "v_exp_f32 v234, v234\n\t"
        "v_exp_f32 v235, v235\n\t"
        "v_cvt_pk_bf16_f32 v228, v228, v229\n\t"
        "v_cvt_pk_bf16_f32 v229, v230, v231\n\t"
        "v_cvt_pk_bf16_f32 v230, v232, v233\n\t"
        "v_cvt_pk_bf16_f32 v231, v234, v235\n\t"
        "s_waitcnt lgkmcnt(3)\n\ts_nop 0\n\t"
        "v_mfma_f32_32x32x16_bf16 %[o], v[252:255], v[228:231], %[o]\n\t"
        "s_sub_u32 %[d], %[d], 1\n\ts_cmp_lg_u32 %[d], 0\n\ts_cbranch_scc1 Lloop_%=\n"
        "Ltail_%=:\n\t"
        "s_bitcmp1_b32 %[n], 0\n\ts_waitcnt lgkmcnt(0)\n\ts_cbranch_scc0 Lend_%=\n\t"
        "v_exp_f32 v204, v204\n\t"
        "v_exp_f32 v205, v205\n\t"
        "v_exp_f32 v206, v206\n\t"
        "v_exp_f32 v207, v207\n\t"
        "v_exp_f32 v208, v208\n\t"
        "v_exp_f32 v209, v209\n\t"
        "v_exp_f32 v210, v210\n\t"
        "v_exp_f32 v211, v211\n\t"
        "v_cvt_pk_bf16_f32 v204, v204, v205\n\t"
        "v_cvt_pk_bf16_f32 v205, v206, v207\n\t"
        "v_cvt_pk_bf16_f32 v206, v208, v209\n\t"
        "v_cvt_pk_bf16_f32 v207, v210, v211\n\t"
        "s_nop 1\n\t"
        "v_mfma_f32_32x32x16_bf16 %[o], v[240:243], v[204:207], %[o]\n\t"
        "v_exp_f32 v212, v212\n\t"
        "v_exp_f32 v213, v213\n\t"
        "v_exp_f32 v214, v214\n\t"
        "v_exp_f32 v215, v215\n\t"
        "v_exp_f32 v216, v216\n\t"
        "v_exp_f32 v217, v217\n\t"
        "v_exp_f32 v218, v218\n\t"
        "v_exp_f32 v219, v219\n\t"
        "v_cvt_pk_bf16_f32 v212, v212, v213\n\t"
        "v_cvt_pk_bf16_f32 v213, v214, v215\n\t"
        "v_cvt_pk_bf16_f32 v214, v216, v217\n\t"
        "v_cvt_pk_bf16_f32 v215, v218, v219\n\t"
        "s_nop 1\n\t"
        "v_mfma_f32_32x32x16_bf16 %[o], v[244:247], v[212:215], %[o]\n"
        "Lend_%=:\n\ts_nop 11"
        : [o] "+v"(o), [ka] "+v"(ka), [va] "+v"(va), [d] "=&s"(d)
        : [q] "v"(qf), [c0] "v"(c0), [ks] "s"(ks), [vs] "v"(vs), [n] "s"(n)
        : "memory", "scc", "v204", "v205", "v206", "v207", "v208", "v209", "v210", "v211", "v212", "v213", "v214", "v215", "v216", "v217", "v218", "v219", "v220", "v221", "v222", "v223", "v224", "v225", "v226", "v227", "v228", "v229", "v230", "v231", "v232", "v233", "v234", "v235", "v236", "v237", "v238", "v239", "v240", "v241", "v242", "v243", "v244", "v245", "v246", "v247", "v248", "v249", "v250", "v251", "v252", "v253", "v254", "v255");
}
constexpr float BOUND2 = 4096.f;
DI f32x16 attn_exact(LAS char* lds, int tw, int hd, int s0, bool first_masked, const bf16x8 qf, int lane) {
    const int h = lane >> 5, tok = lane & 31;
    const LAS char* kb = lds + L_KV + hd * 1024 + lane * 16;
    const bool isdata = ((lane >> 4) & 1) == hd;
    const LAS char* vb = isdata ? (lds + L_KV + 2048 + lane * 16) : (lds + L_ONES);
    const int vstep = isdata ? 4096 : 0;
    const f32x16 zero = {};
    f32x16 o = {};
    float mx = -INFINITY;
    for (int pass = 0; pass < 2; ++pass) {
        for (int sl = s0; sl <= 8 + tw; ++sl) {
            f32x16 s = mfma32(*(const LAS bf16x8*)(kb + sl * 4096), qf, zero);
            const bool diag = sl == 8 + tw, first = first_masked && sl == s0;
#pragma unroll
            for (int i = 0; i < 16; ++i) { const int kk = (i & 3) + 8 * (i >> 2) + 4 * h; if ((diag && kk > tok) || (first && kk <= tok)) s[i] = -INFINITY; }
            if (pass == 0) mx = max16(mx, s);
            else {
#pragma unroll
                for (int i = 0; i < 16; ++i) s[i] -= mx;
                u32x4 p0, p1; exp_pack(s, p0, p1);
                o = mfma32(*(const LAS bf16x8*)(vb + sl * vstep), asfrag(p0), o);
                o = mfma32(*(const LAS bf16x8*)(vb + sl * vstep + 1024), asfrag(p1), o);
            }
        }
        if (pass == 0) mx = max_xor32(mx);
    }
    return o;
}
DI void attn_phase(const Params& p, LAS char* lds, rsrc_t rs, int w, int g, int r, int lyr, int lane, unsigned tag TS_PARAM) {
    const int h = lane >> 5, tok = lane & 31;
    const int hd = w >> 2, tw = hd ? 3 - (w & 3) : (w & 3), T = 4 * g + tw;
    const bf16x8 qf = *(const LAS bf16x8*)(lds + L_Q + tw * 2048 + hd * 1024 + lane * 16);
    const unsigned kb = (unsigned)(uintptr_t)(lds + L_KV + hd * 1024 + lane * 16);
    const bool isdata = ((lane >> 4) & 1) == hd;
    const unsigned vb = (unsigned)(uintptr_t)(isdata ? (lds + L_KV + 2048 + lane * 16) : (lds + L_ONES));
    const int vstep = isdata ? 4096 : 0;
    const LAS float* kn = (const LAS float*)(lds + L_KN);
    const float qn = ((const LAS float*)(lds + L_QN))[tw * 2 + hd] * 1.05f;
    const float kown = fmaxf(fmaxf(kn[16 + hd], kn[18 + hd]), fmaxf(kn[20 + hd], kn[22 + hd]));
    bool fast = __builtin_amdgcn_readfirstlane(qn * kown <= BOUND2);
    f32x16 t, o = {};
#pragma unroll
    for (int i = 0; i < 16; ++i) t[i] = __builtin_fmaf((float)(tok - 4 * h), 1e30f, (0.5f - (float)((i & 3) + 8 * (i >> 2))) * 1e30f);
    const int s4 = 4 * hd, tile0 = 4 * g - 8 + s4;
    const bool loader = tw == 0 && tile0 >= 0;
    u32x4 hv[16]; f32x2 hn;
    if (loader) {
        wait_word(p, flagp(p, 1 + 5 * lyr, g - 2 + hd, r), tag);
        TS(tb + 1);
        const int kvo = KV_OFF + lyr * 4 * MB + (tile0 * 16 + r) * 4096;
#pragma unroll
        for (int i = 0; i < 16; ++i) hv[i] = ldw(rs, lane * 16 + (i & 3) * 1024, kvo + (i >> 2) * 65536);
        hn = ldst(rs, (lane & 3) * 128, KN_OFF + ((lyr * 64 + tile0) * 16 + r) * 8);
        SB();
    }
    if (fast) {
        f32x16 m;
#pragma unroll
        for (int i = 0; i < 16; ++i) m[i] = fminf(t[i], 0.f);
        attn_seg(kb + (8 + tw) * 4096, vb + (8 + tw) * vstep, -4096, -vstep, tw + 1, qf, m, o);
    }
    TS(tb);
    if (loader) {
        LAS char* kvl = lds + L_KV + s4 * 4096 + lane * 16;
#pragma unroll
        for (int i = 0; i < 16; ++i) *(LAS u32x4*)(kvl + i * 1024) = hv[i];
        if (lane < 4) *(LAS f32x2*)(lds + L_KN + (s4 + lane) * 8) = hn;
    }
    BAR();
    TS(tb + 2);
    const int s0 = T >= 8 ? tw : 8 - 4 * g;
    if (s0 < 8) {
        float kh = 0.f;
#pragma unroll
        for (int j = 0; j < 8; ++j) kh = fmaxf(kh, j >= 8 - 4 * g ? kn[2 * j + hd] : 0.f);
        fast = fast && __builtin_amdgcn_readfirstlane(qn * kh <= BOUND2);
        if (fast) {
            f32x16 m;
#pragma unroll
            for (int i = 0; i < 16; ++i) m[i] = T >= 8 ? fminf(-t[i], 0.f) : 0.f;
            attn_seg(kb + s0 * 4096, vb + s0 * vstep, 4096, vstep, 8 - s0, qf, m, o);
        }
    }
    if (!fast) o = attn_exact(lds, tw, hd, s0, T >= 8, qf, lane);
    attn_finish(rs, o, hd, T, r, lyr, lane);
}
DI void ln_stats(const f32x2 (&sv)[8], float& mean, float& rstd) {
    float S = 0.f, SS = 0.f;
#pragma unroll
    for (int i = 0; i < 8; ++i) { S += sv[i].x; SS += sv[i].y; }
    S = sum_xor32(S); SS = sum_xor32(SS);
    mean = S * (1.f / DM); const float var = fmaxf(SS * (1.f / DM) - mean * mean, 0.f); rstd = __builtin_amdgcn_rsqf(var + EPS);
}
DI unsigned read_tag(const Params& p) {
    return (unsigned)__builtin_amdgcn_readfirstlane(__hip_atomic_load((gu32*)(p.ws + EPOCH_OFF), __ATOMIC_RELAXED, __HIP_MEMORY_SCOPE_AGENT)) + 1u;
}
#define LDA(arr, s, base) arr[s] = ldw(rs, lane * 16 + ((s) & 3) * 1024, (base) + ((s) >> 2) * 4096)

#define WODELAY 36
#define PD0 4
#define PDV 12
constexpr int a_issued(int s, int total) { int v = PD0 + 2 * (s + 1); const int cap = s + 1 + PDV; v = v < cap ? v : cap; return v < total ? v : total; }

DI void main_path(const Params& p, LAS char* lds, rsrc_t rs, const int w, const int g_, const int r_) {
    unsigned tag = 0;
    TS_DECL();
    TS(0);
    {
        const int layer = 0; PHASE_VARS();
        const int T = 4 * g + (r & 3), u = (r >> 2) * 4 + w;
        const uint16_t* row = p.x + (size_t)(T * 32 + tok) * DM + 32 * u + 4 * h;
        u32x2 a0 = *(const GAS u32x2*)(row), a1 = *(const GAS u32x2*)(row + 8), a2 = *(const GAS u32x2*)(row + 16), a3 = *(const GAS u32x2*)(row + 24);
        tag = read_tag(p); asm volatile("" :: "s"(tag) : "memory");
        stw(rs, lane * 16, XF_OFF + (T * 32 + 2 * u) * 1024, u32x4{a0.x, a0.y, a1.x, a1.y});
        stw(rs, lane * 16, XF_OFF + (T * 32 + 2 * u + 1) * 1024, u32x4{a2.x, a2.y, a3.x, a3.y});
        const unsigned c[8] = {a0.x, a0.y, a1.x, a1.y, a2.x, a2.y, a3.x, a3.y};
        stats_store(rs, 0, T, u, lane, c);
        DRAIN();
        BAR();
        if (w == 0 && lane == 0) { set_flag(p, 0, g, r, tag); __hip_atomic_store((gu32*)(p.ws + ACK_OFF) + blockIdx.x * 32, tag, __ATOMIC_RELAXED, __HIP_MEMORY_SCOPE_AGENT); }
    }
    TS(1);
    for (int layer = 0; layer < 2; ++layer) {
        const int tb = 2 + layer * 24; (void)tb;
        {
            PHASE_VARS();
            if (w == 0) {
                if (lyr == 0) {
                    const int j = lane & 15;
                    wait_word(p, (lane & 16) ? flagp(p, 0, j, r) : flagp(p, 0, g, j), tag);
                } else wait_group(p, 5, g, tag, lane);
            }
            BAR();
            TS(tb + 0);
            const int T = 4 * g + w;
            const LAS char* wb = lds + L_WQ + lane * 16;
            LAS float* csq = (LAS float*)(lds + L_CSQ);
            LAS float* stt = (LAS float*)(lds + L_STT);
            f32x16 aq = {}, ak = {}, av = {};
            u32x4 xa[32];
            f32x2 sv[8];
            const int sto = ST_OFF + 2 * lyr * ST_BUF + T * 4096, xo = XF_OFF + lyr * 2 * MB + T * 32768;
#pragma unroll
            for (int s = 0; s < PD0; ++s) { LDA(xa, s, xo); SB(); }
            if (lyr == 0) BAR();
            bf16x8 wf[3][3];
#pragma unroll
            for (int s = 0; s < 2; ++s)
#pragma unroll
                for (int t = 0; t < 3; ++t) wf[s][t] = ldsfrag(wb + (s * 3 + t) * 1024);
#pragma unroll
            for (int s = 0; s < 32; ++s) {
                if ((s == 6 || s == 14 || s == 22) && lyr == 0) BAR();
#pragma unroll
                for (int j = (s == 0 ? PD0 : a_issued(s - 1, 32)); j < a_issued(s, 32); ++j) LDA(xa, j, xo);
                if (s >= 16 && s < 24) sv[s - 16] = ldst(rs, (8 * h * 32 + tok) * 8 + (s - 16) * 256, sto);
                if (s + 2 < 32) {
#pragma unroll
                    for (int t = 0; t < 3; ++t) wf[(s + 2) % 3][t] = ldsfrag(wb + ((s + 2) * 3 + t) * 1024);
                }
                const bf16x8 x = asfrag(xa[s]);
                aq = mfma32(wf[s % 3][0], x, aq); ak = mfma32(wf[s % 3][1], x, ak); av = mfma32(x, wf[s % 3][2], av);
                SGB(0x100, 1); SGB(0x8, 1); SGB(0x20, 1); SGB(0x100, 1); SGB(0x8, 1); SGB(0x20, 1); SGB(0x100, 1); SGB(0x8, 1);
                SB();
            }
            float mean, rstd; ln_stats(sv, mean, rstd);
            if (h == 0) { stt[w * 64 + tok] = rstd; stt[w * 64 + 32 + tok] = mean * rstd; }
            TS(tb + 1);
            BAR();
            TS(tb + 2);
            const float mr = mean * rstd;
            float q[16], k[16], v[16];
            const float cv = csq[64 + tok];
#pragma unroll
            for (int g4 = 0; g4 < 4; ++g4) {
                f32x4 cq = *(LAS f32x4*)(csq + 8 * g4 + 4 * h), ck = *(LAS f32x4*)(csq + 32 + 8 * g4 + 4 * h);
                f32x4 rsv = *(LAS f32x4*)(stt + w * 64 + 8 * g4 + 4 * h), ms = *(LAS f32x4*)(stt + w * 64 + 32 + 8 * g4 + 4 * h);
#pragma unroll
                for (int i = 0; i < 4; ++i) {
                    k[4 * g4 + i] = ak[4 * g4 + i] * rstd - mr * ck[i];
                    v[4 * g4 + i] = av[4 * g4 + i] * rsv[i] - ms[i] * cv;
                    q[4 * g4 + i] = (aq[4 * g4 + i] * rstd - mr * cq[i]) * QSCALE;
                }
            }
            const u32x4 k0 = {pk(k[0], k[1]), pk(k[2], k[3]), pk(k[4], k[5]), pk(k[6], k[7])};
            const u32x4 k1 = {pk(k[8], k[9]), pk(k[10], k[11]), pk(k[12], k[13]), pk(k[14], k[15])};
            const u32x4 v0 = {pk(v[0], v[1]), pk(v[2], v[3]), pk(v[4], v[5]), pk(v[6], v[7])};
            const u32x4 v1 = {pk(v[8], v[9]), pk(v[10], v[11]), pk(v[12], v[13]), pk(v[14], v[15])};
            const int kvo = KV_OFF + lyr * 4 * MB + (T * 16 + r) * 4096;
            stw(rs, lane * 16, kvo, k0); stw(rs, lane * 16 + 1024, kvo, k1); stw(rs, lane * 16 + 2048, kvo, v0); stw(rs, lane * 16 + 3072, kvo, v1);
            {
                float na = 0.f, nb = 0.f;
#pragma unroll
                for (int i = 0; i < 8; ++i) { na = __builtin_fmaf(k[i], k[i], na); nb = __builtin_fmaf(k[8 + i], k[8 + i], nb); }
                const f32x2 nn = {wave32_max(sum_xor32(na)), wave32_max(sum_xor32(nb))};
                if (lane == 0) { stst(rs, 0, KN_OFF + ((lyr * 64 + T) * 16 + r) * 8, nn); *(LAS f32x2*)(lds + L_KN + (8 + w) * 8) = nn; }
            }
            LAS char* kvl = lds + L_KV + (8 + w) * 4096 + lane * 16;
            *(LAS u32x4*)(kvl) = k0; *(LAS u32x4*)(kvl + 1024) = k1; *(LAS u32x4*)(kvl + 2048) = v0; *(LAS u32x4*)(kvl + 3072) = v1;
            LAS char* ql = lds + L_Q + w * 2048 + lane * 16;
            *(LAS u32x4*)(ql) = u32x4{pk(q[0], q[1]), pk(q[2], q[3]), pk(q[4], q[5]), pk(q[6], q[7])};
            *(LAS u32x4*)(ql + 1024) = u32x4{pk(q[8], q[9]), pk(q[10], q[11]), pk(q[12], q[13]), pk(q[14], q[15])};
            {
                float na = 0.f, nb = 0.f;
#pragma unroll
                for (int i = 0; i < 8; ++i) { na = __builtin_fmaf(q[i], q[i], na); nb = __builtin_fmaf(q[8 + i], q[8 + i], nb); }
                const f32x2 nn = {wave32_max(sum_xor32(na)), wave32_max(sum_xor32(nb))};
                if (lane == 0) *(LAS f32x2*)(lds + L_QN + w * 8) = nn;
            }
            DRAIN();
            BAR();
            if (w == 0 && lane == 0) set_flag(p, 1 + 5 * lyr, g, r, tag);
            TS(tb + 3);
        }
        {
            PHASE_VARS();
            attn_phase(p, lds, rs, w, g, r, lyr, lane, tag TS_ARG(layer == 0 ? 22 : 48));
            TS(tb + 4); TS(tb + 5); TS(tb + 6);
            DRAIN();
            BAR();
            if (w == 0 && lane == 0) set_flag(p, 2 + 5 * lyr, g, r, tag);
            TS(tb + 7);
        }
        {
            PHASE_VARS();
            const int mt = w & 1, kh = (w >> 1) & 1, T = 4 * g + 2 * m + mt, u = 2 * n + kh;
            const int xo = XF_OFF + lyr * 2 * MB + (T * 32 + 2 * u) * 1024;
            const u32x4 r0 = ldw(rs, lane * 16, xo), r1 = ldw(rs, lane * 16 + 1024, xo);
            if (w == 0) wait_group(p, 2 + 5 * lyr, g, tag, lane);
            BAR();
            TS(tb + 8);
            f32x16 acc[2] = {};
            u32x4 xa[16];
            const int oo = O_OFF + lyr * 2 * MB + (T * 32 + 16 * kh) * 1024;
#pragma unroll
            for (int s = 0; s < PD0; ++s) { LDA(xa, s, oo); SB(); }
            const LAS char* wb = lds + 96 * KB + kh * 32768 + lane * 16;
            const LAS char* wlast = lds + (kh ? 94 * KB : 96 * KB + 15 * 2048) + lane * 16;
            bf16x8 wf[4][2];
#pragma unroll
            for (int s = 0; s < 3; ++s) { wf[s][0] = ldsfrag(wb + s * 2048); wf[s][1] = ldsfrag(wb + s * 2048 + 1024); }
#pragma unroll
            for (int s = 0; s < 16; ++s) {
#pragma unroll
                for (int j = (s == 0 ? PD0 : a_issued(s - 1, 16)); j < a_issued(s, 16); ++j) LDA(xa, j, oo);
                if (s + 3 < 15) { wf[(s + 3) & 3][0] = ldsfrag(wb + (s + 3) * 2048); wf[(s + 3) & 3][1] = ldsfrag(wb + (s + 3) * 2048 + 1024); }
                if (s + 3 == 15) { wf[3][0] = ldsfrag(wlast); wf[3][1] = ldsfrag(wlast + 1024); }
                const bf16x8 x = asfrag(xa[s]);
                acc[0] = mfma32(wf[s & 3][0], x, acc[0]); acc[1] = mfma32(wf[s & 3][1], x, acc[1]);
                SGB(0x100, 1); SGB(0x8, 1); SGB(0x20, 2); SGB(0x100, 1); SGB(0x8, 1);
                SB();
            }
            const f32x16 snd = kh ? acc[0] : acc[1];
#pragma unroll
            for (int g4 = 0; g4 < 4; ++g4) *(LAS f32x4*)(lds + L_RED2 + ((w * 4 + g4) * 64 + lane) * 16) = f32x4{snd[4 * g4], snd[4 * g4 + 1], snd[4 * g4 + 2], snd[4 * g4 + 3]};
            TS(tb + 9);
            BAR();
            const f32x16 own = kh ? acc[1] : acc[0];
            const int pw = w ^ 2;
            float v[16];
#pragma unroll
            for (int g4 = 0; g4 < 4; ++g4) { f32x4 t = *(LAS f32x4*)(lds + L_RED2 + ((pw * 4 + g4) * 64 + lane) * 16);
#pragma unroll
                for (int i = 0; i < 4; ++i) v[4 * g4 + i] = own[4 * g4 + i] + t[i]; }
            const unsigned rr[8] = {r0.x, r0.y, r0.z, r0.w, r1.x, r1.y, r1.z, r1.w};
            unsigned c[8];
#pragma unroll
            for (int i = 0; i < 8; ++i) c[i] = pk(v[2 * i] + bflo(rr[i]), v[2 * i + 1] + bfhi(rr[i]));
            const int yo = X2_OFF + lyr * 2 * MB + (T * 32 + 2 * u) * 1024;
            stw(rs, lane * 16, yo, u32x4{c[0], c[1], c[2], c[3]});
            stw(rs, lane * 16 + 1024, yo, u32x4{c[4], c[5], c[6], c[7]});
            stats_store(rs, 2 * lyr + 1, T, u, lane, c);
            DRAIN();
            BAR();
            if (w == 0 && lane == 0) set_flag(p, 3 + 5 * lyr, g, r, tag);
            TS(tb + 10);
        }
        {
            PHASE_VARS();
            if (w == 0) wait_group(p, 3 + 5 * lyr, g, tag, lane);
            BAR();
            TS(tb + 11);
            const int T = 4 * g + w;
            const LAS char* wbA = lds + lane * 16;
            const LAS char* wbB = lds + 3072 + (h * 16 + (lane & 15)) * 16;
            LAS float* cs1 = (LAS float*)(lds + L_CS1);
            f32x16 acc[4] = {};
            u32x4 xa[32];
            f32x2 sv[8];
            const int sto = ST_OFF + (2 * lyr + 1) * ST_BUF + T * 4096, xo = X2_OFF + lyr * 2 * MB + T * 32768;
#pragma unroll
            for (int s = 0; s < PD0; ++s) { LDA(xa, s, xo); SB(); }
            auto ldW = [&](int s, int t) { return t < 3 ? ldsfrag(wbA + w1_off(s) + t * 1024) : ldsfrag(wbB + w1_off(s)); };
            bf16x8 wf[2][4];
#pragma unroll
            for (int t = 0; t < 4; ++t) wf[0][t] = ldW(0, t);
#pragma unroll
            for (int s = 0; s < 32; ++s) {
                if (s == 20) BAR();
#pragma unroll
                for (int j = (s == 0 ? PD0 : a_issued(s - 1, 32)); j < a_issued(s, 32); ++j) LDA(xa, j, xo);
                if (s >= 16 && s < 24) sv[s - 16] = ldst(rs, (8 * h * 32 + tok) * 8 + (s - 16) * 256, sto);
                const bf16x8 x = asfrag(xa[s]);
#pragma unroll
                for (int t = 0; t < 4; ++t) {
                    if (s + 1 < 32) wf[(s + 1) & 1][t] = ldW(s + 1, t);
                    acc[t] = mfma32(wf[s & 1][t], x, acc[t]);
                }
                SGB(0x100, 1); SGB(0x8, 1); SGB(0x20, 1); SGB(0x100, 1); SGB(0x8, 1); SGB(0x20, 1); SGB(0x100, 1); SGB(0x8, 1); SGB(0x100, 1); SGB(0x8, 1);
                SB();
            }
            float mean, rstd; ln_stats(sv, mean, rstd);
            const float mr = mean * rstd;
            TS(tb + 12);
            BAR();
            TS(tb + 13);
            const int ho = H_OFF + lyr * 8 * MB + (T * 112 + 7 * r) * 1024;
#pragma unroll
            for (int t = 0; t < 4; ++t) {
#pragma unroll
                for (int sp = 0; sp < (t < 3 ? 2 : 1); ++sp) {
                    float hv[8];
#pragma unroll
                    for (int g2 = 0; g2 < 2; ++g2) {
                        const f32x4 c4 = *(LAS f32x4*)(cs1 + t * 32 + 16 * sp + 8 * g2 + 4 * h);
#pragma unroll
                        for (int i = 0; i < 4; ++i) {
                            const float a = acc[t][8 * sp + 4 * g2 + i];
                            const float z = a * rstd - mr * c4[i];
                            hv[4 * g2 + i] = z * __builtin_amdgcn_rcpf(1.f + __builtin_amdgcn_exp2f(-LOG2E * z));
                        }
                    }
                    stw(rs, lane * 16 + (2 * t + sp) * 1024, ho, u32x4{pk(hv[0], hv[1]), pk(hv[2], hv[3]), pk(hv[4], hv[5]), pk(hv[6], hv[7])});
                }
            }
            DRAIN();
            BAR();
            if (w == 0 && lane == 0) set_flag(p, 4 + 5 * lyr, g, r, tag);
            TS(tb + 14);
        }
        {
            PHASE_VARS();
            const int mt = w & 1, kh = (w >> 1) & 1, T = 4 * g + 2 * m + mt, u = 2 * n + kh;
            const int yo = X2_OFF + lyr * 2 * MB + (T * 32 + 2 * u) * 1024;
            const u32x4 r0 = ldw(rs, lane * 16, yo), r1 = ldw(rs, lane * 16 + 1024, yo);
            if (w == 0) wait_group(p, 4 + 5 * lyr, g, tag, lane);
            BAR();
            TS(tb + 15);
            const LAS char* wb = lds + kh * 2048 + lane * 16;
            f32x16 acc[2] = {};
            const int ho = H_OFF + lyr * 8 * MB + (T * 112 + 56 * kh) * 1024;
            u32x4 xa[56];
#pragma unroll
            for (int s = 0; s < PD0; ++s) { LDA(xa, s, ho); SB(); }
            auto woff = [](int s) { return w2_slot(s); };
            bf16x8 wf[4][2];
#pragma unroll
            for (int s = 0; s < 3; ++s) { wf[s][0] = ldsfrag(wb + woff(s)); wf[s][1] = ldsfrag(wb + woff(s) + 1024); }
#pragma unroll
            for (int s = 0; s < 56; ++s) {
                if (s == 18) { BAR(); TS(tb + 16); }
                if (s == 35) { BAR(); TS(tb + 17); }
#pragma unroll
                for (int j = (s == 0 ? PD0 : a_issued(s - 1, 56)); j < a_issued(s, 56); ++j) LDA(xa, j, ho);
                if (s + 3 < 56) { wf[(s + 3) & 3][0] = ldsfrag(wb + woff(s + 3)); wf[(s + 3) & 3][1] = ldsfrag(wb + woff(s + 3) + 1024); }
                const bf16x8 x = asfrag(xa[s]);
                acc[0] = mfma32(wf[s & 3][0], x, acc[0]); acc[1] = mfma32(wf[s & 3][1], x, acc[1]);
                SGB(0x100, 1); SGB(0x8, 1); SGB(0x20, 2); SGB(0x100, 1); SGB(0x8, 1);
                SB();
            }
            const f32x16 snd = kh ? acc[0] : acc[1];
#pragma unroll
            for (int g4 = 0; g4 < 4; ++g4) *(LAS f32x4*)(lds + L_RED4 + ((w * 4 + g4) * 64 + lane) * 16) = f32x4{snd[4 * g4], snd[4 * g4 + 1], snd[4 * g4 + 2], snd[4 * g4 + 3]};
            TS(tb + 18);
            BAR();
            const f32x16 own = kh ? acc[1] : acc[0];
            const int pw = w ^ 2;
            float v[16];
#pragma unroll
            for (int g4 = 0; g4 < 4; ++g4) { f32x4 t = *(LAS f32x4*)(lds + L_RED4 + ((pw * 4 + g4) * 64 + lane) * 16);
#pragma unroll
                for (int i = 0; i < 4; ++i) v[4 * g4 + i] = own[4 * g4 + i] + t[i]; }
            const unsigned rr[8] = {r0.x, r0.y, r0.z, r0.w, r1.x, r1.y, r1.z, r1.w};
            unsigned c[8];
#pragma unroll
            for (int i = 0; i < 8; ++i) c[i] = pk(v[2 * i] + bflo(rr[i]), v[2 * i + 1] + bfhi(rr[i]));
            if (lyr == 0) {
                stw(rs, lane * 16, XF_OFF + 2 * MB + (T * 32 + 2 * u) * 1024, u32x4{c[0], c[1], c[2], c[3]});
                stw(rs, lane * 16 + 1024, XF_OFF + 2 * MB + (T * 32 + 2 * u) * 1024, u32x4{c[4], c[5], c[6], c[7]});
                stats_store(rs, 2, T, u, lane, c);
                DRAIN();
                BAR();
                if (w == 0 && lane == 0) set_flag(p, 5, g, r, tag);
            } else {
                auto s0 = __builtin_amdgcn_permlane32_swap(c[0], c[2], false, false);
                auto s1 = __builtin_amdgcn_permlane32_swap(c[1], c[3], false, false);
                auto s2 = __builtin_amdgcn_permlane32_swap(c[4], c[6], false, false);
                auto s3 = __builtin_amdgcn_permlane32_swap(c[5], c[7], false, false);
                uint16_t* orow = p.out + (size_t)(T * 32 + (lane & 31)) * DM + 32 * u + 8 * h;
                *(GAS u32x4*)(orow) = u32x4{s0[0], s1[0], s0[1], s1[1]};
                *(GAS u32x4*)(orow + 16) = u32x4{s2[0], s3[0], s2[1], s3[1]};
            }
            TS(tb + 19);
        }
    }
    asm volatile("s_waitcnt lgkmcnt(0)" ::: "memory");
    if (w == 0) TS_DUMP();
}

template <int PART> DI void pretile(const Params& p, LAS char* lds, unsigned lds0, rsrc_t rs, int hw, int g, int r, int lane) {
    const int h = lane >> 5, a = (lane & 15) >> 2, pp = lane & 3, ch = (lane >> 4) & 1;
    const int n = r >> 1, j = 2 * g + (r & 1);
    const unsigned scr0 = lds0 + L_SCR + hw * 8192;
    const LAS char* scr = lds + L_SCR + hw * 8192;
    if (hw == 0) {
        const char* base = (const char*)(p.wqkv + (size_t)32 * g * 1536 + 32 * r);
        if (PART == 0) {
#pragma unroll
            for (int i = 0; i < 6; ++i) { const int L = 64 * i + lane, k = L / 12, c = L % 12; glds16s<false>(base, (unsigned)(k * 1536 + (c >> 2) * 512 + (c & 3) * 8) * 2, scr0 + i * 1024); }
            return;
        }
        DRAIN();
        const LAS char* wb = scr + (4 * h + a) * 192 + ch * 32 + pp * 8;
#pragma unroll
        for (int s = 0; s < 2; ++s)
#pragma unroll
            for (int t = 0; t < 3; ++t) stw(rs, lane * 16, WQT_OFF + ((r * 32 + 2 * g + s) * 3 + t) * 1024, wfrag_tr(wb + s * 16 * 192 + t * 64, 8 * 192));
    } else if (hw == 1) {
        const char* base = (const char*)(p.w1 + (size_t)32 * g * FF + 112 * r);
        if (PART == 0) {
#pragma unroll
            for (int b = 0; b < 7; ++b) {
                const int L = 64 * b + lane, G = L / 56, x = L - G * 56;
                const int aa = x < 48 ? ((x >> 2) & 3) : ((x - 48) >> 1);
                const int c = x < 48 ? ((x >> 4) * 4 + (x & 3)) : (12 + (x & 1));
                glds16s<false>(base, (unsigned)((4 * G + aa) * FF + c * 8) * 2, scr0 + b * 1024);
            }
            return;
        }
        DRAIN();
        const LAS char* wbA = scr + h * 896 + a * 64 + ch * 32 + pp * 8;
        const LAS char* wbB = scr + h * 896 + 768 + a * 32 + pp * 8;
#pragma unroll
        for (int s = 0; s < 2; ++s) {
            const int dst = W1T_OFF + r * 114688 + (2 * g + s) * 3584;
#pragma unroll
            for (int t = 0; t < 3; ++t) stw(rs, lane * 16, dst + t * 1024, wfrag_tr(wbA + s * 3584 + t * 256, 1792));
            const u32x4 f3 = wfrag_tr(wbB + s * 3584, 1792);
            if (ch == 0) stw(rs, (h * 16 + (lane & 15)) * 16, dst + 3072, f3);
        }
    } else {
        const int rr = lane >> 3, slot = lane & 7, chunk = slot ^ (((rr >> 1) & 1) << 2);
        const unsigned vo = (unsigned)(rr * DM + chunk * 8) * 2;
        const int s0 = hw == 3 ? j : (j < 16 ? 4 * j : 64 + 3 * (j - 16)), ns = hw == 3 ? 1 : (j < 16 ? 4 : 3);
        const char* base = (const char*)((hw == 3 ? p.wo : p.w2) + (size_t)16 * s0 * DM + 64 * n);
        if (PART == 0) {
            for (int i = 0; i < 2 * ns; ++i) glds16s<false>(base + (size_t)i * 8 * DM * 2, vo, scr0 + i * 1024);
            return;
        }
        DRAIN();
        const int sw = ((a >> 1) & 1) << 6;
        const LAS char* wb0 = scr + (4 * h + a) * 128 + (0 ^ sw) + ch * 32 + pp * 8;
        const LAS char* wb1 = scr + (4 * h + a) * 128 + (64 ^ sw) + ch * 32 + pp * 8;
        for (int i = 0; i < ns; ++i) {
            const int s = s0 + i;
            const int kh = s >= 56 ? 1 : 0, sl = s - 56 * kh;
            const int dst = hw == 3 ? WOT_OFF + n * 65536 + s * 2048 : W2T_OFF + (n * 56 + sl) * 4096 + kh * 2048;
            stw(rs, lane * 16, dst, wfrag_tr(wb0 + i * 2048, 1024));
            stw(rs, lane * 16, dst + 1024, wfrag_tr(wb1 + i * 2048, 1024));
        }
    }
    DRAIN();
}

constexpr int w2_regsp(int j) { return j < 10 ? j : j < 30 ? j + 4 : j + 8; }
DI void helper_path(const Params& p, LAS char* lds, unsigned lds0_, rsrc_t rs, const int w, const int g_, const int r_) {
    unsigned tag = 0;
    TS_DECL();
    {   const int layer = 0; PHASE_VARS(); HELPER_VARS();
        tag = read_tag(p); asm volatile("" :: "s"(tag) : "memory");
        if (hw == 3) {
            unsigned keep;
            const unsigned vo = (unsigned)(lane < 44 ? lane : 43) << 20;
            asm volatile("s_mov_b32 %0, m0\n\ts_mov_b32 m0, %3\n\ts_nop 0\n\tglobal_load_lds_dword %1, %2\n\ts_mov_b32 m0, %0" : "=&s"(keep) : "v"(vo), "s"(p.ws), "s"(lds0 + 140 * KB) : "memory");
            const unsigned vo2 = (unsigned)(lane & 1) << 20;
            asm volatile("s_mov_b32 %0, m0\n\ts_mov_b32 m0, %3\n\ts_nop 0\n\tglobal_load_lds_dword %1, %2\n\ts_mov_b32 m0, %0" : "=&s"(keep) : "v"(vo2), "s"(p.out), "s"(lds0 + 141 * KB) : "memory");
        }
        pretile<0>(p, lds, lds0, rs, hw, g, r, lane);
        if (hw == 0) { pretile<1>(p, lds, lds0, rs, hw, g, r, lane); BAR(); }
        else { BAR(); pretile<1>(p, lds, lds0, rs, hw, g, r, lane); }
    }
    for (int layer = 0; layer < 2; ++layer) {
        {
            PHASE_VARS(); HELPER_VARS();
            HTS(layer * 20 + 0);
            BAR();
            if (lyr == 0) {
                if (w == 6 && lane == 0) set_flag(p, 10, g, r, tag);
                stage_lin(wsb, lds0, WQT_OFF + r * 98304, L_WQ, 96, hw, lane);
                float acc = 0.f;
                const LAS char* wbq = lds + L_WQ + (hw < 3 ? hw : 0) * 1024 + lane * 16;
                WAITV(18); BAR();
                acc = colsum_frags<8>(wbq, [](int i) { return (0 + i) * 3072; }, acc);
                WAITV(12); BAR();
                acc = colsum_frags<8>(wbq, [](int i) { return (8 + i) * 3072; }, acc);
                WAITV(6); BAR();
                acc = colsum_frags<8>(wbq, [](int i) { return (16 + i) * 3072; }, acc);
                DRAIN(); BAR();
                acc = colsum_frags<8>(wbq, [](int i) { return (24 + i) * 3072; }, acc);
                acc = sum_xor32(acc);
                if (h == 0 && hw < 3) ((LAS float*)(lds + L_CSQ))[hw * 32 + tok] = acc;
                wait_word(p, flagp(p, 10, lane & 15, (lane & 16) ? (r ^ 1) : r), tag);
            } else {
                __builtin_amdgcn_s_sleep(WODELAY);
                for (int i = hw; i < 62; i += 4) glds16s<true>(wsb + WOT_OFF + n * 65536 + i * 1024, lane * 16, lds0 + 96 * KB + i * 1024);
            }
            BAR();
            if (w == 7 && lane < 2) *(LAS u32x4*)(lds + L_ONES + lane * 1024) = u32x4{0x3f803f80u, 0x3f803f80u, 0x3f803f80u, 0x3f803f80u};
            HTS(layer * 20 + 2);
            stage_lin(wsb, lds0, WOT_OFF + n * 65536 + 62 * KB, 94 * KB, 2, hw, lane);
            stage_lin(wsb, lds0, W1T_OFF + r * 114688, 56 * KB, 35, hw, lane);
            HTS(layer * 20 + 3);
            BAR();
            if (lyr == 0) stage_lin(wsb, lds0, WOT_OFF + n * 65536, 96 * KB, 62, hw, lane);
        }
        u32x4 buf[48];
        {   PHASE_VARS(); HELPER_VARS();

            attn_phase(p, lds, rs, w, g, r, lyr, lane, tag TS_ARG(layer == 0 ? 40 : 44));
            HTS(layer * 20 + 4);
            DRAIN();
            HTS(layer * 20 + 5);
            BAR();
            stage_lin(wsb, lds0, W1T_OFF + r * 114688 + 35 * KB, 0, 39, hw, lane);
            HTS(layer * 20 + 6);
        }
        {
            PHASE_VARS(); HELPER_VARS();
            const int wsrc = W2T_OFF + n * 56 * 4096 + hw * 1024;
            BAR();
#pragma unroll
            for (int j = 0; j < 12; ++j) { buf[j] = ldw(rs, lane * 16, wsrc + w2_regsp(j) * 4096); SB(); }
            BAR();
#pragma unroll
            for (int j = 12; j < 22; ++j) { buf[j] = ldw(rs, lane * 16, wsrc + w2_regsp(j) * 4096); SB(); }
            BAR();
            stage_lin(wsb, lds0, W1T_OFF + r * 114688 + 75264, 94 * KB, 39, hw, lane);
        }
        {
            PHASE_VARS(); HELPER_VARS();
            HTS(layer * 20 + 8);
            WAITV(31);
            BAR();
            const int wsrc = W2T_OFF + n * 56 * 4096 + hw * 1024;
            stage_lin(wsb, lds0, W2T_OFF + (n * 56 + 10) * 4096, 40 * KB, 16, hw, lane);
            stage_lin(wsb, lds0, W2T_OFF + (n * 56 + 34) * 4096, 136 * KB, 16, hw, lane);
#pragma unroll
            for (int j = 22; j < 34; ++j) { buf[j] = ldw(rs, lane * 16, wsrc + w2_regsp(j) * 4096); SB(); }
            WAITV(20);
            HTS(layer * 20 + 10);
            BAR();
#pragma unroll
            for (int j = 34; j < 48; ++j) { buf[j] = ldw(rs, lane * 16, wsrc + w2_regsp(j) * 4096); SB(); }
            if (lyr == 0) {
                LAS float* cs1 = (LAS float*)(lds + L_CS1);
                const LAS char* wb = hw < 3 ? lds + hw * 1024 + lane * 16 : lds + 3072 + (h * 16 + (lane & 15)) * 16;
                float cs_part = colsum_frags<11>(wb, [](int i) { return w1_off(i); }, 0.f);
                cs_part = colsum_frags<11>(wb, [](int i) { return w1_off(11 + i); }, cs_part);
                cs_part = colsum_frags<10>(wb, [](int i) { return w1_off(22 + i); }, cs_part);
                const float s = sum_xor32(cs_part);
                if (h == 0 && (hw < 3 || tok < 16)) cs1[hw * 32 + tok] = s;
            }
            WAITV(14);
            BAR();
            HTS(layer * 20 + 11);
#pragma unroll
            for (int j = 0; j < 30; ++j) *(LAS u32x4*)(lds + w2_slot(w2_regsp(j)) + hw * 1024 + lane * 16) = buf[j];
            if (lyr == 0) {
#pragma unroll
                for (int j = 0; j < 24; ++j) { buf[j] = ldw(rs, lane * 16, WQT_OFF + r * 98304 + (4 * j + hw) * 1024); SB(); }
            }
            HTS(layer * 20 + 12);
            BAR();
        }
        {
            PHASE_VARS(); HELPER_VARS();
            BAR();
            BAR();
#pragma unroll
            for (int j = 30; j < 48; ++j) *(LAS u32x4*)(lds + w2_slot(w2_regsp(j)) + hw * 1024 + lane * 16) = buf[j];
            if (lyr == 1 && blockIdx.x == 0 && w == 7) {
                bool ok = false;
                for (unsigned spins = 0; spins < (1u << 16) && !ok; ++spins) {
                    bool mine = true;
#pragma unroll
                    for (int j = 0; j < 4; ++j) mine &= __hip_atomic_load((gu32*)(p.ws + ACK_OFF) + (lane + 64 * j) * 32, __ATOMIC_RELAXED, __HIP_MEMORY_SCOPE_AGENT) == tag;
                    ok = __all(mine);
                }
                if (lane == 0) __hip_atomic_store((gu32*)(p.ws + EPOCH_OFF), tag, __ATOMIC_RELAXED, __HIP_MEMORY_SCOPE_AGENT);
            }
            HTS(layer * 20 + 15);
            BAR();
            BAR();
            if (lyr == 0) {
#pragma unroll
                for (int j = 0; j < 24; ++j) *(LAS u32x4*)(lds + L_WQ + (4 * j + hw) * 1024 + lane * 16) = buf[j];
                HTS(17);
                BAR();
            }
        }
    }
    asm volatile("s_waitcnt lgkmcnt(0)" ::: "memory");
    if (w == 4) HTS_DUMP();
}

__global__ __launch_bounds__(512, 2) void k_mega(Params p) {
    extern __shared__ __attribute__((aligned(16))) char smem_[];
    LAS char* lds = (LAS char*)smem_;
    const unsigned lds0 = (unsigned)(uintptr_t)smem_;
    const int b = blockIdx.x, idx = b >> 3;
    const int g_ = ((b & 7) >> 1) * 4 + (idx >> 3), r_ = (b & 1) * 8 + (idx & 7);
    const int w = __builtin_amdgcn_readfirstlane(threadIdx.x >> 6);
    const rsrc_t rs = __builtin_amdgcn_make_buffer_rsrc((void*)p.ws, 0, WS_USED, 0x00020000);
    if (w < 4) main_path(p, lds, rs, w, g_, r_);
    else helper_path(p, lds, lds0, rs, w, g_, r_);
}
}

extern "C" void kernel_launch(void* const* d_in, const int* in_sizes, int n_in, void* d_out, int out_size, void* d_ws, size_t ws_size, hipStream_t stream) {
    Params p;
    p.x = (const uint16_t*)d_in[0]; p.wqkv = (const uint16_t*)d_in[1]; p.wo = (const uint16_t*)d_in[2];
    p.w1 = (const uint16_t*)d_in[4]; p.w2 = (const uint16_t*)d_in[5];
    p.out = (uint16_t*)d_out; p.ws = (char*)d_ws;
    (void)hipFuncSetAttribute((const void*)k_mega, hipFuncAttributeMaxDynamicSharedMemorySize, LDS_BYTES);
    k_mega<<<256, 512, LDS_BYTES, stream>>>(p);
}
```

```cpp
#include <hip/hip_runtime.h>
#include <hip/hip_bf16.h>
#include <stdint.h>

#define GAS __attribute__((address_space(1)))
#define LAS __attribute__((address_space(3)))
#define DI __device__ __forceinline__

typedef __bf16 bf16x8 __attribute__((ext_vector_type(8)));
typedef __bf16 bf16x2 __attribute__((ext_vector_type(2)));
typedef short s16x4 __attribute__((ext_vector_type(4)));
typedef float f32x16 __attribute__((ext_vector_type(16)));
typedef float f32x4 __attribute__((ext_vector_type(4)));
typedef float f32x2 __attribute__((ext_vector_type(2)));
typedef unsigned u32x4 __attribute__((ext_vector_type(4)));
typedef unsigned u32x2 __attribute__((ext_vector_type(2)));
typedef GAS unsigned gu32;

#define TS_DECL()
#define TS_PARAM
#define TS_ARG(tb)
#define TS(i)
#define TS_DUMP()
#define HTS(i)
#define HTS_DUMP()
#define WNT_STR ""
#define WAUX 16
#define MAPMODE 1
namespace {
constexpr int DM = 512, FF = 1792;
constexpr float EPS = 1e-5f;
constexpr float LOG2E = 1.4426950408889634f;
constexpr float QSCALE = 0.25f * LOG2E;

constexpr int MB = 1 << 20, KB = 1024;
constexpr int XF_OFF = 0;
constexpr int O_OFF = 4 * MB;
constexpr int X2_OFF = 8 * MB;
constexpr int H_OFF = 12 * MB;
constexpr int KV_OFF = 28 * MB;
constexpr int WQT_OFF = 36 * MB;
constexpr int WOT_OFF = WQT_OFF + 1536 * KB;
constexpr int W1T_OFF = 38 * MB;
constexpr int W2T_OFF = 40 * MB;
constexpr int ST_OFF = 42 * MB;
constexpr int ST_BUF = 262144;
constexpr int KN_OFF = 43 * MB;
constexpr int FLAG_OFF = 43 * MB + 65536;
constexpr int FLAG_BYTES = 11 * 256 * 128;
constexpr int TMO_OFF = FLAG_OFF + FLAG_BYTES;
constexpr int EPOCH_OFF = TMO_OFF + 256;
constexpr int ACK_OFF = EPOCH_OFF + 256;
constexpr int WS_USED = 44 * MB;

constexpr int L_WQ = 0;
constexpr int L_Q = 96 * KB, L_KV = 104 * KB, L_ONES = 152 * KB;
constexpr int L_WO = 0, L_RED2 = 142 * KB;
constexpr int L_RED4 = 96 * KB;
constexpr int L_SCR = 100 * KB;
constexpr int L_TAB = 158 * KB, L_CSQ = L_TAB, L_CS1 = L_TAB + 512, L_STT = L_TAB + 1024;
constexpr int L_KN = L_TAB + 384, L_QN = L_TAB + 384 + 96;
constexpr int LDS_BYTES = 160 * KB;
constexpr int w1_off(int s) { return s < 9 ? 64 * KB + s * 3584 : s < 22 ? 96 * KB + (s - 9) * 3584 : (s - 22) * 3584; }
constexpr int w2_slot(int sp) { return (sp < 38 ? sp : sp - 38) * 4096; }
constexpr int AUX_SC1 = 16;

struct Params {
    const uint16_t* x; const uint16_t* wqkv; const uint16_t* wo; const uint16_t* w1; const uint16_t* w2;
    uint16_t* out; char* ws;
};

DI int lane_id() { int l; asm volatile("v_mbcnt_lo_u32_b32 %0, -1, 0\n\tv_mbcnt_hi_u32_b32 %0, -1, %0" : "=v"(l)); return l; }
DI f32x16 mfma32(bf16x8 a, bf16x8 b, f32x16 c) { return __builtin_amdgcn_mfma_f32_32x32x16_bf16(a, b, c, 0, 0, 0); }
DI unsigned pk(float a, float b) { bf16x2 v = {(__bf16)a, (__bf16)b}; return __builtin_bit_cast(unsigned, v); }
DI float bflo(unsigned u) { return __uint_as_float(u << 16); }
DI float bfhi(unsigned u) { return __uint_as_float(u & 0xffff0000u); }
DI bf16x8 asfrag(u32x4 v) { return __builtin_bit_cast(bf16x8, v); }
DI s16x4 trd(const LAS char* p) { return __builtin_amdgcn_ds_read_tr16_b64_v4i16((LAS s16x4*)p); }
DI u32x4 wfrag_tr(const LAS char* p, int off2) {
    const u32x2 lo = __builtin_bit_cast(u32x2, trd(p)), hi = __builtin_bit_cast(u32x2, trd(p + off2));
    return u32x4{lo.x, lo.y, hi.x, hi.y};
}
DI bf16x8 ldsfrag(const LAS char* p) { return *(const LAS bf16x8*)p; }
template <int N, class F> DI float colsum_frags(const LAS char* p, F off, float acc) {
    u32x4 f[N];
#pragma unroll
    for (int i = 0; i < N; ++i) f[i] = *(const LAS u32x4*)(p + off(i));
    float a1 = 0.f;
#pragma unroll
    for (int i = 0; i < N; ++i) {
        unsigned w0 = f[i].x, w1 = f[i].y, w2 = f[i].z, w3 = f[i].w;
        asm("v_dot2c_f32_bf16 %0, %2, %6\n\tv_dot2c_f32_bf16 %1, %3, %6\n\tv_dot2c_f32_bf16 %0, %4, %6\n\tv_dot2c_f32_bf16 %1, %5, %6"
            : "+v"(acc), "+v"(a1) : "v"(w0), "v"(w1), "v"(w2), "v"(w3), "v"(0x3f803f80u));
    }
    asm volatile("s_nop 2" : "+v"(acc), "+v"(a1));
    return acc + a1;
}
DI float sum_xor32(float v) { auto r = __builtin_amdgcn_permlane32_swap(__float_as_uint(v), __float_as_uint(v), false, false); return __uint_as_float(r[0]) + __uint_as_float(r[1]); }
DI float max_xor32(float v) { auto r = __builtin_amdgcn_permlane32_swap(__float_as_uint(v), __float_as_uint(v), false, false); return fmaxf(__uint_as_float(r[0]), __uint_as_float(r[1])); }
template <bool SC1> DI void glds16s(const void* sbase, unsigned voff, unsigned lds_addr) {
    unsigned keep;
    if (SC1) asm volatile("s_mov_b32 %0, m0\n\ts_mov_b32 m0, %3\n\ts_nop 0\n\tglobal_load_lds_dwordx4 %1, %2 sc1" WNT_STR "\n\ts_mov_b32 m0, %0" : "=&s"(keep) : "v"(voff), "s"(sbase), "s"(lds_addr) : "memory");
    else asm volatile("s_mov_b32 %0, m0\n\ts_mov_b32 m0, %3\n\ts_nop 0\n\tglobal_load_lds_dwordx4 %1, %2\n\ts_mov_b32 m0, %0" : "=&s"(keep) : "v"(voff), "s"(sbase), "s"(lds_addr) : "memory");
}
#define BAR() asm volatile("s_waitcnt lgkmcnt(0)\n\ts_barrier" ::: "memory")
#define DRAIN() asm volatile("s_waitcnt vmcnt(0)" ::: "memory")
#define WAITV(n) asm volatile("s_waitcnt vmcnt(" #n ")" ::: "memory")
#define SB() __builtin_amdgcn_sched_barrier(0)
#define SGB(mask, n) __builtin_amdgcn_sched_group_barrier(mask, n, 0)

typedef __amdgpu_buffer_rsrc_t rsrc_t;
DI u32x4 ldw(rsrc_t rs, int voff, int soff) { return __builtin_amdgcn_raw_buffer_load_b128(rs, voff, soff, AUX_SC1); }
DI u32x4 ldwt(rsrc_t rs, int voff, int soff) { return __builtin_amdgcn_raw_buffer_load_b128(rs, voff, soff, WAUX); }
DI void stw(rsrc_t rs, int voff, int soff, u32x4 v) { __builtin_amdgcn_raw_buffer_store_b128(v, rs, voff + soff, 0, AUX_SC1); }
DI f32x2 ldst(rsrc_t rs, int voff, int soff) { return __builtin_bit_cast(f32x2, __builtin_amdgcn_raw_buffer_load_b64(rs, voff, soff, AUX_SC1)); }
DI void stst(rsrc_t rs, int voff, int soff, f32x2 v) { __builtin_amdgcn_raw_buffer_store_b64(__builtin_bit_cast(u32x2, v), rs, voff, soff, AUX_SC1); }

#define FSTRIDE 32
DI gu32* flagp(const Params& p, int ph, int g, int r) { return (gu32*)(p.ws + FLAG_OFF) + (ph * 16 + g) * 16 * 32 + r * FSTRIDE; }
DI void set_flag(const Params& p, int ph, int g, int r, unsigned tag) { asm volatile("" : "+s"(tag)); __hip_atomic_store(flagp(p, ph, g, r), tag, __ATOMIC_RELAXED, __HIP_MEMORY_SCOPE_AGENT); }
DI void wait_word(const Params& p, gu32* a, unsigned tag) {
    for (unsigned spins = 0;; ++spins) {
        const unsigned v = __hip_atomic_load(a, __ATOMIC_RELAXED, __HIP_MEMORY_SCOPE_AGENT);
        if (__all(v == tag)) break;
        if (spins > (1u << 20)) { unsigned one = 1u; asm volatile("" : "+s"(one)); __hip_atomic_store((gu32*)(p.ws + TMO_OFF), one, __ATOMIC_RELAXED, __HIP_MEMORY_SCOPE_AGENT); break; }
        __builtin_amdgcn_s_sleep(1);
    }
    __builtin_amdgcn_fence(__ATOMIC_ACQUIRE, "wavefront");
}
DI void wait_group(const Params& p, int ph, int g, unsigned tag, int lane) { wait_word(p, flagp(p, ph, g, lane & 15), tag); }

DI void stats_store(rsrc_t rs, int buf, int T, int u, int lane, const unsigned (&c)[8]) {
    float s = 0.f, ss = 0.f;
#pragma unroll
    for (int i = 0; i < 8; ++i) { float lo = bflo(c[i]), hi = bfhi(c[i]); s += lo + hi; ss += lo * lo + hi * hi; }
    s = sum_xor32(s); ss = sum_xor32(ss);
    if (lane < 32) stst(rs, lane * 8, ST_OFF + buf * ST_BUF + (T * 16 + u) * 256, f32x2{s, ss});
}

DI void stage_lin(const char* wsb, unsigned lds0, int src, int dst, int n, int hw, int lane, int nw = 4) {
    const unsigned vo = lane * 16;
    for (int i = hw; i < n; i += nw) glds16s<true>(wsb + src + i * 1024, vo, lds0 + dst + i * 1024);
}

#define HELPER_VARS() \
    int hw = w_ - 4; asm volatile("" : "+s"(hw)); \
    const char* wsb = p.ws; asm volatile("" : "+s"(wsb)); \
    unsigned lds0 = lds0_; asm volatile("" : "+s"(lds0)); (void)hw; (void)wsb; (void)lds0;
#define PHASE_VARS() \
    int lane = lane_id(); asm volatile("" : "+v"(lane)); \
    int lyr = layer; asm volatile("" : "+s"(lyr)); \
    int g = g_, r = r_, w = w_; asm volatile("" : "+s"(g), "+s"(r), "+s"(w)); \
    const int h = lane >> 5, tok = lane & 31; const int m = r & 1, n = r >> 1; (void)h; (void)tok; (void)m; (void)n; (void)lyr;

DI void attn_finish(rsrc_t rs, const f32x16& o, int hd, int T, int r, int lyr, int lane) {
    float ov[8]; float l;
    if (hd == 0) { l = o[8];
#pragma unroll
        for (int i = 0; i < 8; ++i) ov[i] = o[i];
    } else { l = o[0];
#pragma unroll
        for (int i = 0; i < 8; ++i) ov[i] = o[8 + i];
    }
    const float inv = __builtin_amdgcn_rcpf(l);
    stw(rs, lane * 16, O_OFF + lyr * 2 * MB + (T * 32 + 2 * r + hd) * 1024,
        u32x4{pk(ov[0] * inv, ov[1] * inv), pk(ov[2] * inv, ov[3] * inv), pk(ov[4] * inv, ov[5] * inv), pk(ov[6] * inv, ov[7] * inv)});
}
DI float max16(float m, const f32x16& s) {
#pragma unroll
    for (int i = 0; i < 16; i += 2) m = fmaxf(fmaxf(m, s[i]), s[i + 1]);
    return m;
}
DI void max16x2(float& m0, float& m1, const f32x16& s) {
#pragma unroll
    for (int i = 0; i < 16; i += 4) { m0 = fmaxf(fmaxf(m0, s[i]), s[i + 1]); m1 = fmaxf(fmaxf(m1, s[i + 2]), s[i + 3]); }
}
DI void exp_pack(const f32x16& s, u32x4& p0, u32x4& p1) {
    unsigned pw[8];
#pragma unroll
    for (int i = 0; i < 8; ++i) pw[i] = pk(__builtin_amdgcn_exp2f(s[2 * i]), __builtin_amdgcn_exp2f(s[2 * i + 1]));
    p0 = u32x4{pw[0], pw[1], pw[2], pw[3]}; p1 = u32x4{pw[4], pw[5], pw[6], pw[7]};
}
DI float wave32_max(float v) {
    int x = __float_as_int(v);
    x = max(x, __builtin_amdgcn_update_dpp(x, x, 0x111, 0xf, 0xf, false));
    x = max(x, __builtin_amdgcn_update_dpp(x, x, 0x112, 0xf, 0xf, false));
    x = max(x, __builtin_amdgcn_update_dpp(x, x, 0x114, 0xf, 0xf, false));
    x = max(x, __builtin_amdgcn_update_dpp(x, x, 0x118, 0xf, 0xf, false));
    x = max(x, __builtin_amdgcn_update_dpp(x, x, 0x142, 0xa, 0xf, false));
    return __int_as_float(__builtin_amdgcn_readlane(x, 31));
}
DI void attn_seg(unsigned ka, unsigned va, int ks, int vs, int n, const bf16x8 qf, const f32x16 c0, f32x16& o) {
    int d;
    asm volatile(
        "ds_read_b128 v[248:251], %[ka]\n\tv_add_u32 %[ka], %[ks], %[ka]\n\tds_read_b128 v[236:239], %[ka]\n\tv_add_u32 %[ka], %[ks], %[ka]\n\t"
        "ds_read_b128 v[240:243], %[va]\n\tds_read_b128 v[244:247], %[va] offset:1024\n\tv_add_u32 %[va], %[vs], %[va]\n\t"
        "s_lshr_b32 %[d], %[n], 1\n\ts_waitcnt lgkmcnt(3)\n\t"
        "v_mfma_f32_32x32x16_bf16 v[204:219], v[248:251], %[q], %[c0]\n\t"
        "s_cmp_eq_u32 %[d], 0\n\ts_nop 10\n\ts_cbranch_scc1 Ltail_%=\n"
        "Lloop_%=:\n\t"
        "v_exp_f32 v204, v204\n\t"
        "v_exp_f32 v205, v205\n\t"
        "v_exp_f32 v206, v206\n\t"
        "v_exp_f32 v207, v207\n\t"
        "v_exp_f32 v208, v208\n\t"
        "v_exp_f32 v209, v209\n\t"
        "v_exp_f32 v210, v210\n\t"
        "v_exp_f32 v211, v211\n\t"
        "v_cvt_pk_bf16_f32 v204, v204, v205\n\t"
        "v_cvt_pk_bf16_f32 v205, v206, v207\n\t"
        "v_cvt_pk_bf16_f32 v206, v208, v209\n\t"
        "v_cvt_pk_bf16_f32 v207, v210, v211\n\t"
        "s_waitcnt lgkmcnt(1)\n\ts_nop 0\n\t"
        "v_mfma_f32_32x32x16_bf16 %[o], v[240:243], v[204:207], %[o]\n\t"
        "v_exp_f32 v212, v212\n\t"
        "v_exp_f32 v213, v213\n\t"
        "v_exp_f32 v214, v214\n\t"
        "v_exp_f32 v215, v215\n\t"
        "v_mfma_f32_32x32x16_bf16 v[220:235], v[236:239], %[q], 0\n\t"
        "ds_read_b128 v[236:239], %[ka]\n\tds_read_b128 v[248:251], %[va]\n\tds_read_b128 v[252:255], %[va] offset:1024\n\t"
        "v_add_u32 %[ka], %[ks], %[ka]\n\tv_add_u32 %[va], %[vs], %[va]\n\t"
        "v_exp_f32 v216, v216\n\t"
        "v_exp_f32 v217, v217\n\t"
        "v_exp_f32 v218, v218\n\t"
        "v_exp_f32 v219, v219\n\t"
        "v_cvt_pk_bf16_f32 v212, v212, v213\n\t"
        "v_cvt_pk_bf16_f32 v213, v214, v215\n\t"
        "v_cvt_pk_bf16_f32 v214, v216, v217\n\t"
        "v_cvt_pk_bf16_f32 v215, v218, v219\n\t"
        "s_waitcnt lgkmcnt(3)\n\ts_nop 0\n\t"
        "v_mfma_f32_32x32x16_bf16 %[o], v[244:247], v[212:215], %[o]\n\t"
        "v_exp_f32 v220, v220\n\t"
        "v_exp_f32 v221, v221\n\t"
        "v_exp_f32 v222, v222\n\t"
        "v_exp_f32 v223, v223\n\t"
        "v_exp_f32 v224, v224\n\t"
        "v_exp_f32 v225, v225\n\t"
        "v_exp_f32 v226, v226\n\t"
        "v_exp_f32 v227, v227\n\t"
        "v_cvt_pk_bf16_f32 v220, v220, v221\n\t"
        "v_cvt_pk_bf16_f32 v221, v222, v223\n\t"
        "v_cvt_pk_bf16_f32 v222, v224, v225\n\t"
        "v_cvt_pk_bf16_f32 v223, v226, v227\n\t"
        "s_waitcnt lgkmcnt(1)\n\ts_nop 0\n\t"
        "v_mfma_f32_32x32x16_bf16 %[o], v[248:251], v[220:223], %[o]\n\t"
        "v_exp_f32 v228, v228\n\t"
        "v_exp_f32 v229, v229\n\t"
        "v_exp_f32 v230, v230\n\t"
        "v_exp_f32 v231, v231\n\t"
        "v_mfma_f32_32x32x16_bf16 v[204:219], v[236:239], %[q], 0\n\t"
        "ds_read_b128 v[236:239], %[ka]\n\tds_read_b128 v[240:243], %[va]\n\tds_read_b128 v[244:247], %[va] offset:1024\n\t"
        "v_add_u32 %[ka], %[ks], %[ka]\n\tv_add_u32 %[va], %[vs], %[va]\n\t"
        "v_exp_f32 v232, v232\n\t"
        "v_exp_f32 v233, v233\n\t"
        "v_exp_f32 v234, v234\n\t"
        "v_exp_f32 v235, v235\n\t"
        "v_cvt_pk_bf16_f32 v228, v228, v229\n\t"
        "v_cvt_pk_bf16_f32 v229, v230, v231\n\t"
        "v_cvt_pk_bf16_f32 v230, v232, v233\n\t"
        "v_cvt_pk_bf16_f32 v231, v234, v235\n\t"
        "s_waitcnt lgkmcnt(3)\n\ts_nop 0\n\t"
        "v_mfma_f32_32x32x16_bf16 %[o], v[252:255], v[228:231], %[o]\n\t"
        "s_sub_u32 %[d], %[d], 1\n\ts_cmp_lg_u32 %[d], 0\n\ts_cbranch_scc1 Lloop_%=\n"
        "Ltail_%=:\n\t"
        "s_bitcmp1_b32 %[n], 0\n\ts_waitcnt lgkmcnt(0)\n\ts_cbranch_scc0 Lend_%=\n\t"
        "v_exp_f32 v204, v204\n\t"
        "v_exp_f32 v205, v205\n\t"
        "v_exp_f32 v206, v206\n\t"
        "v_exp_f32 v207, v207\n\t"
        "v_exp_f32 v208, v208\n\t"
        "v_exp_f32 v209, v209\n\t"
        "v_exp_f32 v210, v210\n\t"
        "v_exp_f32 v211, v211\n\t"
        "v_cvt_pk_bf16_f32 v204, v204, v205\n\t"
        "v_cvt_pk_bf16_f32 v205, v206, v207\n\t"
        "v_cvt_pk_bf16_f32 v206, v208, v209\n\t"
        "v_cvt_pk_bf16_f32 v207, v210, v211\n\t"
        "s_nop 1\n\t"
        "v_mfma_f32_32x32x16_bf16 %[o], v[240:243], v[204:207], %[o]\n\t"
        "v_exp_f32 v212, v212\n\t"
        "v_exp_f32 v213, v213\n\t"
        "v_exp_f32 v214, v214\n\t"
        "v_exp_f32 v215, v215\n\t"
        "v_exp_f32 v216, v216\n\t"
        "v_exp_f32 v217, v217\n\t"
        "v_exp_f32 v218, v218\n\t"
        "v_exp_f32 v219, v219\n\t"
        "v_cvt_pk_bf16_f32 v212, v212, v213\n\t"
        "v_cvt_pk_bf16_f32 v213, v214, v215\n\t"
        "v_cvt_pk_bf16_f32 v214, v216, v217\n\t"
        "v_cvt_pk_bf16_f32 v215, v218, v219\n\t"
        "s_nop 1\n\t"
        "v_mfma_f32_32x32x16_bf16 %[o], v[244:247], v[212:215], %[o]\n"
        "Lend_%=:\n\ts_nop 11"
        : [o] "+v"(o), [ka] "+v"(ka), [va] "+v"(va), [d] "=&s"(d)
        : [q] "v"(qf), [c0] "v"(c0), [ks] "s"(ks), [vs] "v"(vs), [n] "s"(n)
        : "memory", "scc", "v204", "v205", "v206", "v207", "v208", "v209", "v210", "v211", "v212", "v213", "v214", "v215", "v216", "v217", "v218", "v219", "v220", "v221", "v222", "v223", "v224", "v225", "v226", "v227", "v228", "v229", "v230", "v231", "v232", "v233", "v234", "v235", "v236", "v237", "v238", "v239", "v240", "v241", "v242", "v243", "v244", "v245", "v246", "v247", "v248", "v249", "v250", "v251", "v252", "v253", "v254", "v255");
}
constexpr float BOUND2 = 4096.f;
DI f32x16 attn_exact(LAS char* lds, int tw, int hd, int s0, bool first_masked, const bf16x8 qf, int lane) {
    const int h = lane >> 5, tok = lane & 31;
    const LAS char* kb = lds + L_KV + hd * 1024 + lane * 16;
    const bool isdata = ((lane >> 4) & 1) == hd;
    const LAS char* vb = isdata ? (lds + L_KV + 2048 + lane * 16) : (lds + L_ONES);
    const int vstep = isdata ? 4096 : 0;
    const f32x16 zero = {};
    f32x16 o = {};
    float mx = -INFINITY;
    for (int pass = 0; pass < 2; ++pass) {
        for (int sl = s0; sl <= 8 + tw; ++sl) {
            f32x16 s = mfma32(*(const LAS bf16x8*)(kb + sl * 4096), qf, zero);
            const bool diag = sl == 8 + tw, first = first_masked && sl == s0;
#pragma unroll
            for (int i = 0; i < 16; ++i) { const int kk = (i & 3) + 8 * (i >> 2) + 4 * h; if ((diag && kk > tok) || (first && kk <= tok)) s[i] = -INFINITY; }
            if (pass == 0) mx = max16(mx, s);
            else {
#pragma unroll
                for (int i = 0; i < 16; ++i) s[i] -= mx;
                u32x4 p0, p1; exp_pack(s, p0, p1);
                o = mfma32(*(const LAS bf16x8*)(vb + sl * vstep), asfrag(p0), o);
                o = mfma32(*(const LAS bf16x8*)(vb + sl * vstep + 1024), asfrag(p1), o);
            }
        }
        if (pass == 0) mx = max_xor32(mx);
    }
    return o;
}
DI void attn_phase(const Params& p, LAS char* lds, rsrc_t rs, int w, int g, int r, int lyr, int lane, unsigned tag TS_PARAM) {
    const int h = lane >> 5, tok = lane & 31;
    const int hd = w >> 2, tw = hd ? 3 - (w & 3) : (w & 3), T = 4 * g + tw;
    const bf16x8 qf = *(const LAS bf16x8*)(lds + L_Q + tw * 2048 + hd * 1024 + lane * 16);
    const unsigned kb = (unsigned)(uintptr_t)(lds + L_KV + hd * 1024 + lane * 16);
    const bool isdata = ((lane >> 4) & 1) == hd;
    const unsigned vb = (unsigned)(uintptr_t)(isdata ? (lds + L_KV + 2048 + lane * 16) : (lds + L_ONES));
    const int vstep = isdata ? 4096 : 0;
    const LAS float* kn = (const LAS float*)(lds + L_KN);
    const float qn = ((const LAS float*)(lds + L_QN))[tw * 2 + hd] * 1.05f;
    const float kown = fmaxf(fmaxf(kn[16 + hd], kn[18 + hd]), fmaxf(kn[20 + hd], kn[22 + hd]));
    bool fast = __builtin_amdgcn_readfirstlane(qn * kown <= BOUND2);
    float dd = (float)(tok - 4 * h) * 1e30f; asm volatile("" : "+v"(dd));
    f32x16 t, o = {};
#pragma unroll
    for (int i = 0; i < 16; ++i) t[i] = dd + (0.5f - (float)((i & 3) + 8 * (i >> 2))) * 1e30f;
    const int s4 = w == 7 ? 4 : 0, tile0 = 4 * g - 8 + s4;
    const bool loader = w >= 6 && tile0 >= 0;
    u32x4 hv[16]; f32x2 hn;
    if (loader) {
        wait_word(p, flagp(p, 1 + 5 * lyr, g - 2 + (s4 >> 2), r), tag);
        TS(tb + 1);
        const int kvo = KV_OFF + lyr * 4 * MB + (tile0 * 16 + r) * 4096;
#pragma unroll
        for (int i = 0; i < 16; ++i) hv[i] = ldw(rs, lane * 16 + (i & 3) * 1024, kvo + (i >> 2) * 65536);
        hn = ldst(rs, (lane & 3) * 128, KN_OFF + ((lyr * 64 + tile0) * 16 + r) * 8);
        SB();
    }
    if (fast) {
        f32x16 m;
#pragma unroll
        for (int i = 0; i < 16; ++i) m[i] = fminf(t[i], 0.f);
        attn_seg(kb + (8 + tw) * 4096, vb + (8 + tw) * vstep, -4096, -vstep, tw + 1, qf, m, o);
    }
    TS(tb);
    if (loader) {
        LAS char* kvl = lds + L_KV + s4 * 4096 + lane * 16;
#pragma unroll
        for (int i = 0; i < 16; ++i) *(LAS u32x4*)(kvl + i * 1024) = hv[i];
        if (lane < 4) *(LAS f32x2*)(lds + L_KN + (s4 + lane) * 8) = hn;
    }
    BAR();
    TS(tb + 2);
    const int s0 = T >= 8 ? tw : 8 - 4 * g;
    if (s0 < 8) {
        float kh = 0.f;
#pragma unroll
        for (int j = 0; j < 8; ++j) kh = fmaxf(kh, j >= 8 - 4 * g ? kn[2 * j + hd] : 0.f);
        fast = fast && __builtin_amdgcn_readfirstlane(qn * kh <= BOUND2);
        if (fast) {
            f32x16 m;
#pragma unroll
            for (int i = 0; i < 16; ++i) m[i] = T >= 8 ? fminf(-t[i], 0.f) : 0.f;
            attn_seg(kb + s0 * 4096, vb + s0 * vstep, 4096, vstep, 8 - s0, qf, m, o);
        }
    }
    if (!fast) o = attn_exact(lds, tw, hd, s0, T >= 8, qf, lane);
    attn_finish(rs, o, hd, T, r, lyr, lane);
}
DI void ln_stats(const f32x2 (&sv)[8], float& mean, float& rstd) {
    float S = 0.f, SS = 0.f;
#pragma unroll
    for (int i = 0; i < 8; ++i) { S += sv[i].x; SS += sv[i].y; }
    S = sum_xor32(S); SS = sum_xor32(SS);
    mean = S * (1.f / DM); const float var = fmaxf(SS * (1.f / DM) - mean * mean, 0.f); rstd = __builtin_amdgcn_rsqf(var + EPS);
}
DI unsigned read_tag(const Params& p) {
    return (unsigned)__builtin_amdgcn_readfirstlane(__hip_atomic_load((gu32*)(p.ws + EPOCH_OFF), __ATOMIC_RELAXED, __HIP_MEMORY_SCOPE_AGENT)) + 1u;
}
#define LDA(arr, s, base) arr[s] = ldw(rs, lane * 16 + ((s) & 3) * 1024, (base) + ((s) >> 2) * 4096)

#define WODELAY 36
#define PD0 4
#define PDV 12
constexpr int a_issued(int s, int total, int pd0 = PD0, int pdv = PDV) { int v = pd0 + 2 * (s + 1); const int cap = s + 1 + pdv; v = v < cap ? v : cap; return v < total ? v : total; }
#define PD40 4
#define PD4V 12

DI void main_path(const Params& p, LAS char* lds, rsrc_t rs, const int w_, const int g_, const int r_) {
    unsigned tag = 0;
    TS_DECL();
    TS(0);
    {
        const int layer = 0; PHASE_VARS();
        const int T = 4 * g + (r & 3), u = (r >> 2) * 4 + w;
        const uint16_t* row = p.x + (size_t)(T * 32 + tok) * DM + 32 * u + 4 * h;
        u32x2 a0 = *(const GAS u32x2*)(row), a1 = *(const GAS u32x2*)(row + 8), a2 = *(const GAS u32x2*)(row + 16), a3 = *(const GAS u32x2*)(row + 24);
        tag = read_tag(p); asm volatile("" :: "s"(tag) : "memory");
        stw(rs, lane * 16, XF_OFF + (T * 32 + 2 * u) * 1024, u32x4{a0.x, a0.y, a1.x, a1.y});
        stw(rs, lane * 16, XF_OFF + (T * 32 + 2 * u + 1) * 1024, u32x4{a2.x, a2.y, a3.x, a3.y});
        const unsigned c[8] = {a0.x, a0.y, a1.x, a1.y, a2.x, a2.y, a3.x, a3.y};
        stats_store(rs, 0, T, u, lane, c);
        DRAIN();
        BAR();
        if (w == 0 && lane == 0) { set_flag(p, 0, g, r, tag); __hip_atomic_store((gu32*)(p.ws + ACK_OFF) + blockIdx.x * 32, tag, __ATOMIC_RELAXED, __HIP_MEMORY_SCOPE_AGENT); }
    }
    TS(1);
    for (int layer = 0; layer < 2; ++layer) {
        const int tb = 2 + layer * 24; (void)tb;
        {
            PHASE_VARS();
            if (w == 0) {
                if (lyr == 0) {
                    const int j = lane & 15;
                    wait_word(p, (lane & 16) ? flagp(p, 0, j, r) : flagp(p, 0, g, j), tag);
                } else wait_group(p, 5, g, tag, lane);
            }
            BAR();
            TS(tb + 0);
            const int T = 4 * g + w;
            const LAS char* wb = lds + L_WQ + lane * 16;
            LAS float* csq = (LAS float*)(lds + L_CSQ);
            LAS float* stt = (LAS float*)(lds + L_STT);
            f32x16 ak = {}, av = {};
            u32x4 xa[32];
            f32x2 sv[8];
            const int sto = ST_OFF + 2 * lyr * ST_BUF + T * 4096, xo = XF_OFF + lyr * 2 * MB + T * 32768;
            if (lyr == 0) {
                const char* src = p.ws + WQT_OFF + r * 98304;
#pragma unroll
                for (int i = 0; i < 4; ++i) { const int j = 4 * i + w, off = ((j & 1) * 32 + (j >> 1)) * 1024; glds16s<true>(src + off, lane * 16, (unsigned)(uintptr_t)lds + L_WQ + off); }
            }
#pragma unroll
            for (int s = 0; s < PD0; ++s) { LDA(xa, s, xo); SB(); }
            if (lyr == 0) { asm volatile("s_waitcnt vmcnt(%0)" :: "i"(PD0) : "memory"); BAR(); }
            bf16x8 wf[4][2];
#pragma unroll
            for (int s = 0; s < 3; ++s) { wf[s][0] = ldsfrag(wb + s * 1024); wf[s][1] = ldsfrag(wb + (32 + s) * 1024); }
#pragma unroll
            for (int s = 0; s < 32; ++s) {
                if ((s == 5 || s == 13 || s == 21) && lyr == 0) BAR();
#pragma unroll
                for (int j = (s == 0 ? PD0 : a_issued(s - 1, 32)); j < a_issued(s, 32); ++j) LDA(xa, j, xo);
                if (s >= 12 && s < 20) sv[s - 12] = ldst(rs, (8 * h * 32 + tok) * 8 + (s - 12) * 256, sto);
                if (s + 3 < 32) { wf[(s + 3) & 3][0] = ldsfrag(wb + (s + 3) * 1024); wf[(s + 3) & 3][1] = ldsfrag(wb + (32 + s + 3) * 1024); }
                const bf16x8 x = asfrag(xa[s]);
                ak = mfma32(wf[s & 3][0], x, ak); av = mfma32(x, wf[s & 3][1], av);
                SGB(0x100, 1); SGB(0x8, 1); SGB(0x20, 2); SGB(0x100, 1); SGB(0x8, 1);
                SB();
            }
            float mean, rstd; ln_stats(sv, mean, rstd);
            if (h == 0) { stt[w * 64 + tok] = rstd; stt[w * 64 + 32 + tok] = mean * rstd; }
            TS(tb + 1);
            BAR();
            TS(tb + 2);
            const float mr = mean * rstd;
            {
                float k[16], v[16];
                const float cv = csq[64 + tok];
#pragma unroll
                for (int g4 = 0; g4 < 4; ++g4) {
                    const f32x4 ck = *(LAS f32x4*)(csq + 32 + 8 * g4 + 4 * h);
                    const f32x4 rsv = *(LAS f32x4*)(stt + w * 64 + 8 * g4 + 4 * h), ms = *(LAS f32x4*)(stt + w * 64 + 32 + 8 * g4 + 4 * h);
#pragma unroll
                    for (int i = 0; i < 4; ++i) {
                        k[4 * g4 + i] = ak[4 * g4 + i] * rstd - mr * ck[i];
                        v[4 * g4 + i] = av[4 * g4 + i] * rsv[i] - ms[i] * cv;
                    }
                }
                const u32x4 k0 = {pk(k[0], k[1]), pk(k[2], k[3]), pk(k[4], k[5]), pk(k[6], k[7])};
                const u32x4 k1 = {pk(k[8], k[9]), pk(k[10], k[11]), pk(k[12], k[13]), pk(k[14], k[15])};
                const u32x4 v0 = {pk(v[0], v[1]), pk(v[2], v[3]), pk(v[4], v[5]), pk(v[6], v[7])};
                const u32x4 v1 = {pk(v[8], v[9]), pk(v[10], v[11]), pk(v[12], v[13]), pk(v[14], v[15])};
                const int kvo = KV_OFF + lyr * 4 * MB + (T * 16 + r) * 4096;
                stw(rs, lane * 16, kvo, k0); stw(rs, lane * 16 + 1024, kvo, k1); stw(rs, lane * 16 + 2048, kvo, v0); stw(rs, lane * 16 + 3072, kvo, v1);
                float na = 0.f, nb = 0.f;
#pragma unroll
                for (int i = 0; i < 8; ++i) { na = __builtin_fmaf(k[i], k[i], na); nb = __builtin_fmaf(k[8 + i], k[8 + i], nb); }
                const f32x2 nn = {wave32_max(sum_xor32(na)), wave32_max(sum_xor32(nb))};
                if (lane == 0) { stst(rs, 0, KN_OFF + ((lyr * 64 + T) * 16 + r) * 8, nn); *(LAS f32x2*)(lds + L_KN + (8 + w) * 8) = nn; }
                LAS char* kvl = lds + L_KV + (8 + w) * 4096 + lane * 16;
                *(LAS u32x4*)(kvl) = k0; *(LAS u32x4*)(kvl + 1024) = k1; *(LAS u32x4*)(kvl + 2048) = v0; *(LAS u32x4*)(kvl + 3072) = v1;
            }
            SB();
            f32x16 aq = {};
            bf16x8 wq[4];
#pragma unroll
            for (int s = 0; s < 3; ++s) wq[s] = ldsfrag(wb + (64 + s) * 1024);
#pragma unroll
            for (int s = 0; s < 32; ++s) {
                if (s == 22) {
                    DRAIN();
                    BAR();
                    if (w == 0 && lane == 0) set_flag(p, 1 + 5 * lyr, g, r, tag);
                    TS(tb + 3);
                }
                if (s + 3 < 32) wq[(s + 3) & 3] = ldsfrag(wb + (64 + s + 3) * 1024);
                aq = mfma32(wq[s & 3], asfrag(xa[s]), aq);
                SGB(0x100, 1); SGB(0x8, 1);
                SB();
            }
            {
                float q[16];
#pragma unroll
                for (int g4 = 0; g4 < 4; ++g4) {
                    const f32x4 cq = *(LAS f32x4*)(csq + 8 * g4 + 4 * h);
#pragma unroll
                    for (int i = 0; i < 4; ++i) q[4 * g4 + i] = (aq[4 * g4 + i] * rstd - mr * cq[i]) * QSCALE;
                }
                LAS char* ql = lds + L_Q + w * 2048 + lane * 16;
                *(LAS u32x4*)(ql) = u32x4{pk(q[0], q[1]), pk(q[2], q[3]), pk(q[4], q[5]), pk(q[6], q[7])};
                *(LAS u32x4*)(ql + 1024) = u32x4{pk(q[8], q[9]), pk(q[10], q[11]), pk(q[12], q[13]), pk(q[14], q[15])};
                float na = 0.f, nb = 0.f;
#pragma unroll
                for (int i = 0; i < 8; ++i) { na = __builtin_fmaf(q[i], q[i], na); nb = __builtin_fmaf(q[8 + i], q[8 + i], nb); }
                const f32x2 nn = {wave32_max(sum_xor32(na)), wave32_max(sum_xor32(nb))};
                if (lane == 0) *(LAS f32x2*)(lds + L_QN + w * 8) = nn;
            }
            BAR();
        }
        {
            PHASE_VARS();
            attn_phase(p, lds, rs, w, g, r, lyr, lane, tag TS_ARG(layer == 0 ? 22 : 48));
            TS(tb + 4); TS(tb + 5); TS(tb + 6);
            DRAIN();
            BAR();
            if (w == 0 && lane == 0) set_flag(p, 2 + 5 * lyr, g, r, tag);
            TS(tb + 7);
        }
        {
            PHASE_VARS();
            const int mt = w & 1, kh = (w >> 1) & 1, T = 4 * g + 2 * m + mt, u = 2 * n + kh;
            const int xo = XF_OFF + lyr * 2 * MB + (T * 32 + 2 * u) * 1024;
            const u32x4 r0 = ldw(rs, lane * 16, xo), r1 = ldw(rs, lane * 16 + 1024, xo);
            if (w == 0) wait_group(p, 2 + 5 * lyr, g, tag, lane);
            BAR();
            TS(tb + 8);
            f32x16 acc[2] = {};
            u32x4 xa[16];
            const int oo = O_OFF + lyr * 2 * MB + (T * 32 + 16 * kh) * 1024;
#pragma unroll
            for (int s = 0; s < 16; ++s) { LDA(xa, s, oo); SB(); }
            const LAS char* wb = lds + L_WO + kh * 32768 + lane * 16;
            bf16x8 wf[4][2];
#pragma unroll
            for (int s = 0; s < 3; ++s) { wf[s][0] = ldsfrag(wb + s * 2048); wf[s][1] = ldsfrag(wb + s * 2048 + 1024); }
#pragma unroll
            for (int s = 0; s < 16; ++s) {
                if (s + 3 < 16) { wf[(s + 3) & 3][0] = ldsfrag(wb + (s + 3) * 2048); wf[(s + 3) & 3][1] = ldsfrag(wb + (s + 3) * 2048 + 1024); }
                const bf16x8 x = asfrag(xa[s]);
                acc[0] = mfma32(wf[s & 3][0], x, acc[0]); acc[1] = mfma32(wf[s & 3][1], x, acc[1]);
                SGB(0x100, 1); SGB(0x8, 1); SGB(0x20, 2); SGB(0x100, 1); SGB(0x8, 1);
                SB();
            }
            const f32x16 snd = kh ? acc[0] : acc[1];
#pragma unroll
            for (int g4 = 0; g4 < 4; ++g4) *(LAS f32x4*)(lds + L_RED2 + ((w * 4 + g4) * 64 + lane) * 16) = f32x4{snd[4 * g4], snd[4 * g4 + 1], snd[4 * g4 + 2], snd[4 * g4 + 3]};
            TS(tb + 9);
            BAR();
            const f32x16 own = kh ? acc[1] : acc[0];
            const int pw = w ^ 2;
            float v[16];
#pragma unroll
            for (int g4 = 0; g4 < 4; ++g4) { f32x4 t = *(LAS f32x4*)(lds + L_RED2 + ((pw * 4 + g4) * 64 + lane) * 16);
#pragma unroll
                for (int i = 0; i < 4; ++i) v[4 * g4 + i] = own[4 * g4 + i] + t[i]; }
            const unsigned rr[8] = {r0.x, r0.y, r0.z, r0.w, r1.x, r1.y, r1.z, r1.w};
            unsigned c[8];
#pragma unroll
            for (int i = 0; i < 8; ++i) c[i] = pk(v[2 * i] + bflo(rr[i]), v[2 * i + 1] + bfhi(rr[i]));
            const int yo = X2_OFF + lyr * 2 * MB + (T * 32 + 2 * u) * 1024;
            stw(rs, lane * 16, yo, u32x4{c[0], c[1], c[2], c[3]});
            stw(rs, lane * 16 + 1024, yo, u32x4{c[4], c[5], c[6], c[7]});
            stats_store(rs, 2 * lyr + 1, T, u, lane, c);
            DRAIN();
            BAR();
            if (w == 0 && lane == 0) set_flag(p, 3 + 5 * lyr, g, r, tag);
            TS(tb + 10);
        }
        {
            PHASE_VARS();
            if (w == 0) wait_group(p, 3 + 5 * lyr, g, tag, lane);
            BAR();
            TS(tb + 11);
            const int T = 4 * g + w;
            const LAS char* wbA = lds + lane * 16;
            const LAS char* wbB = lds + 3072 + (h * 16 + (lane & 15)) * 16;
            LAS float* cs1 = (LAS float*)(lds + L_CS1);
            f32x16 acc[4] = {};
            u32x4 xa[32];
            f32x2 sv[8];
            const int sto = ST_OFF + (2 * lyr + 1) * ST_BUF + T * 4096, xo = X2_OFF + lyr * 2 * MB + T * 32768;
#pragma unroll
            for (int s = 0; s < PD0; ++s) { LDA(xa, s, xo); SB(); }
            auto ldW = [&](int s, int t) { return t < 3 ? ldsfrag(wbA + w1_off(s) + t * 1024) : ldsfrag(wbB + w1_off(s)); };
            bf16x8 wf[2][4];
#pragma unroll
            for (int t = 0; t < 4; ++t) wf[0][t] = ldW(0, t);
#pragma unroll
            for (int s = 0; s < 32; ++s) {
                if (s == 21) BAR();
#pragma unroll
                for (int j = (s == 0 ? PD0 : a_issued(s - 1, 32)); j < a_issued(s, 32); ++j) LDA(xa, j, xo);
                if (s >= 16 && s < 24) sv[s - 16] = ldst(rs, (8 * h * 32 + tok) * 8 + (s - 16) * 256, sto);
                const bf16x8 x = asfrag(xa[s]);
#pragma unroll
                for (int t = 0; t < 4; ++t) {
                    if (s + 1 < 32) wf[(s + 1) & 1][t] = ldW(s + 1, t);
                    acc[t] = mfma32(wf[s & 1][t], x, acc[t]);
                }
                SGB(0x100, 1); SGB(0x8, 1); SGB(0x20, 1); SGB(0x100, 1); SGB(0x8, 1); SGB(0x20, 1); SGB(0x100, 1); SGB(0x8, 1); SGB(0x100, 1); SGB(0x8, 1);
                SB();
            }
            float mean, rstd; ln_stats(sv, mean, rstd);
            TS(tb + 12);
            BAR();
            TS(tb + 13);
            const int ho = H_OFF + lyr * 8 * MB + (T * 112 + 7 * r) * 1024;
            const f32x2 nm2 = {-mean, -mean}, rs2 = {rstd, rstd}, ry2 = {-LOG2E * rstd, -LOG2E * rstd}, one2 = {1.f, 1.f};
#pragma unroll
            for (int t = 0; t < 4; ++t) {
                constexpr int dummy = 0; (void)dummy;
                const int np = t < 3 ? 8 : 4;
                f32x2 tt[8], z[8], e[8];
#pragma unroll
                for (int g4 = 0; g4 < 4; ++g4) {
                    if (2 * g4 < np) {
                        const f32x4 c4 = *(LAS f32x4*)(cs1 + t * 32 + 8 * g4 + 4 * h);
                        tt[2 * g4] = __builtin_elementwise_fma(nm2, f32x2{c4[0], c4[1]}, f32x2{acc[t][4 * g4], acc[t][4 * g4 + 1]});
                        tt[2 * g4 + 1] = __builtin_elementwise_fma(nm2, f32x2{c4[2], c4[3]}, f32x2{acc[t][4 * g4 + 2], acc[t][4 * g4 + 3]});
                    }
                }
#pragma unroll
                for (int i = 0; i < 8; ++i) if (i < np) e[i] = tt[i] * ry2;
                SB();
#pragma unroll
                for (int i = 0; i < 8; ++i) if (i < np) e[i] = f32x2{__builtin_amdgcn_exp2f(e[i].x), __builtin_amdgcn_exp2f(e[i].y)};
#pragma unroll
                for (int i = 0; i < 8; ++i) if (i < np) z[i] = tt[i] * rs2;
                SB();
#pragma unroll
                for (int i = 0; i < 8; ++i) if (i < np) e[i] = e[i] + one2;
                SB();
#pragma unroll
                for (int i = 0; i < 8; ++i) if (i < np) e[i] = f32x2{__builtin_amdgcn_rcpf(e[i].x), __builtin_amdgcn_rcpf(e[i].y)};
                SB();
#pragma unroll
                for (int i = 0; i < 8; ++i) if (i < np) z[i] = z[i] * e[i];
                stw(rs, lane * 16 + (2 * t) * 1024, ho, u32x4{pk(z[0].x, z[0].y), pk(z[1].x, z[1].y), pk(z[2].x, z[2].y), pk(z[3].x, z[3].y)});
                if (t < 3) stw(rs, lane * 16 + (2 * t + 1) * 1024, ho, u32x4{pk(z[4].x, z[4].y), pk(z[5].x, z[5].y), pk(z[6].x, z[6].y), pk(z[7].x, z[7].y)});
                SB();
            }
            DRAIN();
            BAR();
            if (w == 0 && lane == 0) set_flag(p, 4 + 5 * lyr, g, r, tag);
            TS(tb + 14);
        }
        {
            PHASE_VARS();
            const int mt = w & 1, kh = (w >> 1) & 1, T = 4 * g + 2 * m + mt, u = 2 * n + kh;
            const int yo = X2_OFF + lyr * 2 * MB + (T * 32 + 2 * u) * 1024;
            const u32x4 r0 = ldw(rs, lane * 16, yo), r1 = ldw(rs, lane * 16 + 1024, yo);
            if (w == 0) wait_group(p, 4 + 5 * lyr, g, tag, lane);
            BAR();
            TS(tb + 15);
            const LAS char* wb = lds + kh * 2048 + lane * 16;
            f32x16 acc[2] = {};
            const int ho = H_OFF + lyr * 8 * MB + (T * 112 + 56 * kh) * 1024;
            u32x4 xa[56];
#pragma unroll
            for (int s = 0; s < PD40; ++s) { LDA(xa, s, ho); SB(); }
            auto woff = [](int s) { return w2_slot(s); };
            bf16x8 wf[4][2];
#pragma unroll
            for (int s = 0; s < 3; ++s) { wf[s][0] = ldsfrag(wb + woff(s)); wf[s][1] = ldsfrag(wb + woff(s) + 1024); }
#pragma unroll
            for (int s = 0; s < 56; ++s) {
                if (s == 18) { BAR(); TS(tb + 16); }
                if (s == 35) { BAR(); TS(tb + 17); }
#pragma unroll
                for (int j = (s == 0 ? PD40 : a_issued(s - 1, 56, PD40, PD4V)); j < a_issued(s, 56, PD40, PD4V); ++j) LDA(xa, j, ho);
                if (s + 3 < 56) { wf[(s + 3) & 3][0] = ldsfrag(wb + woff(s + 3)); wf[(s + 3) & 3][1] = ldsfrag(wb + woff(s + 3) + 1024); }
                const bf16x8 x = asfrag(xa[s]);
                acc[0] = mfma32(wf[s & 3][0], x, acc[0]); acc[1] = mfma32(wf[s & 3][1], x, acc[1]);
                SGB(0x100, 1); SGB(0x8, 1); SGB(0x20, 2); SGB(0x100, 1); SGB(0x8, 1);
                SB();
            }
            const f32x16 snd = kh ? acc[0] : acc[1];
#pragma unroll
            for (int g4 = 0; g4 < 4; ++g4) *(LAS f32x4*)(lds + L_RED4 + ((w * 4 + g4) * 64 + lane) * 16) = f32x4{snd[4 * g4], snd[4 * g4 + 1], snd[4 * g4 + 2], snd[4 * g4 + 3]};
            TS(tb + 18);
            BAR();
            const f32x16 own = kh ? acc[1] : acc[0];
            const int pw = w ^ 2;
            float v[16];
#pragma unroll
            for (int g4 = 0; g4 < 4; ++g4) { f32x4 t = *(LAS f32x4*)(lds + L_RED4 + ((pw * 4 + g4) * 64 + lane) * 16);
#pragma unroll
                for (int i = 0; i < 4; ++i) v[4 * g4 + i] = own[4 * g4 + i] + t[i]; }
            const unsigned rr[8] = {r0.x, r0.y, r0.z, r0.w, r1.x, r1.y, r1.z, r1.w};
            unsigned c[8];
#pragma unroll
            for (int i = 0; i < 8; ++i) c[i] = pk(v[2 * i] + bflo(rr[i]), v[2 * i + 1] + bfhi(rr[i]));
            if (lyr == 0) {
                stw(rs, lane * 16, XF_OFF + 2 * MB + (T * 32 + 2 * u) * 1024, u32x4{c[0], c[1], c[2], c[3]});
                stw(rs, lane * 16 + 1024, XF_OFF + 2 * MB + (T * 32 + 2 * u) * 1024, u32x4{c[4], c[5], c[6], c[7]});
                stats_store(rs, 2, T, u, lane, c);
                DRAIN();
                BAR();
                if (w == 0 && lane == 0) set_flag(p, 5, g, r, tag);
            } else {
                auto s0 = __builtin_amdgcn_permlane32_swap(c[0], c[2], false, false);
                auto s1 = __builtin_amdgcn_permlane32_swap(c[1], c[3], false, false);
                auto s2 = __builtin_amdgcn_permlane32_swap(c[4], c[6], false, false);
                auto s3 = __builtin_amdgcn_permlane32_swap(c[5], c[7], false, false);
                uint16_t* orow = p.out + (size_t)(T * 32 + (lane & 31)) * DM + 32 * u + 8 * h;
                *(GAS u32x4*)(orow) = u32x4{s0[0], s1[0], s0[1], s1[1]};
                *(GAS u32x4*)(orow + 16) = u32x4{s2[0], s3[0], s2[1], s3[1]};
            }
            TS(tb + 19);
        }
    }
    asm volatile("s_waitcnt lgkmcnt(0)" ::: "memory");
    if (w_ == 0) TS_DUMP();
}

template <int PART> DI void pretile(const Params& p, LAS char* lds, unsigned lds0, rsrc_t rs, int hw, int g, int r, int lane) {
    const int h = lane >> 5, a = (lane & 15) >> 2, pp = lane & 3, ch = (lane >> 4) & 1;
    const int n = r >> 1, j = 2 * g + (r & 1);
    const unsigned scr0 = lds0 + L_SCR + hw * 8192;
    const LAS char* scr = lds + L_SCR + hw * 8192;
    if (hw == 0) {
        const char* base = (const char*)(p.wqkv + (size_t)32 * g * 1536 + 32 * r);
        if (PART == 0) {
#pragma unroll
            for (int i = 0; i < 6; ++i) { const int L = 64 * i + lane, k = L / 12, c = L % 12; glds16s<false>(base, (unsigned)(k * 1536 + (c >> 2) * 512 + (c & 3) * 8) * 2, scr0 + i * 1024); }
            return;
        }
        DRAIN();
        const LAS char* wb = scr + (4 * h + a) * 192 + ch * 32 + pp * 8;
#pragma unroll
        for (int s = 0; s < 2; ++s)
#pragma unroll
            for (int t = 0; t < 3; ++t) stw(rs, lane * 16, WQT_OFF + (r * 96 + ((t + 2) % 3) * 32 + 2 * g + s) * 1024, wfrag_tr(wb + s * 16 * 192 + t * 64, 8 * 192));
    } else if (hw == 1) {
        const char* base = (const char*)(p.w1 + (size_t)32 * g * FF + 112 * r);
        if (PART == 0) {
#pragma unroll
            for (int b = 0; b < 7; ++b) {
                const int L = 64 * b + lane, G = L / 56, x = L - G * 56;
                const int aa = x < 48 ? ((x >> 2) & 3) : ((x - 48) >> 1);
                const int c = x < 48 ? ((x >> 4) * 4 + (x & 3)) : (12 + (x & 1));
                glds16s<false>(base, (unsigned)((4 * G + aa) * FF + c * 8) * 2, scr0 + b * 1024);
            }
            return;
        }
        DRAIN();
        const LAS char* wbA = scr + h * 896 + a * 64 + ch * 32 + pp * 8;
        const LAS char* wbB = scr + h * 896 + 768 + a * 32 + pp * 8;
#pragma unroll
        for (int s = 0; s < 2; ++s) {
            const int dst = W1T_OFF + r * 114688 + (2 * g + s) * 3584;
#pragma unroll
            for (int t = 0; t < 3; ++t) stw(rs, lane * 16, dst + t * 1024, wfrag_tr(wbA + s * 3584 + t * 256, 1792));
            const u32x4 f3 = wfrag_tr(wbB + s * 3584, 1792);
            if (ch == 0) stw(rs, (h * 16 + (lane & 15)) * 16, dst + 3072, f3);
        }
    } else {
        const int rr = lane >> 3, slot = lane & 7, chunk = slot ^ (((rr >> 1) & 1) << 2);
        const unsigned vo = (unsigned)(rr * DM + chunk * 8) * 2;
        const int s0 = hw == 3 ? j : (j < 16 ? 4 * j : 64 + 3 * (j - 16)), ns = hw == 3 ? 1 : (j < 16 ? 4 : 3);
        const char* base = (const char*)((hw == 3 ? p.wo : p.w2) + (size_t)16 * s0 * DM + 64 * n);
        if (PART == 0) {
            for (int i = 0; i < 2 * ns; ++i) glds16s<false>(base + (size_t)i * 8 * DM * 2, vo, scr0 + i * 1024);
            return;
        }
        DRAIN();
        const int sw = ((a >> 1) & 1) << 6;
        const LAS char* wb0 = scr + (4 * h + a) * 128 + (0 ^ sw) + ch * 32 + pp * 8;
        const LAS char* wb1 = scr + (4 * h + a) * 128 + (64 ^ sw) + ch * 32 + pp * 8;
        for (int i = 0; i < ns; ++i) {
            const int s = s0 + i;
            const int kh = s >= 56 ? 1 : 0, sl = s - 56 * kh;
            const int dst = hw == 3 ? WOT_OFF + n * 65536 + s * 2048 : W2T_OFF + (n * 56 + sl) * 4096 + kh * 2048;
            stw(rs, lane * 16, dst, wfrag_tr(wb0 + i * 2048, 1024));
            stw(rs, lane * 16, dst + 1024, wfrag_tr(wb1 + i * 2048, 1024));
        }
    }
    DRAIN();
}

constexpr int w2_regsp(int j) { return j < 9 ? j : j < 29 ? j + 7 : j + 9; }
DI void helper_path(const Params& p, LAS char* lds, unsigned lds0_, rsrc_t rs, const int w_, const int g_, const int r_) {
    unsigned tag = 0;
    TS_DECL();
    {   const int layer = 0; PHASE_VARS(); HELPER_VARS();
        tag = read_tag(p); asm volatile("" :: "s"(tag) : "memory");
        if (hw == 3) {
            unsigned keep;
            const unsigned vo = (unsigned)(lane < 44 ? lane : 43) << 20;
            asm volatile("s_mov_b32 %0, m0\n\ts_mov_b32 m0, %3\n\ts_nop 0\n\tglobal_load_lds_dword %1, %2\n\ts_mov_b32 m0, %0" : "=&s"(keep) : "v"(vo), "s"(p.ws), "s"(lds0 + 140 * KB) : "memory");
            const unsigned vo2 = (unsigned)(lane & 1) << 20;
            asm volatile("s_mov_b32 %0, m0\n\ts_mov_b32 m0, %3\n\ts_nop 0\n\tglobal_load_lds_dword %1, %2\n\ts_mov_b32 m0, %0" : "=&s"(keep) : "v"(vo2), "s"(p.out), "s"(lds0 + 141 * KB) : "memory");
        }
        pretile<0>(p, lds, lds0, rs, hw, g, r, lane);
        if (hw == 0) { pretile<1>(p, lds, lds0, rs, hw, g, r, lane); BAR(); }
        else { BAR(); pretile<1>(p, lds, lds0, rs, hw, g, r, lane); }
    }
    for (int layer = 0; layer < 2; ++layer) {
        {
            PHASE_VARS(); HELPER_VARS();
            HTS(layer * 20 + 0);
            BAR();
            if (w == 7 && lane < 2) *(LAS u32x4*)(lds + L_ONES + lane * 1024) = u32x4{0x3f803f80u, 0x3f803f80u, 0x3f803f80u, 0x3f803f80u};
            float accq = 0.f;
            if (lyr == 0) {
                if (w == 6 && lane == 0) set_flag(p, 10, g, r, tag);
                const char* src = wsb + WQT_OFF + r * 98304;
                auto wq_pieces = [&](int j0, int j1) { for (int j = j0 + hw; j < j1; j += 4) { const int off = (j < 64 ? (j & 1) * 32 + (j >> 1) : j) * 1024; glds16s<true>(src + off, lane * 16, lds0 + L_WQ + off); } };
                const LAS char* wbq = lds + L_WQ + (hw < 2 ? hw : 0) * 32768 + lane * 16;
                float acc = 0.f;
                wq_pieces(16, 32);
                BAR();
                wq_pieces(32, 48);
                WAITV(4); BAR();
                wq_pieces(48, 64);
                acc = colsum_frags<8>(wbq, [](int i) { return (0 + i) * 1024; }, acc);
                WAITV(4); BAR();
                wq_pieces(64, 96);
                acc = colsum_frags<8>(wbq, [](int i) { return (8 + i) * 1024; }, acc);
                WAITV(8); BAR();
                acc = colsum_frags<8>(wbq, [](int i) { return (16 + i) * 1024; }, acc);
                acc = colsum_frags<8>(wbq, [](int i) { return (24 + i) * 1024; }, acc);
                acc = sum_xor32(acc);
                if (h == 0 && hw < 2) ((LAS float*)(lds + L_CSQ))[32 + hw * 32 + tok] = acc;
                wait_word(p, flagp(p, 10, lane & 15, (lane & 16) ? (r ^ 1) : r), tag);
                DRAIN();
            }
            BAR();
            HTS(layer * 20 + 2);
            stage_lin(wsb, lds0, WOT_OFF + n * 65536, L_WO, 64, hw, lane);
            if (lyr == 0 && hw == 2) {
                const LAS char* wbq = lds + L_WQ + 65536 + lane * 16;
#pragma unroll
                for (int q4 = 0; q4 < 4; ++q4) accq = colsum_frags<8>(wbq + q4 * 8192, [](int i) { return i * 1024; }, accq);
                accq = sum_xor32(accq);
                if (h == 0) ((LAS float*)(lds + L_CSQ))[tok] = accq;
            }
            HTS(layer * 20 + 3);
            BAR();
            BAR();
        }
        u32x4 buf[47];
        {   PHASE_VARS(); HELPER_VARS();

            attn_phase(p, lds, rs, w, g, r, lyr, lane, tag TS_ARG(layer == 0 ? 40 : 44));
            HTS(layer * 20 + 4);
            DRAIN();
            HTS(layer * 20 + 5);
            BAR();
            stage_lin(wsb, lds0, W1T_OFF + r * 114688, 64 * KB, 32, hw, lane);
            stage_lin(wsb, lds0, W1T_OFF + r * 114688 + 32256, 96 * KB, 46, hw, lane);
            HTS(layer * 20 + 6);
        }
        {
            PHASE_VARS(); HELPER_VARS();
            const int wsrc = W2T_OFF + n * 56 * 4096 + hw * 1024;
            BAR();
#pragma unroll
            for (int j = 0; j < 12; ++j) { buf[j] = ldwt(rs, lane * 16, wsrc + w2_regsp(j) * 4096); SB(); }
            BAR();
#pragma unroll
            for (int j = 12; j < 22; ++j) { buf[j] = ldwt(rs, lane * 16, wsrc + w2_regsp(j) * 4096); SB(); }
            BAR();
            stage_lin(wsb, lds0, W1T_OFF + r * 114688 + 77 * KB, 0, 35, hw, lane);
        }
        {
            PHASE_VARS(); HELPER_VARS();
            HTS(layer * 20 + 8);
            WAITV(30);
            BAR();
            const int wsrc = W2T_OFF + n * 56 * 4096 + hw * 1024;
            stage_lin(wsb, lds0, W2T_OFF + (n * 56 + 9) * 4096, 36 * KB, 28, hw, lane);
            stage_lin(wsb, lds0, W2T_OFF + (n * 56 + 36) * 4096, 144 * KB, 8, hw, lane);
#pragma unroll
            for (int j = 22; j < 34; ++j) { buf[j] = ldwt(rs, lane * 16, wsrc + w2_regsp(j) * 4096); SB(); }
            WAITV(21);
            HTS(layer * 20 + 10);
            BAR();
#pragma unroll
            for (int j = 34; j < 47; ++j) { buf[j] = ldwt(rs, lane * 16, wsrc + w2_regsp(j) * 4096); SB(); }
            if (lyr == 0) {
                LAS float* cs1 = (LAS float*)(lds + L_CS1);
                const LAS char* wb = hw < 3 ? lds + hw * 1024 + lane * 16 : lds + 3072 + (h * 16 + (lane & 15)) * 16;
                float cs_part = colsum_frags<11>(wb, [](int i) { return w1_off(i); }, 0.f);
                cs_part = colsum_frags<11>(wb, [](int i) { return w1_off(11 + i); }, cs_part);
                cs_part = colsum_frags<10>(wb, [](int i) { return w1_off(22 + i); }, cs_part);
                const float s = sum_xor32(cs_part);
                if (h == 0 && (hw < 3 || tok < 16)) cs1[hw * 32 + tok] = s;
            }
            WAITV(13);
            BAR();
            HTS(layer * 20 + 11);
#pragma unroll
            for (int j = 0; j < 29; ++j) *(LAS u32x4*)(lds + w2_slot(w2_regsp(j)) + hw * 1024 + lane * 16) = buf[j];
            if (lyr == 0) {
#pragma unroll
                for (int j = 0; j < 24; ++j) { buf[j] = ldwt(rs, lane * 16, WQT_OFF + r * 98304 + (4 * j + hw) * 1024); SB(); }
            }
            HTS(layer * 20 + 12);
            BAR();
        }
        {
            PHASE_VARS(); HELPER_VARS();
            BAR();
            BAR();
#pragma unroll
            for (int j = 29; j < 47; ++j) *(LAS u32x4*)(lds + w2_slot(w2_regsp(j)) + hw * 1024 + lane * 16) = buf[j];
            if (lyr == 1 && blockIdx.x == 0 && w == 7) {
                bool ok = false;
                for (unsigned spins = 0; spins < (1u << 16) && !ok; ++spins) {
                    bool mine = true;
#pragma unroll
                    for (int j = 0; j < 4; ++j) mine &= __hip_atomic_load((gu32*)(p.ws + ACK_OFF) + (lane + 64 * j) * 32, __ATOMIC_RELAXED, __HIP_MEMORY_SCOPE_AGENT) == tag;
                    ok = __all(mine);
                }
                unsigned tg = tag; asm volatile("" : "+s"(tg));
                if (lane == 0) __hip_atomic_store((gu32*)(p.ws + EPOCH_OFF), tg, __ATOMIC_RELAXED, __HIP_MEMORY_SCOPE_AGENT);
            }
            HTS(layer * 20 + 15);
            BAR();
            BAR();
            if (lyr == 0) {
#pragma unroll
                for (int j = 0; j < 24; ++j) *(LAS u32x4*)(lds + L_WQ + (4 * j + hw) * 1024 + lane * 16) = buf[j];
                HTS(17);
                BAR();
            }
        }
    }
    asm volatile("s_waitcnt lgkmcnt(0)" ::: "memory");
    if (w_ == 4) HTS_DUMP();
}

__global__ __launch_bounds__(512, 2) void k_mega(Params p) {
    extern __shared__ __attribute__((aligned(16))) char smem_[];
    LAS char* lds = (LAS char*)smem_;
    const unsigned lds0 = (unsigned)(uintptr_t)smem_;
    const int b = blockIdx.x, idx = b >> 3;
    const int g_ = ((b & 7) >> 1) * 4 + (idx >> 3), r_ = (b & 1) * 8 + (idx & 7);
    const int w = __builtin_amdgcn_readfirstlane(threadIdx.x >> 6);
    const rsrc_t rs = __builtin_amdgcn_make_buffer_rsrc((void*)p.ws, 0, WS_USED, 0x00020000);
    if (w < 4) main_path(p, lds, rs, w, g_, r_);
    else helper_path(p, lds, lds0, rs, w, g_, r_);
}
}

extern "C" void kernel_launch(void* const* d_in, const int* in_sizes, int n_in, void* d_out, int out_size, void* d_ws, size_t ws_size, hipStream_t stream) {
    Params p;
    p.x = (const uint16_t*)d_in[0]; p.wqkv = (const uint16_t*)d_in[1]; p.wo = (const uint16_t*)d_in[2];
    p.w1 = (const uint16_t*)d_in[4]; p.w2 = (const uint16_t*)d_in[5];
    p.out = (uint16_t*)d_out; p.ws = (char*)d_ws;
    (void)hipFuncSetAttribute((const void*)k_mega, hipFuncAttributeMaxDynamicSharedMemorySize, LDS_BYTES);
    k_mega<<<256, 512, LDS_BYTES, stream>>>(p);
}
```

```cpp
#include <hip/hip_runtime.h>
#include <hip/hip_bf16.h>
#include <stdint.h>

#define GAS __attribute__((address_space(1)))
#define LAS __attribute__((address_space(3)))
#define DI __device__ __forceinline__

typedef __bf16 bf16x8 __attribute__((ext_vector_type(8)));
typedef __bf16 bf16x2 __attribute__((ext_vector_type(2)));
typedef short s16x4 __attribute__((ext_vector_type(4)));
typedef float f32x16 __attribute__((ext_vector_type(16)));
typedef float f32x4 __attribute__((ext_vector_type(4)));
typedef float f32x2 __attribute__((ext_vector_type(2)));
typedef unsigned u32x4 __attribute__((ext_vector_type(4)));
typedef unsigned u32x2 __attribute__((ext_vector_type(2)));
typedef GAS unsigned gu32;

#define TS_DECL()
#define TS_PARAM
#define TS_ARG(tb)
#define TS(i)
#define TS_DUMP()
#define HTS(i)
#define HTS_DUMP()
#define MAPMODE 1
namespace {
constexpr int DM = 512, FF = 1792;
constexpr float EPS = 1e-5f;
constexpr float LOG2E = 1.4426950408889634f;
constexpr float QSCALE = 0.25f * LOG2E;

constexpr int MB = 1 << 20, KB = 1024;
constexpr int XF_OFF = 0;
constexpr int O_OFF = 4 * MB;
constexpr int X2_OFF = 8 * MB;
constexpr int H_OFF = 12 * MB;
constexpr int KV_OFF = 28 * MB;
constexpr int WQT_OFF = 36 * MB;
constexpr int WOT_OFF = WQT_OFF + 1536 * KB;
constexpr int W1T_OFF = 38 * MB;
constexpr int W2T_OFF = 40 * MB;
constexpr int ST_OFF = 42 * MB;
constexpr int ST_BUF = 262144;
constexpr int KN_OFF = 43 * MB;
constexpr int FLAG_OFF = 43 * MB + 65536;
constexpr int FLAG_BYTES = 11 * 256 * 128;
constexpr int TMO_OFF = FLAG_OFF + FLAG_BYTES;
constexpr int EPOCH_OFF = TMO_OFF + 256;
constexpr int ACK_OFF = EPOCH_OFF + 256;
constexpr int WS_USED = 44 * MB;

constexpr int L_WQ = 0;
constexpr int L_Q = 0, L_KV = 8 * KB, L_ONES = 91 * KB;
constexpr int L_RED2 = 40 * KB;
constexpr int wo_off(int s) { return s < 31 ? 96 * KB + s * 2048 : 94 * KB; }
constexpr int L_RED4 = 96 * KB;
constexpr int L_SCR = 100 * KB;
constexpr int L_TAB = 158 * KB, L_CSQ = L_TAB, L_CS1 = L_TAB + 512, L_STT = L_TAB + 1024;
constexpr int L_KN = L_TAB + 384, L_QN = L_TAB + 384 + 96;
constexpr int LDS_BYTES = 160 * KB;
constexpr int w1_off(int s) { return s < 10 ? 56 * KB + s * 3584 : s < 21 ? (s - 10) * 3584 : 94 * KB + (s - 21) * 3584; }
constexpr int w2_slot(int sp) { return (sp < 38 ? sp : sp - 38) * 4096; }
constexpr int AUX_SC1 = 16;

struct Params {
    const uint16_t* x; const uint16_t* wqkv; const uint16_t* wo; const uint16_t* w1; const uint16_t* w2;
    uint16_t* out; char* ws;
};

DI int lane_id() { return (int)__builtin_amdgcn_mbcnt_hi(~0u, __builtin_amdgcn_mbcnt_lo(~0u, 0u)); }
DI f32x16 mfma32(bf16x8 a, bf16x8 b, f32x16 c) { return __builtin_amdgcn_mfma_f32_32x32x16_bf16(a, b, c, 0, 0, 0); }
DI unsigned pk(float a, float b) { bf16x2 v = {(__bf16)a, (__bf16)b}; return __builtin_bit_cast(unsigned, v); }
DI float bflo(unsigned u) { return __uint_as_float(u << 16); }
DI float bfhi(unsigned u) { return __uint_as_float(u & 0xffff0000u); }
DI bf16x8 asfrag(u32x4 v) { return __builtin_bit_cast(bf16x8, v); }
DI s16x4 trd(const LAS char* p) { return __builtin_amdgcn_ds_read_tr16_b64_v4i16((LAS s16x4*)p); }
DI u32x4 wfrag_tr(const LAS char* p, int off2) {
    const u32x2 lo = __builtin_bit_cast(u32x2, trd(p)), hi = __builtin_bit_cast(u32x2, trd(p + off2));
    return u32x4{lo.x, lo.y, hi.x, hi.y};
}
DI bf16x8 ldsfrag(const LAS char* p) { return *(const LAS bf16x8*)p; }
template <int N, class F> DI float colsum_frags(const LAS char* p, F off, float acc) {
    u32x4 f[N];
#pragma unroll
    for (int i = 0; i < N; ++i) f[i] = *(const LAS u32x4*)(p + off(i));
    float a1 = 0.f;
#pragma unroll
    for (int i = 0; i < N; ++i) {
        unsigned w0 = f[i].x, w1 = f[i].y, w2 = f[i].z, w3 = f[i].w;
        asm("v_dot2c_f32_bf16 %0, %2, %6\n\tv_dot2c_f32_bf16 %1, %3, %6\n\tv_dot2c_f32_bf16 %0, %4, %6\n\tv_dot2c_f32_bf16 %1, %5, %6"
            : "+v"(acc), "+v"(a1) : "v"(w0), "v"(w1), "v"(w2), "v"(w3), "v"(0x3f803f80u));
    }
    asm volatile("s_nop 2" : "+v"(acc), "+v"(a1));
    return acc + a1;
}
DI float sum_xor32(float v) { auto r = __builtin_amdgcn_permlane32_swap(__float_as_uint(v), __float_as_uint(v), false, false); return __uint_as_float(r[0]) + __uint_as_float(r[1]); }
DI float max_xor32(float v) { auto r = __builtin_amdgcn_permlane32_swap(__float_as_uint(v), __float_as_uint(v), false, false); return fmaxf(__uint_as_float(r[0]), __uint_as_float(r[1])); }
template <bool SC1> DI void glds16s(const void* sbase, unsigned voff, unsigned lds_addr) {
    unsigned keep;
    if (SC1) asm volatile("s_mov_b32 %0, m0\n\ts_mov_b32 m0, %3\n\ts_nop 0\n\tglobal_load_lds_dwordx4 %1, %2 sc1\n\ts_mov_b32 m0, %0" : "=&s"(keep) : "v"(voff), "s"(sbase), "s"(lds_addr) : "memory");
    else asm volatile("s_mov_b32 %0, m0\n\ts_mov_b32 m0, %3\n\ts_nop 0\n\tglobal_load_lds_dwordx4 %1, %2\n\ts_mov_b32 m0, %0" : "=&s"(keep) : "v"(voff), "s"(sbase), "s"(lds_addr) : "memory");
}
#define BAR() asm volatile("s_waitcnt lgkmcnt(0)\n\ts_barrier" ::: "memory")
#define DRAIN() asm volatile("s_waitcnt vmcnt(0)" ::: "memory")
#define WAITV(n) asm volatile("s_waitcnt vmcnt(" #n ")" ::: "memory")
#define SB() __builtin_amdgcn_sched_barrier(0)
#define SGB(mask, n) __builtin_amdgcn_sched_group_barrier(mask, n, 0)

typedef __amdgpu_buffer_rsrc_t rsrc_t;
DI u32x4 ldw(rsrc_t rs, int voff, int soff) { return __builtin_amdgcn_raw_buffer_load_b128(rs, voff, soff, AUX_SC1); }
DI void stw(rsrc_t rs, int voff, int soff, u32x4 v) { __builtin_amdgcn_raw_buffer_store_b128(v, rs, voff + soff, 0, AUX_SC1); }
DI f32x2 ldst(rsrc_t rs, int voff, int soff) { return __builtin_bit_cast(f32x2, __builtin_amdgcn_raw_buffer_load_b64(rs, voff, soff, AUX_SC1)); }
DI void stst(rsrc_t rs, int voff, int soff, f32x2 v) { __builtin_amdgcn_raw_buffer_store_b64(__builtin_bit_cast(u32x2, v), rs, voff, soff, AUX_SC1); }

#define FSTRIDE 32
DI gu32* flagp(const Params& p, int ph, int g, int r) { return (gu32*)(p.ws + FLAG_OFF) + (ph * 16 + g) * 16 * 32 + r * FSTRIDE; }
DI void set_flag(const Params& p, int ph, int g, int r, unsigned tag) { __hip_atomic_store(flagp(p, ph, g, r), tag, __ATOMIC_RELAXED, __HIP_MEMORY_SCOPE_AGENT); }
DI void wait_word(const Params& p, gu32* a, unsigned tag) {
    for (unsigned spins = 0;; ++spins) {
        const unsigned v = __hip_atomic_load(a, __ATOMIC_RELAXED, __HIP_MEMORY_SCOPE_AGENT);
        if (__all(v == tag)) break;
        if (spins > (1u << 20)) { __hip_atomic_store((gu32*)(p.ws + TMO_OFF), 1u, __ATOMIC_RELAXED, __HIP_MEMORY_SCOPE_AGENT); break; }
        __builtin_amdgcn_s_sleep(1);
    }
    __builtin_amdgcn_fence(__ATOMIC_ACQUIRE, "wavefront");
}
DI void wait_group(const Params& p, int ph, int g, unsigned tag, int lane) { wait_word(p, flagp(p, ph, g, lane & 15), tag); }

DI void stats_store(rsrc_t rs, int buf, int T, int u, int lane, const unsigned (&c)[8]) {
    float s = 0.f, ss = 0.f;
#pragma unroll
    for (int i = 0; i < 8; ++i) { float lo = bflo(c[i]), hi = bfhi(c[i]); s += lo + hi; ss += lo * lo + hi * hi; }
    s = sum_xor32(s); ss = sum_xor32(ss);
    if (lane < 32) stst(rs, lane * 8, ST_OFF + buf * ST_BUF + (T * 16 + u) * 256, f32x2{s, ss});
}

DI void stage_lin(const char* wsb, unsigned lds0, int src, int dst, int n, int hw, int lane, int nw = 4) {
    const unsigned vo = lane * 16;
    for (int i = hw; i < n; i += nw) glds16s<true>(wsb + src + i * 1024, vo, lds0 + dst + i * 1024);
}

#define HELPER_VARS() \
    int hw = w - 4; asm volatile("" : "+s"(hw)); \
    const char* wsb = p.ws; asm volatile("" : "+s"(wsb)); \
    unsigned lds0 = lds0_; asm volatile("" : "+s"(lds0)); (void)hw; (void)wsb; (void)lds0;
#define PHASE_VARS() \
    int lane = lane_id(); asm volatile("" : "+v"(lane)); \
    int lyr = layer; asm volatile("" : "+s"(lyr)); \
    int g = g_, r = r_; asm volatile("" : "+s"(g), "+s"(r)); \
    const int h = lane >> 5, tok = lane & 31; const int m = r & 1, n = r >> 1; (void)h; (void)tok; (void)m; (void)n; (void)lyr;

DI void attn_finish(rsrc_t rs, const f32x16& o, int hd, int T, int r, int lyr, int lane) {
    float ov[8]; float l;
    if (hd == 0) { l = o[8];
#pragma unroll
        for (int i = 0; i < 8; ++i) ov[i] = o[i];
    } else { l = o[0];
#pragma unroll
        for (int i = 0; i < 8; ++i) ov[i] = o[8 + i];
    }
    const float inv = __builtin_amdgcn_rcpf(l);
    stw(rs, lane * 16, O_OFF + lyr * 2 * MB + (T * 32 + 2 * r + hd) * 1024,
        u32x4{pk(ov[0] * inv, ov[1] * inv), pk(ov[2] * inv, ov[3] * inv), pk(ov[4] * inv, ov[5] * inv), pk(ov[6] * inv, ov[7] * inv)});
}
DI float max16(float m, const f32x16& s) {
#pragma unroll
    for (int i = 0; i < 16; i += 2) m = fmaxf(fmaxf(m, s[i]), s[i + 1]);
    return m;
}
DI void max16x2(float& m0, float& m1, const f32x16& s) {
#pragma unroll
    for (int i = 0; i < 16; i += 4) { m0 = fmaxf(fmaxf(m0, s[i]), s[i + 1]); m1 = fmaxf(fmaxf(m1, s[i + 2]), s[i + 3]); }
}
DI void exp_pack(const f32x16& s, u32x4& p0, u32x4& p1) {
    unsigned pw[8];
#pragma unroll
    for (int i = 0; i < 8; ++i) pw[i] = pk(__builtin_amdgcn_exp2f(s[2 * i]), __builtin_amdgcn_exp2f(s[2 * i + 1]));
    p0 = u32x4{pw[0], pw[1], pw[2], pw[3]}; p1 = u32x4{pw[4], pw[5], pw[6], pw[7]};
}
DI float wave32_max(float v) {
    int x = __float_as_int(v);
    x = max(x, __builtin_amdgcn_update_dpp(x, x, 0x111, 0xf, 0xf, false));
    x = max(x, __builtin_amdgcn_update_dpp(x, x, 0x112, 0xf, 0xf, false));
    x = max(x, __builtin_amdgcn_update_dpp(x, x, 0x114, 0xf, 0xf, false));
    x = max(x, __builtin_amdgcn_update_dpp(x, x, 0x118, 0xf, 0xf, false));
    x = max(x, __builtin_amdgcn_update_dpp(x, x, 0x142, 0xa, 0xf, false));
    return __int_as_float(__builtin_amdgcn_readlane(x, 31));
}
DI void attn_seg(unsigned ka, unsigned va, int ks, int vs, int n, const bf16x8 qf, const f32x16 c0, f32x16& o) {
    int d;
    asm volatile(
        "ds_read_b128 v[248:251], %[ka]\n\tv_add_u32 %[ka], %[ks], %[ka]\n\tds_read_b128 v[236:239], %[ka]\n\tv_add_u32 %[ka], %[ks], %[ka]\n\t"
        "ds_read_b128 v[240:243], %[va]\n\tds_read_b128 v[244:247], %[va] offset:1024\n\tv_add_u32 %[va], %[vs], %[va]\n\t"
        "s_lshr_b32 %[d], %[n], 1\n\ts_waitcnt lgkmcnt(3)\n\t"
        "v_mfma_f32_32x32x16_bf16 v[204:219], v[248:251], %[q], %[c0]\n\t"
        "s_cmp_eq_u32 %[d], 0\n\ts_nop 10\n\ts_cbranch_scc1 Ltail_%=\n"
        "Lloop_%=:\n\t"
        "v_exp_f32 v204, v204\n\t"
        "v_exp_f32 v205, v205\n\t"
        "v_exp_f32 v206, v206\n\t"
        "v_exp_f32 v207, v207\n\t"
        "v_exp_f32 v208, v208\n\t"
        "v_exp_f32 v209, v209\n\t"
        "v_exp_f32 v210, v210\n\t"
        "v_exp_f32 v211, v211\n\t"
        "v_cvt_pk_bf16_f32 v204, v204, v205\n\t"
        "v_cvt_pk_bf16_f32 v205, v206, v207\n\t"
        "v_cvt_pk_bf16_f32 v206, v208, v209\n\t"
        "v_cvt_pk_bf16_f32 v207, v210, v211\n\t"
        "s_waitcnt lgkmcnt(1)\n\ts_nop 0\n\t"
        "v_mfma_f32_32x32x16_bf16 %[o], v[240:243], v[204:207], %[o]\n\t"
        "v_exp_f32 v212, v212\n\t"
        "v_exp_f32 v213, v213\n\t"
        "v_exp_f32 v214, v214\n\t"
        "v_exp_f32 v215, v215\n\t"
        "v_mfma_f32_32x32x16_bf16 v[220:235], v[236:239], %[q], 0\n\t"
        "ds_read_b128 v[236:239], %[ka]\n\tds_read_b128 v[248:251], %[va]\n\tds_read_b128 v[252:255], %[va] offset:1024\n\t"
        "v_add_u32 %[ka], %[ks], %[ka]\n\tv_add_u32 %[va], %[vs], %[va]\n\t"
        "v_exp_f32 v216, v216\n\t"
        "v_exp_f32 v217, v217\n\t"
        "v_exp_f32 v218, v218\n\t"
        "v_exp_f32 v219, v219\n\t"
        "v_cvt_pk_bf16_f32 v212, v212, v213\n\t"
        "v_cvt_pk_bf16_f32 v213, v214, v215\n\t"
        "v_cvt_pk_bf16_f32 v214, v216, v217\n\t"
        "v_cvt_pk_bf16_f32 v215, v218, v219\n\t"
        "s_waitcnt lgkmcnt(3)\n\ts_nop 0\n\t"
        "v_mfma_f32_32x32x16_bf16 %[o], v[244:247], v[212:215], %[o]\n\t"
        "v_exp_f32 v220, v220\n\t"
        "v_exp_f32 v221, v221\n\t"
        "v_exp_f32 v222, v222\n\t"
        "v_exp_f32 v223, v223\n\t"
        "v_exp_f32 v224, v224\n\t"
        "v_exp_f32 v225, v225\n\t"
        "v_exp_f32 v226, v226\n\t"
        "v_exp_f32 v227, v227\n\t"
        "v_cvt_pk_bf16_f32 v220, v220, v221\n\t"
        "v_cvt_pk_bf16_f32 v221, v222, v223\n\t"
        "v_cvt_pk_bf16_f32 v222, v224, v225\n\t"
        "v_cvt_pk_bf16_f32 v223, v226, v227\n\t"
        "s_waitcnt lgkmcnt(1)\n\ts_nop 0\n\t"
        "v_mfma_f32_32x32x16_bf16 %[o], v[248:251], v[220:223], %[o]\n\t"
        "v_exp_f32 v228, v228\n\t"
        "v_exp_f32 v229, v229\n\t"
        "v_exp_f32 v230, v230\n\t"
        "v_exp_f32 v231, v231\n\t"
        "v_mfma_f32_32x32x16_bf16 v[204:219], v[236:239], %[q], 0\n\t"
        "ds_read_b128 v[236:239], %[ka]\n\tds_read_b128 v[240:243], %[va]\n\tds_read_b128 v[244:247], %[va] offset:1024\n\t"
        "v_add_u32 %[ka], %[ks], %[ka]\n\tv_add_u32 %[va], %[vs], %[va]\n\t"
        "v_exp_f32 v232, v232\n\t"
        "v_exp_f32 v233, v233\n\t"
        "v_exp_f32 v234, v234\n\t"
        "v_exp_f32 v235, v235\n\t"
        "v_cvt_pk_bf16_f32 v228, v228, v229\n\t"
        "v_cvt_pk_bf16_f32 v229, v230, v231\n\t"
        "v_cvt_pk_bf16_f32 v230, v232, v233\n\t"
        "v_cvt_pk_bf16_f32 v231, v234, v235\n\t"
        "s_waitcnt lgkmcnt(3)\n\ts_nop 0\n\t"
        "v_mfma_f32_32x32x16_bf16 %[o], v[252:255], v[228:231], %[o]\n\t"
        "s_sub_u32 %[d], %[d], 1\n\ts_cmp_lg_u32 %[d], 0\n\ts_cbranch_scc1 Lloop_%=\n"
        "Ltail_%=:\n\t"
        "s_bitcmp1_b32 %[n], 0\n\ts_waitcnt lgkmcnt(0)\n\ts_cbranch_scc0 Lend_%=\n\t"
        "v_exp_f32 v204, v204\n\t"
        "v_exp_f32 v205, v205\n\t"
        "v_exp_f32 v206, v206\n\t"
        "v_exp_f32 v207, v207\n\t"
        "v_exp_f32 v208, v208\n\t"
        "v_exp_f32 v209, v209\n\t"
        "v_exp_f32 v210, v210\n\t"
        "v_exp_f32 v211, v211\n\t"
        "v_cvt_pk_bf16_f32 v204, v204, v205\n\t"
        "v_cvt_pk_bf16_f32 v205, v206, v207\n\t"
        "v_cvt_pk_bf16_f32 v206, v208, v209\n\t"
        "v_cvt_pk_bf16_f32 v207, v210, v211\n\t"
        "s_nop 1\n\t"
        "v_mfma_f32_32x32x16_bf16 %[o], v[240:243], v[204:207], %[o]\n\t"
        "v_exp_f32 v212, v212\n\t"
        "v_exp_f32 v213, v213\n\t"
        "v_exp_f32 v214, v214\n\t"
        "v_exp_f32 v215, v215\n\t"
        "v_exp_f32 v216, v216\n\t"
        "v_exp_f32 v217, v217\n\t"
        "v_exp_f32 v218, v218\n\t"
        "v_exp_f32 v219, v219\n\t"
        "v_cvt_pk_bf16_f32 v212, v212, v213\n\t"
        "v_cvt_pk_bf16_f32 v213, v214, v215\n\t"
        "v_cvt_pk_bf16_f32 v214, v216, v217\n\t"
        "v_cvt_pk_bf16_f32 v215, v218, v219\n\t"
        "s_nop 1\n\t"
        "v_mfma_f32_32x32x16_bf16 %[o], v[244:247], v[212:215], %[o]\n"
        "Lend_%=:\n\ts_nop 11"
        : [o] "+v"(o), [ka] "+v"(ka), [va] "+v"(va), [d] "=&s"(d)
        : [q] "v"(qf), [c0] "v"(c0), [ks] "s"(ks), [vs] "v"(vs), [n] "s"(n)
        : "memory", "scc", "v204", "v205", "v206", "v207", "v208", "v209", "v210", "v211", "v212", "v213", "v214", "v215", "v216", "v217", "v218", "v219", "v220", "v221", "v222", "v223", "v224", "v225", "v226", "v227", "v228", "v229", "v230", "v231", "v232", "v233", "v234", "v235", "v236", "v237", "v238", "v239", "v240", "v241", "v242", "v243", "v244", "v245", "v246", "v247", "v248", "v249", "v250", "v251", "v252", "v253", "v254", "v255");
}
constexpr float BOUND2 = 4096.f;
DI f32x16 attn_exact(LAS char* lds, int tw, int hd, int s0, bool first_masked, const bf16x8 qf, int lane) {
    const int h = lane >> 5, tok = lane & 31;
    const LAS char* kb = lds + L_KV + hd * 1024 + lane * 16;
    const bool isdata = ((lane >> 4) & 1) == hd;
    const LAS char* vb = isdata ? (lds + L_KV + 2048 + lane * 16) : (lds + L_ONES);
    const int vstep = isdata ? 4096 : 0;
    const f32x16 zero = {};
    f32x16 o = {};
    float mx = -INFINITY;
    for (int pass = 0; pass < 2; ++pass) {
        for (int sl = s0; sl <= 8 + tw; ++sl) {
            f32x16 s = mfma32(*(const LAS bf16x8*)(kb + sl * 4096), qf, zero);
            const bool diag = sl == 8 + tw, first = first_masked && sl == s0;
#pragma unroll
            for (int i = 0; i < 16; ++i) { const int kk = (i & 3) + 8 * (i >> 2) + 4 * h; if ((diag && kk > tok) || (first && kk <= tok)) s[i] = -INFINITY; }
            if (pass == 0) mx = max16(mx, s);
            else {
#pragma unroll
                for (int i = 0; i < 16; ++i) s[i] -= mx;
                u32x4 p0, p1; exp_pack(s, p0, p1);
                o = mfma32(*(const LAS bf16x8*)(vb + sl * vstep), asfrag(p0), o);
                o = mfma32(*(const LAS bf16x8*)(vb + sl * vstep + 1024), asfrag(p1), o);
            }
        }
        if (pass == 0) mx = max_xor32(mx);
    }
    return o;
}
DI void attn_phase(const Params& p, LAS char* lds, rsrc_t rs, int w, int g, int r, int lyr, int lane, unsigned tag TS_PARAM) {
    const int h = lane >> 5, tok = lane & 31;
    const int hd = w >> 2, tw = hd ? 3 - (w & 3) : (w & 3), T = 4 * g + tw;
    const bf16x8 qf = *(const LAS bf16x8*)(lds + L_Q + tw * 2048 + hd * 1024 + lane * 16);
    const unsigned kb = (unsigned)(uintptr_t)(lds + L_KV + hd * 1024 + lane * 16);
    const bool isdata = ((lane >> 4) & 1) == hd;
    const unsigned vb = (unsigned)(uintptr_t)(isdata ? (lds + L_KV + 2048 + lane * 16) : (lds + L_ONES));
    const int vstep = isdata ? 4096 : 0;
    const LAS float* kn = (const LAS float*)(lds + L_KN);
    const float qn = ((const LAS float*)(lds + L_QN))[tw * 2 + hd] * 1.05f;
    const float kown = fmaxf(fmaxf(kn[16 + hd], kn[18 + hd]), fmaxf(kn[20 + hd], kn[22 + hd]));
    bool fast = __builtin_amdgcn_readfirstlane(qn * kown <= BOUND2);
    f32x16 t, o = {};
#pragma unroll
    for (int i = 0; i < 16; ++i) t[i] = __builtin_fmaf((float)(tok - 4 * h), 1e30f, (0.5f - (float)((i & 3) + 8 * (i >> 2))) * 1e30f);
    const int s4 = 4 * hd, tile0 = 4 * g - 8 + s4;
    const bool loader = tw == 0 && tile0 >= 0;
    u32x4 hv[16]; f32x2 hn;
    if (loader) {
        wait_word(p, flagp(p, 1 + 5 * lyr, g - 2 + hd, r), tag);
        TS(tb + 1);
        const int kvo = KV_OFF + lyr * 4 * MB + (tile0 * 16 + r) * 4096;
#pragma unroll
        for (int i = 0; i < 16; ++i) hv[i] = ldw(rs, lane * 16 + (i & 3) * 1024, kvo + (i >> 2) * 65536);
        hn = ldst(rs, (lane & 3) * 128, KN_OFF + ((lyr * 64 + tile0) * 16 + r) * 8);
        SB();
    }
    if (fast) {
        f32x16 m;
#pragma unroll
        for (int i = 0; i < 16; ++i) m[i] = fminf(t[i], 0.f);
        attn_seg(kb + (8 + tw) * 4096, vb + (8 + tw) * vstep, -4096, -vstep, tw + 1, qf, m, o);
    }
    TS(tb);
    if (loader) {
        LAS char* kvl = lds + L_KV + s4 * 4096 + lane * 16;
#pragma unroll
        for (int i = 0; i < 16; ++i) *(LAS u32x4*)(kvl + i * 1024) = hv[i];
        if (lane < 4) *(LAS f32x2*)(lds + L_KN + (s4 + lane) * 8) = hn;
    }
    BAR();
    TS(tb + 2);
    const int s0 = T >= 8 ? tw : 8 - 4 * g;
    if (s0 < 8) {
        float kh = 0.f;
#pragma unroll
        for (int j = 0; j < 8; ++j) kh = fmaxf(kh, j >= 8 - 4 * g ? kn[2 * j + hd] : 0.f);
        fast = fast && __builtin_amdgcn_readfirstlane(qn * kh <= BOUND2);
        if (fast) {
            f32x16 m;
#pragma unroll
            for (int i = 0; i < 16; ++i) m[i] = T >= 8 ? fminf(-t[i], 0.f) : 0.f;
            attn_seg(kb + s0 * 4096, vb + s0 * vstep, 4096, vstep, 8 - s0, qf, m, o);
        }
    }
    if (!fast) o = attn_exact(lds, tw, hd, s0, T >= 8, qf, lane);
    attn_finish(rs, o, hd, T, r, lyr, lane);
}
DI void ln_stats(const f32x2 (&sv)[8], float& mean, float& rstd) {
    float S = 0.f, SS = 0.f;
#pragma unroll
    for (int i = 0; i < 8; ++i) { S += sv[i].x; SS += sv[i].y; }
    S = sum_xor32(S); SS = sum_xor32(SS);
    mean = S * (1.f / DM); const float var = fmaxf(SS * (1.f / DM) - mean * mean, 0.f); rstd = __builtin_amdgcn_rsqf(var + EPS);
}
DI unsigned read_tag(const Params& p) {
    return (unsigned)__builtin_amdgcn_readfirstlane(__hip_atomic_load((gu32*)(p.ws + EPOCH_OFF), __ATOMIC_RELAXED, __HIP_MEMORY_SCOPE_AGENT)) + 1u;
}
#define LDA(arr, s, base) arr[s] = ldw(rs, lane * 16 + ((s) & 3) * 1024, (base) + ((s) >> 2) * 4096)

#define WODELAY 36
#define PD0 4
#define PDV 12
constexpr int a_issued(int s, int total) { int v = PD0 + 2 * (s + 1); const int cap = s + 1 + PDV; v = v < cap ? v : cap; return v < total ? v : total; }

DI void main_path(const Params& p, LAS char* lds, rsrc_t rs, const int w, const int g_, const int r_) {
    unsigned tag = 0;
    TS_DECL();
    TS(0);
    {
        const int layer = 0; PHASE_VARS();
        const int T = 4 * g + (r & 3), u = (r >> 2) * 4 + w;
        const uint16_t* row = p.x + (size_t)(T * 32 + tok) * DM + 32 * u + 4 * h;
        u32x2 a0 = *(const GAS u32x2*)(row), a1 = *(const GAS u32x2*)(row + 8), a2 = *(const GAS u32x2*)(row + 16), a3 = *(const GAS u32x2*)(row + 24);
        tag = read_tag(p); asm volatile("" :: "s"(tag) : "memory");
        stw(rs, lane * 16, XF_OFF + (T * 32 + 2 * u) * 1024, u32x4{a0.x, a0.y, a1.x, a1.y});
        stw(rs, lane * 16, XF_OFF + (T * 32 + 2 * u + 1) * 1024, u32x4{a2.x, a2.y, a3.x, a3.y});
        const unsigned c[8] = {a0.x, a0.y, a1.x, a1.y, a2.x, a2.y, a3.x, a3.y};
        stats_store(rs, 0, T, u, lane, c);
        DRAIN();
        BAR();
        if (w == 0 && lane == 0) { set_flag(p, 0, g, r, tag); __hip_atomic_store((gu32*)(p.ws + ACK_OFF) + blockIdx.x * 32, tag, __ATOMIC_RELAXED, __HIP_MEMORY_SCOPE_AGENT); }
    }
    TS(1);
    for (int layer = 0; layer < 2; ++layer) {
        const int tb = 2 + layer * 24; (void)tb;
        {
            PHASE_VARS();
            if (w == 0) {
                if (lyr == 0) {
                    const int j = lane & 15;
                    wait_word(p, (lane & 16) ? flagp(p, 0, j, r) : flagp(p, 0, g, j), tag);
                } else wait_group(p, 5, g, tag, lane);
            }
            BAR();
            TS(tb + 0);
            const int T = 4 * g + w;
            const LAS char* wb = lds + L_WQ + lane * 16;
            LAS float* csq = (LAS float*)(lds + L_CSQ);
            LAS float* stt = (LAS float*)(lds + L_STT);
            f32x16 aq = {}, ak = {}, av = {};
            u32x4 xa[32];
            f32x2 sv[8];
            const int sto = ST_OFF + 2 * lyr * ST_BUF + T * 4096, xo = XF_OFF + lyr * 2 * MB + T * 32768;
#pragma unroll
            for (int s = 0; s < PD0; ++s) { LDA(xa, s, xo); SB(); }
            if (lyr == 0) BAR();
            bf16x8 wf[3][3];
#pragma unroll
            for (int s = 0; s < 2; ++s)
#pragma unroll
                for (int t = 0; t < 3; ++t) wf[s][t] = ldsfrag(wb + (s * 3 + t) * 1024);
#pragma unroll
            for (int s = 0; s < 32; ++s) {
                if ((s == 6 || s == 14 || s == 22) && lyr == 0) BAR();
#pragma unroll
                for (int j = (s == 0 ? PD0 : a_issued(s - 1, 32)); j < a_issued(s, 32); ++j) LDA(xa, j, xo);
                if (s >= 16 && s < 24) sv[s - 16] = ldst(rs, (8 * h * 32 + tok) * 8 + (s - 16) * 256, sto);
                if (s + 2 < 32) {
#pragma unroll
                    for (int t = 0; t < 3; ++t) wf[(s + 2) % 3][t] = ldsfrag(wb + ((s + 2) * 3 + t) * 1024);
                }
                const bf16x8 x = asfrag(xa[s]);
                aq = mfma32(wf[s % 3][0], x, aq); ak = mfma32(wf[s % 3][1], x, ak); av = mfma32(x, wf[s % 3][2], av);
                SGB(0x100, 1); SGB(0x8, 1); SGB(0x20, 1); SGB(0x100, 1); SGB(0x8, 1); SGB(0x20, 1); SGB(0x100, 1); SGB(0x8, 1);
                SB();
            }
            float mean, rstd; ln_stats(sv, mean, rstd);
            if (h == 0) { stt[w * 64 + tok] = rstd; stt[w * 64 + 32 + tok] = mean * rstd; }
            TS(tb + 1);
            BAR();
            TS(tb + 2);
            const float mr = mean * rstd;
            float q[16], k[16], v[16];
            const float cv = csq[64 + tok];
#pragma unroll
            for (int g4 = 0; g4 < 4; ++g4) {
                f32x4 cq = *(LAS f32x4*)(csq + 8 * g4 + 4 * h), ck = *(LAS f32x4*)(csq + 32 + 8 * g4 + 4 * h);
                f32x4 rsv = *(LAS f32x4*)(stt + w * 64 + 8 * g4 + 4 * h), ms = *(LAS f32x4*)(stt + w * 64 + 32 + 8 * g4 + 4 * h);
#pragma unroll
                for (int i = 0; i < 4; ++i) {
                    k[4 * g4 + i] = ak[4 * g4 + i] * rstd - mr * ck[i];
                    v[4 * g4 + i] = av[4 * g4 + i] * rsv[i] - ms[i] * cv;
                    q[4 * g4 + i] = (aq[4 * g4 + i] * rstd - mr * cq[i]) * QSCALE;
                }
            }
            const u32x4 k0 = {pk(k[0], k[1]), pk(k[2], k[3]), pk(k[4], k[5]), pk(k[6], k[7])};
            const u32x4 k1 = {pk(k[8], k[9]), pk(k[10], k[11]), pk(k[12], k[13]), pk(k[14], k[15])};
            const u32x4 v0 = {pk(v[0], v[1]), pk(v[2], v[3]), pk(v[4], v[5]), pk(v[6], v[7])};
            const u32x4 v1 = {pk(v[8], v[9]), pk(v[10], v[11]), pk(v[12], v[13]), pk(v[14], v[15])};
            const int kvo = KV_OFF + lyr * 4 * MB + (T * 16 + r) * 4096;
            stw(rs, lane * 16, kvo, k0); stw(rs, lane * 16 + 1024, kvo, k1); stw(rs, lane * 16 + 2048, kvo, v0); stw(rs, lane * 16 + 3072, kvo, v1);
            {
                float na = 0.f, nb = 0.f;
#pragma unroll
                for (int i = 0; i < 8; ++i) { na = __builtin_fmaf(k[i], k[i], na); nb = __builtin_fmaf(k[8 + i], k[8 + i], nb); }
                const f32x2 nn = {wave32_max(sum_xor32(na)), wave32_max(sum_xor32(nb))};
                if (lane == 0) { stst(rs, 0, KN_OFF + ((lyr * 64 + T) * 16 + r) * 8, nn); *(LAS f32x2*)(lds + L_KN + (8 + w) * 8) = nn; }
            }
            LAS char* kvl = lds + L_KV + (8 + w) * 4096 + lane * 16;
            *(LAS u32x4*)(kvl) = k0; *(LAS u32x4*)(kvl + 1024) = k1; *(LAS u32x4*)(kvl + 2048) = v0; *(LAS u32x4*)(kvl + 3072) = v1;
            LAS char* ql = lds + L_Q + w * 2048 + lane * 16;
            *(LAS u32x4*)(ql) = u32x4{pk(q[0], q[1]), pk(q[2], q[3]), pk(q[4], q[5]), pk(q[6], q[7])};
            *(LAS u32x4*)(ql + 1024) = u32x4{pk(q[8], q[9]), pk(q[10], q[11]), pk(q[12], q[13]), pk(q[14], q[15])};
            {
                float na = 0.f, nb = 0.f;
#pragma unroll
                for (int i = 0; i < 8; ++i) { na = __builtin_fmaf(q[i], q[i], na); nb = __builtin_fmaf(q[8 + i], q[8 + i], nb); }
                const f32x2 nn = {wave32_max(sum_xor32(na)), wave32_max(sum_xor32(nb))};
                if (lane == 0) *(LAS f32x2*)(lds + L_QN + w * 8) = nn;
            }
            DRAIN();
            BAR();
            if (w == 0 && lane == 0) set_flag(p, 1 + 5 * lyr, g, r, tag);
            TS(tb + 3);
        }
        {
            PHASE_VARS();
            attn_phase(p, lds, rs, w, g, r, lyr, lane, tag TS_ARG(layer == 0 ? 22 : 48));
            TS(tb + 4); TS(tb + 5); TS(tb + 6);
            DRAIN();
            BAR();
            if (w == 0 && lane == 0) set_flag(p, 2 + 5 * lyr, g, r, tag);
            TS(tb + 7);
        }
        {
            PHASE_VARS();
            const int mt = w & 1, kh = (w >> 1) & 1, T = 4 * g + 2 * m + mt, u = 2 * n + kh;
            const int xo = XF_OFF + lyr * 2 * MB + (T * 32 + 2 * u) * 1024;
            const u32x4 r0 = ldw(rs, lane * 16, xo), r1 = ldw(rs, lane * 16 + 1024, xo);
            if (w == 0) wait_group(p, 2 + 5 * lyr, g, tag, lane);
            BAR();
            TS(tb + 8);
            f32x16 acc[2] = {};
            u32x4 xa[16];
            const int oo = O_OFF + lyr * 2 * MB + (T * 32 + 16 * kh) * 1024;
#pragma unroll
            for (int s = 0; s < 16; ++s) { LDA(xa, s, oo); SB(); }
            const LAS char* wb = lds + 96 * KB + kh * 32768 + lane * 16;
            const LAS char* wlast = lds + (kh ? 94 * KB : 96 * KB + 15 * 2048) + lane * 16;
            bf16x8 wf[4][2];
#pragma unroll
            for (int s = 0; s < 3; ++s) { wf[s][0] = ldsfrag(wb + s * 2048); wf[s][1] = ldsfrag(wb + s * 2048 + 1024); }
#pragma unroll
            for (int s = 0; s < 16; ++s) {
                if (s + 3 < 15) { wf[(s + 3) & 3][0] = ldsfrag(wb + (s + 3) * 2048); wf[(s + 3) & 3][1] = ldsfrag(wb + (s + 3) * 2048 + 1024); }
                if (s + 3 == 15) { wf[3][0] = ldsfrag(wlast); wf[3][1] = ldsfrag(wlast + 1024); }
                const bf16x8 x = asfrag(xa[s]);
                acc[0] = mfma32(wf[s & 3][0], x, acc[0]); acc[1] = mfma32(wf[s & 3][1], x, acc[1]);
                SGB(0x100, 1); SGB(0x8, 1); SGB(0x20, 2); SGB(0x100, 1); SGB(0x8, 1);
                SB();
            }
            const f32x16 snd = kh ? acc[0] : acc[1];
#pragma unroll
            for (int g4 = 0; g4 < 4; ++g4) *(LAS f32x4*)(lds + L_RED2 + ((w * 4 + g4) * 64 + lane) * 16) = f32x4{snd[4 * g4], snd[4 * g4 + 1], snd[4 * g4 + 2], snd[4 * g4 + 3]};
            TS(tb + 9);
            BAR();
            const f32x16 own = kh ? acc[1] : acc[0];
            const int pw = w ^ 2;
            float v[16];
#pragma unroll
            for (int g4 = 0; g4 < 4; ++g4) { f32x4 t = *(LAS f32x4*)(lds + L_RED2 + ((pw * 4 + g4) * 64 + lane) * 16);
#pragma unroll
                for (int i = 0; i < 4; ++i) v[4 * g4 + i] = own[4 * g4 + i] + t[i]; }
            const unsigned rr[8] = {r0.x, r0.y, r0.z, r0.w, r1.x, r1.y, r1.z, r1.w};
            unsigned c[8];
#pragma unroll
            for (int i = 0; i < 8; ++i) c[i] = pk(v[2 * i] + bflo(rr[i]), v[2 * i + 1] + bfhi(rr[i]));
            const int yo = X2_OFF + lyr * 2 * MB + (T * 32 + 2 * u) * 1024;
            stw(rs, lane * 16, yo, u32x4{c[0], c[1], c[2], c[3]});
            stw(rs, lane * 16 + 1024, yo, u32x4{c[4], c[5], c[6], c[7]});
            stats_store(rs, 2 * lyr + 1, T, u, lane, c);
            DRAIN();
            BAR();
            if (w == 0 && lane == 0) set_flag(p, 3 + 5 * lyr, g, r, tag);
            TS(tb + 10);
        }
        {
            PHASE_VARS();
            if (w == 0) wait_group(p, 3 + 5 * lyr, g, tag, lane);
            BAR();
            TS(tb + 11);
            const int T = 4 * g + w;
            const LAS char* wbA = lds + lane * 16;
            const LAS char* wbB = lds + 3072 + (h * 16 + (lane & 15)) * 16;
            LAS float* cs1 = (LAS float*)(lds + L_CS1);
            f32x16 acc[4] = {};
            u32x4 xa[32];
            f32x2 sv[8];
            const int sto = ST_OFF + (2 * lyr + 1) * ST_BUF + T * 4096, xo = X2_OFF + lyr * 2 * MB + T * 32768;
#pragma unroll
            for (int s = 0; s < PD0; ++s) { LDA(xa, s, xo); SB(); }
            auto ldW = [&](int s, int t) { return t < 3 ? ldsfrag(wbA + w1_off(s) + t * 1024) : ldsfrag(wbB + w1_off(s)); };
            bf16x8 wf[2][4];
#pragma unroll
            for (int t = 0; t < 4; ++t) wf[0][t] = ldW(0, t);
#pragma unroll
            for (int s = 0; s < 32; ++s) {
                if (s == 20) BAR();
#pragma unroll
                for (int j = (s == 0 ? PD0 : a_issued(s - 1, 32)); j < a_issued(s, 32); ++j) LDA(xa, j, xo);
                if (s >= 16 && s < 24) sv[s - 16] = ldst(rs, (8 * h * 32 + tok) * 8 + (s - 16) * 256, sto);
                const bf16x8 x = asfrag(xa[s]);
#pragma unroll
                for (int t = 0; t < 4; ++t) {
                    if (s + 1 < 32) wf[(s + 1) & 1][t] = ldW(s + 1, t);
                    acc[t] = mfma32(wf[s & 1][t], x, acc[t]);
                }
                SGB(0x100, 1); SGB(0x8, 1); SGB(0x20, 1); SGB(0x100, 1); SGB(0x8, 1); SGB(0x20, 1); SGB(0x100, 1); SGB(0x8, 1); SGB(0x100, 1); SGB(0x8, 1);
                SB();
            }
            float mean, rstd; ln_stats(sv, mean, rstd);
            const float mr = mean * rstd;
            TS(tb + 12);
            BAR();
            TS(tb + 13);
            const int ho = H_OFF + lyr * 8 * MB + (T * 112 + 7 * r) * 1024;
#pragma unroll
            for (int t = 0; t < 4; ++t) {
#pragma unroll
                for (int sp = 0; sp < (t < 3 ? 2 : 1); ++sp) {
                    float hv[8];
#pragma unroll
                    for (int g2 = 0; g2 < 2; ++g2) {
                        const f32x4 c4 = *(LAS f32x4*)(cs1 + t * 32 + 16 * sp + 8 * g2 + 4 * h);
#pragma unroll
                        for (int i = 0; i < 4; ++i) {
                            const float a = acc[t][8 * sp + 4 * g2 + i];
                            const float z = a * rstd - mr * c4[i];
                            hv[4 * g2 + i] = z * __builtin_amdgcn_rcpf(1.f + __builtin_amdgcn_exp2f(-LOG2E * z));
                        }
                    }
                    stw(rs, lane * 16 + (2 * t + sp) * 1024, ho, u32x4{pk(hv[0], hv[1]), pk(hv[2], hv[3]), pk(hv[4], hv[5]), pk(hv[6], hv[7])});
                }
            }
            DRAIN();
            BAR();
            if (w == 0 && lane == 0) set_flag(p, 4 + 5 * lyr, g, r, tag);
            TS(tb + 14);
        }
        {
            PHASE_VARS();
            const int mt = w & 1, kh = (w >> 1) & 1, T = 4 * g + 2 * m + mt, u = 2 * n + kh;
            const int yo = X2_OFF + lyr * 2 * MB + (T * 32 + 2 * u) * 1024;
            const u32x4 r0 = ldw(rs, lane * 16, yo), r1 = ldw(rs, lane * 16 + 1024, yo);
            if (w == 0) wait_group(p, 4 + 5 * lyr, g, tag, lane);
            BAR();
            TS(tb + 15);
            const LAS char* wb = lds + kh * 2048 + lane * 16;
            f32x16 acc[2] = {};
            const int ho = H_OFF + lyr * 8 * MB + (T * 112 + 56 * kh) * 1024;
            u32x4 xa[56];
#pragma unroll
            for (int s = 0; s < PD0; ++s) { LDA(xa, s, ho); SB(); }
            auto woff = [](int s) { return w2_slot(s); };
            bf16x8 wf[4][2];
#pragma unroll
            for (int s = 0; s < 3; ++s) { wf[s][0] = ldsfrag(wb + woff(s)); wf[s][1] = ldsfrag(wb + woff(s) + 1024); }
#pragma unroll
            for (int s = 0; s < 56; ++s) {
                if (s == 18) { BAR(); TS(tb + 16); }
                if (s == 35) { BAR(); TS(tb + 17); }
#pragma unroll
                for (int j = (s == 0 ? PD0 : a_issued(s - 1, 56)); j < a_issued(s, 56); ++j) LDA(xa, j, ho);
                if (s + 3 < 56) { wf[(s + 3) & 3][0] = ldsfrag(wb + woff(s + 3)); wf[(s + 3) & 3][1] = ldsfrag(wb + woff(s + 3) + 1024); }
                const bf16x8 x = asfrag(xa[s]);
                acc[0] = mfma32(wf[s & 3][0], x, acc[0]); acc[1] = mfma32(wf[s & 3][1], x, acc[1]);
                SGB(0x100, 1); SGB(0x8, 1); SGB(0x20, 2); SGB(0x100, 1); SGB(0x8, 1);
                SB();
            }
            const f32x16 snd = kh ? acc[0] : acc[1];
#pragma unroll
            for (int g4 = 0; g4 < 4; ++g4) *(LAS f32x4*)(lds + L_RED4 + ((w * 4 + g4) * 64 + lane) * 16) = f32x4{snd[4 * g4], snd[4 * g4 + 1], snd[4 * g4 + 2], snd[4 * g4 + 3]};
            TS(tb + 18);
            BAR();
            const f32x16 own = kh ? acc[1] : acc[0];
            const int pw = w ^ 2;
            float v[16];
#pragma unroll
            for (int g4 = 0; g4 < 4; ++g4) { f32x4 t = *(LAS f32x4*)(lds + L_RED4 + ((pw * 4 + g4) * 64 + lane) * 16);
#pragma unroll
                for (int i = 0; i < 4; ++i) v[4 * g4 + i] = own[4 * g4 + i] + t[i]; }
            const unsigned rr[8] = {r0.x, r0.y, r0.z, r0.w, r1.x, r1.y, r1.z, r1.w};
            unsigned c[8];
#pragma unroll
            for (int i = 0; i < 8; ++i) c[i] = pk(v[2 * i] + bflo(rr[i]), v[2 * i + 1] + bfhi(rr[i]));
            if (lyr == 0) {
                stw(rs, lane * 16, XF_OFF + 2 * MB + (T * 32 + 2 * u) * 1024, u32x4{c[0], c[1], c[2], c[3]});
                stw(rs, lane * 16 + 1024, XF_OFF + 2 * MB + (T * 32 + 2 * u) * 1024, u32x4{c[4], c[5], c[6], c[7]});
                stats_store(rs, 2, T, u, lane, c);
                DRAIN();
                BAR();
                if (w == 0 && lane == 0) set_flag(p, 5, g, r, tag);
            } else {
                auto s0 = __builtin_amdgcn_permlane32_swap(c[0], c[2], false, false);
                auto s1 = __builtin_amdgcn_permlane32_swap(c[1], c[3], false, false);
                auto s2 = __builtin_amdgcn_permlane32_swap(c[4], c[6], false, false);
                auto s3 = __builtin_amdgcn_permlane32_swap(c[5], c[7], false, false);
                uint16_t* orow = p.out + (size_t)(T * 32 + (lane & 31)) * DM + 32 * u + 8 * h;
                *(GAS u32x4*)(orow) = u32x4{s0[0], s1[0], s0[1], s1[1]};
                *(GAS u32x4*)(orow + 16) = u32x4{s2[0], s3[0], s2[1], s3[1]};
            }
            TS(tb + 19);
        }
    }
    asm volatile("s_waitcnt lgkmcnt(0)" ::: "memory");
    if (w == 0) TS_DUMP();
}

template <int PART> DI void pretile(const Params& p, LAS char* lds, unsigned lds0, rsrc_t rs, int hw, int g, int r, int lane) {
    const int h = lane >> 5, a = (lane & 15) >> 2, pp = lane & 3, ch = (lane >> 4) & 1;
    const int n = r >> 1, j = 2 * g + (r & 1);
    const unsigned scr0 = lds0 + L_SCR + hw * 8192;
    const LAS char* scr = lds + L_SCR + hw * 8192;
    if (hw == 0) {
        const char* base = (const char*)(p.wqkv + (size_t)32 * g * 1536 + 32 * r);
        if (PART == 0) {
#pragma unroll
            for (int i = 0; i < 6; ++i) { const int L = 64 * i + lane, k = L / 12, c = L % 12; glds16s<false>(base, (unsigned)(k * 1536 + (c >> 2) * 512 + (c & 3) * 8) * 2, scr0 + i * 1024); }
            return;
        }
        DRAIN();
        const LAS char* wb = scr + (4 * h + a) * 192 + ch * 32 + pp * 8;
#pragma unroll
        for (int s = 0; s < 2; ++s)
#pragma unroll
            for (int t = 0; t < 3; ++t) stw(rs, lane * 16, WQT_OFF + ((r * 32 + 2 * g + s) * 3 + t) * 1024, wfrag_tr(wb + s * 16 * 192 + t * 64, 8 * 192));
    } else if (hw == 1) {
        const char* base = (const char*)(p.w1 + (size_t)32 * g * FF + 112 * r);
        if (PART == 0) {
#pragma unroll
            for (int b = 0; b < 7; ++b) {
                const int L = 64 * b + lane, G = L / 56, x = L - G * 56;
                const int aa = x < 48 ? ((x >> 2) & 3) : ((x - 48) >> 1);
                const int c = x < 48 ? ((x >> 4) * 4 + (x & 3)) : (12 + (x & 1));
                glds16s<false>(base, (unsigned)((4 * G + aa) * FF + c * 8) * 2, scr0 + b * 1024);
            }
            return;
        }
        DRAIN();
        const LAS char* wbA = scr + h * 896 + a * 64 + ch * 32 + pp * 8;
        const LAS char* wbB = scr + h * 896 + 768 + a * 32 + pp * 8;
#pragma unroll
        for (int s = 0; s < 2; ++s) {
            const int dst = W1T_OFF + r * 114688 + (2 * g + s) * 3584;
#pragma unroll
            for (int t = 0; t < 3; ++t) stw(rs, lane * 16, dst + t * 1024, wfrag_tr(wbA + s * 3584 + t * 256, 1792));
            const u32x4 f3 = wfrag_tr(wbB + s * 3584, 1792);
            if (ch == 0) stw(rs, (h * 16 + (lane & 15)) * 16, dst + 3072, f3);
        }
    } else {
        const int rr = lane >> 3, slot = lane & 7, chunk = slot ^ (((rr >> 1) & 1) << 2);
        const unsigned vo = (unsigned)(rr * DM + chunk * 8) * 2;
        const int s0 = hw == 3 ? j : (j < 16 ? 4 * j : 64 + 3 * (j - 16)), ns = hw == 3 ? 1 : (j < 16 ? 4 : 3);
        const char* base = (const char*)((hw == 3 ? p.wo : p.w2) + (size_t)16 * s0 * DM + 64 * n);
        if (PART == 0) {
            for (int i = 0; i < 2 * ns; ++i) glds16s<false>(base + (size_t)i * 8 * DM * 2, vo, scr0 + i * 1024);
            return;
        }
        DRAIN();
        const int sw = ((a >> 1) & 1) << 6;
        const LAS char* wb0 = scr + (4 * h + a) * 128 + (0 ^ sw) + ch * 32 + pp * 8;
        const LAS char* wb1 = scr + (4 * h + a) * 128 + (64 ^ sw) + ch * 32 + pp * 8;
        for (int i = 0; i < ns; ++i) {
            const int s = s0 + i;
            const int kh = s >= 56 ? 1 : 0, sl = s - 56 * kh;
            const int dst = hw == 3 ? WOT_OFF + n * 65536 + s * 2048 : W2T_OFF + (n * 56 + sl) * 4096 + kh * 2048;
            stw(rs, lane * 16, dst, wfrag_tr(wb0 + i * 2048, 1024));
            stw(rs, lane * 16, dst + 1024, wfrag_tr(wb1 + i * 2048, 1024));
        }
    }
    DRAIN();
}

constexpr int w2_regsp(int j) { return j < 10 ? j : j < 30 ? j + 4 : j + 8; }
DI void helper_path(const Params& p, LAS char* lds, unsigned lds0_, rsrc_t rs, const int w, const int g_, const int r_) {
    unsigned tag = 0;
    TS_DECL();
    {   const int layer = 0; PHASE_VARS(); HELPER_VARS();
        tag = read_tag(p); asm volatile("" :: "s"(tag) : "memory");
        if (hw == 3) {
            unsigned keep;
            const unsigned vo = (unsigned)(lane < 44 ? lane : 43) << 20;
            asm volatile("s_mov_b32 %0, m0\n\ts_mov_b32 m0, %3\n\ts_nop 0\n\tglobal_load_lds_dword %1, %2\n\ts_mov_b32 m0, %0" : "=&s"(keep) : "v"(vo), "s"(p.ws), "s"(lds0 + 140 * KB) : "memory");
            const unsigned vo2 = (unsigned)(lane & 1) << 20;
            asm volatile("s_mov_b32 %0, m0\n\ts_mov_b32 m0, %3\n\ts_nop 0\n\tglobal_load_lds_dword %1, %2\n\ts_mov_b32 m0, %0" : "=&s"(keep) : "v"(vo2), "s"(p.out), "s"(lds0 + 141 * KB) : "memory");
        }
        pretile<0>(p, lds, lds0, rs, hw, g, r, lane);
        if (hw == 0) { pretile<1>(p, lds, lds0, rs, hw, g, r, lane); BAR(); }
        else { BAR(); pretile<1>(p, lds, lds0, rs, hw, g, r, lane); }
    }
    for (int layer = 0; layer < 2; ++layer) {
        {
            PHASE_VARS(); HELPER_VARS();
            HTS(layer * 20 + 0);
            BAR();
            if (lyr == 0) {
                if (w == 6 && lane == 0) set_flag(p, 10, g, r, tag);
                stage_lin(wsb, lds0, WQT_OFF + r * 98304, L_WQ, 96, hw, lane);
                float acc = 0.f;
                const LAS char* wbq = lds + L_WQ + (hw < 3 ? hw : 0) * 1024 + lane * 16;
                WAITV(18); BAR();
                acc = colsum_frags<8>(wbq, [](int i) { return (0 + i) * 3072; }, acc);
                WAITV(12); BAR();
                acc = colsum_frags<8>(wbq, [](int i) { return (8 + i) * 3072; }, acc);
                WAITV(6); BAR();
                acc = colsum_frags<8>(wbq, [](int i) { return (16 + i) * 3072; }, acc);
                DRAIN(); BAR();
                acc = colsum_frags<8>(wbq, [](int i) { return (24 + i) * 3072; }, acc);
                acc = sum_xor32(acc);
                if (h == 0 && hw < 3) ((LAS float*)(lds + L_CSQ))[hw * 32 + tok] = acc;
                wait_word(p, flagp(p, 10, lane & 15, (lane & 16) ? (r ^ 1) : r), tag);
            } else {
                __builtin_amdgcn_s_sleep(WODELAY);
                for (int i = hw; i < 62; i += 4) glds16s<true>(wsb + WOT_OFF + n * 65536 + i * 1024, lane * 16, lds0 + 96 * KB + i * 1024);
            }
            BAR();
            if (w == 7 && lane < 2) *(LAS u32x4*)(lds + L_ONES + lane * 1024) = u32x4{0x3f803f80u, 0x3f803f80u, 0x3f803f80u, 0x3f803f80u};
            HTS(layer * 20 + 2);
            stage_lin(wsb, lds0, WOT_OFF + n * 65536 + 62 * KB, 94 * KB, 2, hw, lane);
            stage_lin(wsb, lds0, W1T_OFF + r * 114688, 56 * KB, 35, hw, lane);
            HTS(layer * 20 + 3);
            BAR();
            if (lyr == 0) stage_lin(wsb, lds0, WOT_OFF + n * 65536, 96 * KB, 62, hw, lane);
        }
        u32x4 buf[48];
        {   PHASE_VARS(); HELPER_VARS();

            attn_phase(p, lds, rs, w, g, r, lyr, lane, tag TS_ARG(layer == 0 ? 40 : 44));
            HTS(layer * 20 + 4);
            DRAIN();
            HTS(layer * 20 + 5);
            BAR();
            stage_lin(wsb, lds0, W1T_OFF + r * 114688 + 35 * KB, 0, 39, hw, lane);
            HTS(layer * 20 + 6);
        }
        {
            PHASE_VARS(); HELPER_VARS();
            const int wsrc = W2T_OFF + n * 56 * 4096 + hw * 1024;
            BAR();
#pragma unroll
            for (int j = 0; j < 12; ++j) { buf[j] = ldw(rs, lane * 16, wsrc + w2_regsp(j) * 4096); SB(); }
            BAR();
#pragma unroll
            for (int j = 12; j < 22; ++j) { buf[j] = ldw(rs, lane * 16, wsrc + w2_regsp(j) * 4096); SB(); }
            BAR();
            stage_lin(wsb, lds0, W1T_OFF + r * 114688 + 75264, 94 * KB, 39, hw, lane);
        }
        {
            PHASE_VARS(); HELPER_VARS();
            HTS(layer * 20 + 8);
            WAITV(31);
            BAR();
            const int wsrc = W2T_OFF + n * 56 * 4096 + hw * 1024;
            stage_lin(wsb, lds0, W2T_OFF + (n * 56 + 10) * 4096, 40 * KB, 16, hw, lane);
            stage_lin(wsb, lds0, W2T_OFF + (n * 56 + 34) * 4096, 136 * KB, 16, hw, lane);
#pragma unroll
            for (int j = 22; j < 34; ++j) { buf[j] = ldw(rs, lane * 16, wsrc + w2_regsp(j) * 4096); SB(); }
            WAITV(20);
            HTS(layer * 20 + 10);
            BAR();
#pragma unroll
            for (int j = 34; j < 48; ++j) { buf[j] = ldw(rs, lane * 16, wsrc + w2_regsp(j) * 4096); SB(); }
            if (lyr == 0) {
                LAS float* cs1 = (LAS float*)(lds + L_CS1);
                const LAS char* wb = hw < 3 ? lds + hw * 1024 + lane * 16 : lds + 3072 + (h * 16 + (lane & 15)) * 16;
                float cs_part = colsum_frags<11>(wb, [](int i) { return w1_off(i); }, 0.f);
                cs_part = colsum_frags<11>(wb, [](int i) { return w1_off(11 + i); }, cs_part);
                cs_part = colsum_frags<10>(wb, [](int i) { return w1_off(22 + i); }, cs_part);
                const float s = sum_xor32(cs_part);
                if (h == 0 && (hw < 3 || tok < 16)) cs1[hw * 32 + tok] = s;
            }
            WAITV(14);
            BAR();
            HTS(layer * 20 + 11);
#pragma unroll
            for (int j = 0; j < 30; ++j) *(LAS u32x4*)(lds + w2_slot(w2_regsp(j)) + hw * 1024 + lane * 16) = buf[j];
            if (lyr == 0) {
#pragma unroll
                for (int j = 0; j < 24; ++j) { buf[j] = ldw(rs, lane * 16, WQT_OFF + r * 98304 + (4 * j + hw) * 1024); SB(); }
            }
            HTS(layer * 20 + 12);
            BAR();
        }
        {
            PHASE_VARS(); HELPER_VARS();
            BAR();
            BAR();
#pragma unroll
            for (int j = 30; j < 48; ++j) *(LAS u32x4*)(lds + w2_slot(w2_regsp(j)) + hw * 1024 + lane * 16) = buf[j];
            if (lyr == 1 && blockIdx.x == 0 && w == 7) {
                bool ok = false;
                for (unsigned spins = 0; spins < (1u << 16) && !ok; ++spins) {
                    bool mine = true;
#pragma unroll
                    for (int j = 0; j < 4; ++j) mine &= __hip_atomic_load((gu32*)(p.ws + ACK_OFF) + (lane + 64 * j) * 32, __ATOMIC_RELAXED, __HIP_MEMORY_SCOPE_AGENT) == tag;
                    ok = __all(mine);
                }
                if (lane == 0) __hip_atomic_store((gu32*)(p.ws + EPOCH_OFF), tag, __ATOMIC_RELAXED, __HIP_MEMORY_SCOPE_AGENT);
            }
            HTS(layer * 20 + 15);
            BAR();
            BAR();
            if (lyr == 0) {
#pragma unroll
                for (int j = 0; j < 24; ++j) *(LAS u32x4*)(lds + L_WQ + (4 * j + hw) * 1024 + lane * 16) = buf[j];
                HTS(17);
                BAR();
            }
        }
    }
    asm volatile("s_waitcnt lgkmcnt(0)" ::: "memory");
    if (w == 4) HTS_DUMP();
}

__global__ __launch_bounds__(512, 2) void k_mega(Params p) {
    extern __shared__ __attribute__((aligned(16))) char smem_[];
    LAS char* lds = (LAS char*)smem_;
    const unsigned lds0 = (unsigned)(uintptr_t)smem_;
    const int b = blockIdx.x, idx = b >> 3;
    const int g_ = ((b & 7) >> 1) * 4 + (idx >> 3), r_ = (b & 1) * 8 + (idx & 7);
    const int w = __builtin_amdgcn_readfirstlane(threadIdx.x >> 6);
    const rsrc_t rs = __builtin_amdgcn_make_buffer_rsrc((void*)p.ws, 0, WS_USED, 0x00020000);
    if (w < 4) main_path(p, lds, rs, w, g_, r_);
    else helper_path(p, lds, lds0, rs, w, g_, r_);
}
}

extern "C" void kernel_launch(void* const* d_in, const int* in_sizes, int n_in, void* d_out, int out_size, void* d_ws, size_t ws_size, hipStream_t stream) {
    Params p;
    p.x = (const uint16_t*)d_in[0]; p.wqkv = (const uint16_t*)d_in[1]; p.wo = (const uint16_t*)d_in[2];
    p.w1 = (const uint16_t*)d_in[4]; p.w2 = (const uint16_t*)d_in[5];
    p.out = (uint16_t*)d_out; p.ws = (char*)d_ws;
    (void)hipFuncSetAttribute((const void*)k_mega, hipFuncAttributeMaxDynamicSharedMemorySize, LDS_BYTES);
    k_mega<<<256, 512, LDS_BYTES, stream>>>(p);
}
```

```cpp
#include <hip/hip_runtime.h>
#include <hip/hip_bf16.h>
#include <stdint.h>

#define GAS __attribute__((address_space(1)))
#define LAS __attribute__((address_space(3)))
#define DI __device__ __forceinline__

typedef __bf16 bf16x8 __attribute__((ext_vector_type(8)));
typedef __bf16 bf16x2 __attribute__((ext_vector_type(2)));
typedef short s16x4 __attribute__((ext_vector_type(4)));
typedef float f32x16 __attribute__((ext_vector_type(16)));
typedef float f32x4 __attribute__((ext_vector_type(4)));
typedef float f32x2 __attribute__((ext_vector_type(2)));
typedef unsigned u32x4 __attribute__((ext_vector_type(4)));
typedef unsigned u32x2 __attribute__((ext_vector_type(2)));
typedef GAS unsigned gu32;

#define TS_DECL()
#define TS_PARAM
#define TS_ARG(tb)
#define TS(i)
#define TS_DUMP()
#define HTS(i)
#define HTS_DUMP()
#define MAPMODE 1
namespace {
constexpr int DM = 512, FF = 1792;
constexpr float EPS = 1e-5f;
constexpr float LOG2E = 1.4426950408889634f;
constexpr float QSCALE = 0.25f * LOG2E;

constexpr int MB = 1 << 20, KB = 1024;
constexpr int XF_OFF = 0;
constexpr int O_OFF = 4 * MB;
constexpr int X2_OFF = 8 * MB;
constexpr int H_OFF = 12 * MB;
constexpr int KV_OFF = 28 * MB;
constexpr int WQT_OFF = 36 * MB;
constexpr int WOT_OFF = WQT_OFF + 1536 * KB;
constexpr int W1T_OFF = 38 * MB;
constexpr int W2T_OFF = 40 * MB;
constexpr int ST_OFF = 42 * MB;
constexpr int ST_BUF = 262144;
constexpr int KN_OFF = 43 * MB;
constexpr int FLAG_OFF = 43 * MB + 65536;
constexpr int FLAG_BYTES = 11 * 256 * 128;
constexpr int TMO_OFF = FLAG_OFF + FLAG_BYTES;
constexpr int EPOCH_OFF = TMO_OFF + 256;
constexpr int ACK_OFF = EPOCH_OFF + 256;
constexpr int WS_USED = 44 * MB;

constexpr int L_WQ = 0;
constexpr int L_Q = 96 * KB, L_KV = 104 * KB, L_ONES = 152 * KB;
constexpr int L_WO = 0, L_RED2 = 142 * KB;
constexpr int L_RED4 = 96 * KB;
constexpr int L_SCR = 100 * KB;
constexpr int L_TAB = 158 * KB, L_CSQ = L_TAB, L_CS1 = L_TAB + 512, L_STT = L_TAB + 1024;
constexpr int L_KN = L_TAB + 384, L_QN = L_TAB + 384 + 96;
constexpr int LDS_BYTES = 160 * KB;
constexpr int w1_off(int s) { return s < 9 ? 64 * KB + s * 3584 : s < 22 ? 96 * KB + (s - 9) * 3584 : (s - 22) * 3584; }
constexpr int w2_slot(int sp) { return (sp < 38 ? sp : sp - 38) * 4096; }
constexpr int AUX_SC1 = 16;

struct Params {
    const uint16_t* x; const uint16_t* wqkv; const uint16_t* wo; const uint16_t* w1; const uint16_t* w2;
    uint16_t* out; char* ws;
};

DI int lane_id() { int l; asm volatile("v_mbcnt_lo_u32_b32 %0, -1, 0\n\tv_mbcnt_hi_u32_b32 %0, -1, %0" : "=v"(l)); return l; }
DI f32x16 mfma32(bf16x8 a, bf16x8 b, f32x16 c) { return __builtin_amdgcn_mfma_f32_32x32x16_bf16(a, b, c, 0, 0, 0); }
DI unsigned pk(float a, float b) { bf16x2 v = {(__bf16)a, (__bf16)b}; return __builtin_bit_cast(unsigned, v); }
DI float bflo(unsigned u) { return __uint_as_float(u << 16); }
DI float bfhi(unsigned u) { return __uint_as_float(u & 0xffff0000u); }
DI bf16x8 asfrag(u32x4 v) { return __builtin_bit_cast(bf16x8, v); }
DI s16x4 trd(const LAS char* p) { return __builtin_amdgcn_ds_read_tr16_b64_v4i16((LAS s16x4*)p); }
DI u32x4 wfrag_tr(const LAS char* p, int off2) {
    const u32x2 lo = __builtin_bit_cast(u32x2, trd(p)), hi = __builtin_bit_cast(u32x2, trd(p + off2));
    return u32x4{lo.x, lo.y, hi.x, hi.y};
}
DI bf16x8 ldsfrag(const LAS char* p) { return *(const LAS bf16x8*)p; }
template <int N, class F> DI float colsum_frags(const LAS char* p, F off, float acc) {
    u32x4 f[N];
#pragma unroll
    for (int i = 0; i < N; ++i) f[i] = *(const LAS u32x4*)(p + off(i));
    float a1 = 0.f;
#pragma unroll
    for (int i = 0; i < N; ++i) {
        unsigned w0 = f[i].x, w1 = f[i].y, w2 = f[i].z, w3 = f[i].w;
        asm("v_dot2c_f32_bf16 %0, %2, %6\n\tv_dot2c_f32_bf16 %1, %3, %6\n\tv_dot2c_f32_bf16 %0, %4, %6\n\tv_dot2c_f32_bf16 %1, %5, %6"
            : "+v"(acc), "+v"(a1) : "v"(w0), "v"(w1), "v"(w2), "v"(w3), "v"(0x3f803f80u));
    }
    asm volatile("s_nop 2" : "+v"(acc), "+v"(a1));
    return acc + a1;
}
DI float sum_xor32(float v) { auto r = __builtin_amdgcn_permlane32_swap(__float_as_uint(v), __float_as_uint(v), false, false); return __uint_as_float(r[0]) + __uint_as_float(r[1]); }
DI float max_xor32(float v) { auto r = __builtin_amdgcn_permlane32_swap(__float_as_uint(v), __float_as_uint(v), false, false); return fmaxf(__uint_as_float(r[0]), __uint_as_float(r[1])); }
template <bool SC1> DI void glds16s(const void* sbase, unsigned voff, unsigned lds_addr) {
    unsigned keep;
    if (SC1) asm volatile("s_mov_b32 %0, m0\n\ts_mov_b32 m0, %3\n\ts_nop 0\n\tglobal_load_lds_dwordx4 %1, %2 sc1\n\ts_mov_b32 m0, %0" : "=&s"(keep) : "v"(voff), "s"(sbase), "s"(lds_addr) : "memory");
    else asm volatile("s_mov_b32 %0, m0\n\ts_mov_b32 m0, %3\n\ts_nop 0\n\tglobal_load_lds_dwordx4 %1, %2\n\ts_mov_b32 m0, %0" : "=&s"(keep) : "v"(voff), "s"(sbase), "s"(lds_addr) : "memory");
}
#define BAR() asm volatile("s_waitcnt lgkmcnt(0)\n\ts_barrier" ::: "memory")
#define DRAIN() asm volatile("s_waitcnt vmcnt(0)" ::: "memory")
#define WAITV(n) asm volatile("s_waitcnt vmcnt(" #n ")" ::: "memory")
#define SB() __builtin_amdgcn_sched_barrier(0)
#define SGB(mask, n) __builtin_amdgcn_sched_group_barrier(mask, n, 0)

typedef __amdgpu_buffer_rsrc_t rsrc_t;
DI u32x4 ldw(rsrc_t rs, int voff, int soff) { return __builtin_amdgcn_raw_buffer_load_b128(rs, voff, soff, AUX_SC1); }
DI void stw(rsrc_t rs, int voff, int soff, u32x4 v) { __builtin_amdgcn_raw_buffer_store_b128(v, rs, voff + soff, 0, AUX_SC1); }
DI f32x2 ldst(rsrc_t rs, int voff, int soff) { return __builtin_bit_cast(f32x2, __builtin_amdgcn_raw_buffer_load_b64(rs, voff, soff, AUX_SC1)); }
DI void stst(rsrc_t rs, int voff, int soff, f32x2 v) { __builtin_amdgcn_raw_buffer_store_b64(__builtin_bit_cast(u32x2, v), rs, voff, soff, AUX_SC1); }

#define FSTRIDE 32
DI gu32* flagp(const Params& p, int ph, int g, int r) { return (gu32*)(p.ws + FLAG_OFF) + (ph * 16 + g) * 16 * 32 + r * FSTRIDE; }
DI void set_flag(const Params& p, int ph, int g, int r, unsigned tag) { asm volatile("" : "+s"(tag)); __hip_atomic_store(flagp(p, ph, g, r), tag, __ATOMIC_RELAXED, __HIP_MEMORY_SCOPE_AGENT); }
DI void wait_word(const Params& p, gu32* a, unsigned tag) {
    for (unsigned spins = 0;; ++spins) {
        const unsigned v = __hip_atomic_load(a, __ATOMIC_RELAXED, __HIP_MEMORY_SCOPE_AGENT);
        if (__all(v == tag)) break;
        if (spins > (1u << 20)) { unsigned one = 1u; asm volatile("" : "+s"(one)); __hip_atomic_store((gu32*)(p.ws + TMO_OFF), one, __ATOMIC_RELAXED, __HIP_MEMORY_SCOPE_AGENT); break; }
        __builtin_amdgcn_s_sleep(1);
    }
    __builtin_amdgcn_fence(__ATOMIC_ACQUIRE, "wavefront");
}
DI void wait_group(const Params& p, int ph, int g, unsigned tag, int lane) { wait_word(p, flagp(p, ph, g, lane & 15), tag); }

DI void stats_store(rsrc_t rs, int buf, int T, int u, int lane, const unsigned (&c)[8]) {
    float s = 0.f, ss = 0.f;
#pragma unroll
    for (int i = 0; i < 8; ++i) { float lo = bflo(c[i]), hi = bfhi(c[i]); s += lo + hi; ss += lo * lo + hi * hi; }
    s = sum_xor32(s); ss = sum_xor32(ss);
    if (lane < 32) stst(rs, lane * 8, ST_OFF + buf * ST_BUF + (T * 16 + u) * 256, f32x2{s, ss});
}

DI void stage_lin(const char* wsb, unsigned lds0, int src, int dst, int n, int hw, int lane, int nw = 4) {
    const unsigned vo = lane * 16;
    for (int i = hw; i < n; i += nw) glds16s<true>(wsb + src + i * 1024, vo, lds0 + dst + i * 1024);
}

#define HELPER_VARS() \
    int hw = w - 4; asm volatile("" : "+s"(hw)); \
    const char* wsb = p.ws; asm volatile("" : "+s"(wsb)); \
    unsigned lds0 = lds0_; asm volatile("" : "+s"(lds0)); (void)hw; (void)wsb; (void)lds0;
#define PHASE_VARS() \
    int lane = lane_id(); asm volatile("" : "+v"(lane)); \
    int lyr = layer; asm volatile("" : "+s"(lyr)); \
    int g = g_, r = r_; asm volatile("" : "+s"(g), "+s"(r)); \
    const int h = lane >> 5, tok = lane & 31; const int m = r & 1, n = r >> 1; (void)h; (void)tok; (void)m; (void)n; (void)lyr;

DI void attn_finish(rsrc_t rs, const f32x16& o, int hd, int T, int r, int lyr, int lane) {
    float ov[8]; float l;
    if (hd == 0) { l = o[8];
#pragma unroll
        for (int i = 0; i < 8; ++i) ov[i] = o[i];
    } else { l = o[0];
#pragma unroll
        for (int i = 0; i < 8; ++i) ov[i] = o[8 + i];
    }
    const float inv = __builtin_amdgcn_rcpf(l);
    stw(rs, lane * 16, O_OFF + lyr * 2 * MB + (T * 32 + 2 * r + hd) * 1024,
        u32x4{pk(ov[0] * inv, ov[1] * inv), pk(ov[2] * inv, ov[3] * inv), pk(ov[4] * inv, ov[5] * inv), pk(ov[6] * inv, ov[7] * inv)});
}
DI float max16(float m, const f32x16& s) {
#pragma unroll
    for (int i = 0; i < 16; i += 2) m = fmaxf(fmaxf(m, s[i]), s[i + 1]);
    return m;
}
DI void max16x2(float& m0, float& m1, const f32x16& s) {
#pragma unroll
    for (int i = 0; i < 16; i += 4) { m0 = fmaxf(fmaxf(m0, s[i]), s[i + 1]); m1 = fmaxf(fmaxf(m1, s[i + 2]), s[i + 3]); }
}
DI void exp_pack(const f32x16& s, u32x4& p0, u32x4& p1) {
    unsigned pw[8];
#pragma unroll
    for (int i = 0; i < 8; ++i) pw[i] = pk(__builtin_amdgcn_exp2f(s[2 * i]), __builtin_amdgcn_exp2f(s[2 * i + 1]));
    p0 = u32x4{pw[0], pw[1], pw[2], pw[3]}; p1 = u32x4{pw[4], pw[5], pw[6], pw[7]};
}
DI float wave32_max(float v) {
    int x = __float_as_int(v);
    x = max(x, __builtin_amdgcn_update_dpp(x, x, 0x111, 0xf, 0xf, false));
    x = max(x, __builtin_amdgcn_update_dpp(x, x, 0x112, 0xf, 0xf, false));
    x = max(x, __builtin_amdgcn_update_dpp(x, x, 0x114, 0xf, 0xf, false));
    x = max(x, __builtin_amdgcn_update_dpp(x, x, 0x118, 0xf, 0xf, false));
    x = max(x, __builtin_amdgcn_update_dpp(x, x, 0x142, 0xa, 0xf, false));
    return __int_as_float(__builtin_amdgcn_readlane(x, 31));
}
DI void attn_seg(unsigned ka, unsigned va, int ks, int vs, int n, const bf16x8 qf, const f32x16 c0, f32x16& o) {
    int d;
    asm volatile(
        "ds_read_b128 v[248:251], %[ka]\n\tv_add_u32 %[ka], %[ks], %[ka]\n\tds_read_b128 v[236:239], %[ka]\n\tv_add_u32 %[ka], %[ks], %[ka]\n\t"
        "ds_read_b128 v[240:243], %[va]\n\tds_read_b128 v[244:247], %[va] offset:1024\n\tv_add_u32 %[va], %[vs], %[va]\n\t"
        "s_lshr_b32 %[d], %[n], 1\n\ts_waitcnt lgkmcnt(3)\n\t"
        "v_mfma_f32_32x32x16_bf16 v[204:219], v[248:251], %[q], %[c0]\n\t"
        "s_cmp_eq_u32 %[d], 0\n\ts_nop 10\n\ts_cbranch_scc1 Ltail_%=\n"
        "Lloop_%=:\n\t"
        "v_exp_f32 v204, v204\n\t"
        "v_exp_f32 v205, v205\n\t"
        "v_exp_f32 v206, v206\n\t"
        "v_exp_f32 v207, v207\n\t"
        "v_exp_f32 v208, v208\n\t"
        "v_exp_f32 v209, v209\n\t"
        "v_exp_f32 v210, v210\n\t"
        "v_exp_f32 v211, v211\n\t"
        "v_cvt_pk_bf16_f32 v204, v204, v205\n\t"
        "v_cvt_pk_bf16_f32 v205, v206, v207\n\t"
        "v_cvt_pk_bf16_f32 v206, v208, v209\n\t"
        "v_cvt_pk_bf16_f32 v207, v210, v211\n\t"
        "s_waitcnt lgkmcnt(1)\n\ts_nop 0\n\t"
        "v_mfma_f32_32x32x16_bf16 %[o], v[240:243], v[204:207], %[o]\n\t"
        "v_exp_f32 v212, v212\n\t"
        "v_exp_f32 v213, v213\n\t"
        "v_exp_f32 v214, v214\n\t"
        "v_exp_f32 v215, v215\n\t"
        "v_mfma_f32_32x32x16_bf16 v[220:235], v[236:239], %[q], 0\n\t"
        "ds_read_b128 v[236:239], %[ka]\n\tds_read_b128 v[248:251], %[va]\n\tds_read_b128 v[252:255], %[va] offset:1024\n\t"
        "v_add_u32 %[ka], %[ks], %[ka]\n\tv_add_u32 %[va], %[vs], %[va]\n\t"
        "v_exp_f32 v216, v216\n\t"
        "v_exp_f32 v217, v217\n\t"
        "v_exp_f32 v218, v218\n\t"
        "v_exp_f32 v219, v219\n\t"
        "v_cvt_pk_bf16_f32 v212, v212, v213\n\t"
        "v_cvt_pk_bf16_f32 v213, v214, v215\n\t"
        "v_cvt_pk_bf16_f32 v214, v216, v217\n\t"
        "v_cvt_pk_bf16_f32 v215, v218, v219\n\t"
        "s_waitcnt lgkmcnt(3)\n\ts_nop 0\n\t"
        "v_mfma_f32_32x32x16_bf16 %[o], v[244:247], v[212:215], %[o]\n\t"
        "v_exp_f32 v220, v220\n\t"
        "v_exp_f32 v221, v221\n\t"
        "v_exp_f32 v222, v222\n\t"
        "v_exp_f32 v223, v223\n\t"
        "v_exp_f32 v224, v224\n\t"
        "v_exp_f32 v225, v225\n\t"
        "v_exp_f32 v226, v226\n\t"
        "v_exp_f32 v227, v227\n\t"
        "v_cvt_pk_bf16_f32 v220, v220, v221\n\t"
        "v_cvt_pk_bf16_f32 v221, v222, v223\n\t"
        "v_cvt_pk_bf16_f32 v222, v224, v225\n\t"
        "v_cvt_pk_bf16_f32 v223, v226, v227\n\t"
        "s_waitcnt lgkmcnt(1)\n\ts_nop 0\n\t"
        "v_mfma_f32_32x32x16_bf16 %[o], v[248:251], v[220:223], %[o]\n\t"
        "v_exp_f32 v228, v228\n\t"
        "v_exp_f32 v229, v229\n\t"
        "v_exp_f32 v230, v230\n\t"
        "v_exp_f32 v231, v231\n\t"
        "v_mfma_f32_32x32x16_bf16 v[204:219], v[236:239], %[q], 0\n\t"
        "ds_read_b128 v[236:239], %[ka]\n\tds_read_b128 v[240:243], %[va]\n\tds_read_b128 v[244:247], %[va] offset:1024\n\t"
        "v_add_u32 %[ka], %[ks], %[ka]\n\tv_add_u32 %[va], %[vs], %[va]\n\t"
        "v_exp_f32 v232, v232\n\t"
        "v_exp_f32 v233, v233\n\t"
        "v_exp_f32 v234, v234\n\t"
        "v_exp_f32 v235, v235\n\t"
        "v_cvt_pk_bf16_f32 v228, v228, v229\n\t"
        "v_cvt_pk_bf16_f32 v229, v230, v231\n\t"
        "v_cvt_pk_bf16_f32 v230, v232, v233\n\t"
        "v_cvt_pk_bf16_f32 v231, v234, v235\n\t"
        "s_waitcnt lgkmcnt(3)\n\ts_nop 0\n\t"
        "v_mfma_f32_32x32x16_bf16 %[o], v[252:255], v[228:231], %[o]\n\t"
        "s_sub_u32 %[d], %[d], 1\n\ts_cmp_lg_u32 %[d], 0\n\ts_cbranch_scc1 Lloop_%=\n"
        "Ltail_%=:\n\t"
        "s_bitcmp1_b32 %[n], 0\n\ts_waitcnt lgkmcnt(0)\n\ts_cbranch_scc0 Lend_%=\n\t"
        "v_exp_f32 v204, v204\n\t"
        "v_exp_f32 v205, v205\n\t"
        "v_exp_f32 v206, v206\n\t"
        "v_exp_f32 v207, v207\n\t"
        "v_exp_f32 v208, v208\n\t"
        "v_exp_f32 v209, v209\n\t"
        "v_exp_f32 v210, v210\n\t"
        "v_exp_f32 v211, v211\n\t"
        "v_cvt_pk_bf16_f32 v204, v204, v205\n\t"
        "v_cvt_pk_bf16_f32 v205, v206, v207\n\t"
        "v_cvt_pk_bf16_f32 v206, v208, v209\n\t"
        "v_cvt_pk_bf16_f32 v207, v210, v211\n\t"
        "s_nop 1\n\t"
        "v_mfma_f32_32x32x16_bf16 %[o], v[240:243], v[204:207], %[o]\n\t"
        "v_exp_f32 v212, v212\n\t"
        "v_exp_f32 v213, v213\n\t"
        "v_exp_f32 v214, v214\n\t"
        "v_exp_f32 v215, v215\n\t"
        "v_exp_f32 v216, v216\n\t"
        "v_exp_f32 v217, v217\n\t"
        "v_exp_f32 v218, v218\n\t"
        "v_exp_f32 v219, v219\n\t"
        "v_cvt_pk_bf16_f32 v212, v212, v213\n\t"
        "v_cvt_pk_bf16_f32 v213, v214, v215\n\t"
        "v_cvt_pk_bf16_f32 v214, v216, v217\n\t"
        "v_cvt_pk_bf16_f32 v215, v218, v219\n\t"
        "s_nop 1\n\t"
        "v_mfma_f32_32x32x16_bf16 %[o], v[244:247], v[212:215], %[o]\n"
        "Lend_%=:\n\ts_nop 11"
        : [o] "+v"(o), [ka] "+v"(ka), [va] "+v"(va), [d] "=&s"(d)
        : [q] "v"(qf), [c0] "v"(c0), [ks] "s"(ks), [vs] "v"(vs), [n] "s"(n)
        : "memory", "scc", "v204", "v205", "v206", "v207", "v208", "v209", "v210", "v211", "v212", "v213", "v214", "v215", "v216", "v217", "v218", "v219", "v220", "v221", "v222", "v223", "v224", "v225", "v226", "v227", "v228", "v229", "v230", "v231", "v232", "v233", "v234", "v235", "v236", "v237", "v238", "v239", "v240", "v241", "v242", "v243", "v244", "v245", "v246", "v247", "v248", "v249", "v250", "v251", "v252", "v253", "v254", "v255");
}
constexpr float BOUND2 = 4096.f;
DI f32x16 attn_exact(LAS char* lds, int tw, int hd, int s0, bool first_masked, const bf16x8 qf, int lane) {
    const int h = lane >> 5, tok = lane & 31;
    const LAS char* kb = lds + L_KV + hd * 1024 + lane * 16;
    const bool isdata = ((lane >> 4) & 1) == hd;
    const LAS char* vb = isdata ? (lds + L_KV + 2048 + lane * 16) : (lds + L_ONES);
    const int vstep = isdata ? 4096 : 0;
    const f32x16 zero = {};
    f32x16 o = {};
    float mx = -INFINITY;
    for (int pass = 0; pass < 2; ++pass) {
        for (int sl = s0; sl <= 8 + tw; ++sl) {
            f32x16 s = mfma32(*(const LAS bf16x8*)(kb + sl * 4096), qf, zero);
            const bool diag = sl == 8 + tw, first = first_masked && sl == s0;
#pragma unroll
            for (int i = 0; i < 16; ++i) { const int kk = (i & 3) + 8 * (i >> 2) + 4 * h; if ((diag && kk > tok) || (first && kk <= tok)) s[i] = -INFINITY; }
            if (pass == 0) mx = max16(mx, s);
            else {
#pragma unroll
                for (int i = 0; i < 16; ++i) s[i] -= mx;
                u32x4 p0, p1; exp_pack(s, p0, p1);
                o = mfma32(*(const LAS bf16x8*)(vb + sl * vstep), asfrag(p0), o);
                o = mfma32(*(const LAS bf16x8*)(vb + sl * vstep + 1024), asfrag(p1), o);
            }
        }
        if (pass == 0) mx = max_xor32(mx);
    }
    return o;
}
DI void attn_phase(const Params& p, LAS char* lds, rsrc_t rs, int w, int g, int r, int lyr, int lane, unsigned tag TS_PARAM) {
    const int h = lane >> 5, tok = lane & 31;
    const int hd = w >> 2, tw = hd ? 3 - (w & 3) : (w & 3), T = 4 * g + tw;
    const bf16x8 qf = *(const LAS bf16x8*)(lds + L_Q + tw * 2048 + hd * 1024 + lane * 16);
    const unsigned kb = (unsigned)(uintptr_t)(lds + L_KV + hd * 1024 + lane * 16);
    const bool isdata = ((lane >> 4) & 1) == hd;
    const unsigned vb = (unsigned)(uintptr_t)(isdata ? (lds + L_KV + 2048 + lane * 16) : (lds + L_ONES));
    const int vstep = isdata ? 4096 : 0;
    const LAS float* kn = (const LAS float*)(lds + L_KN);
    const float qn = ((const LAS float*)(lds + L_QN))[tw * 2 + hd] * 1.05f;
    const float kown = fmaxf(fmaxf(kn[16 + hd], kn[18 + hd]), fmaxf(kn[20 + hd], kn[22 + hd]));
    bool fast = __builtin_amdgcn_readfirstlane(qn * kown <= BOUND2);
    float dd = (float)(tok - 4 * h) * 1e30f; asm volatile("" : "+v"(dd));
    f32x16 t, o = {};
#pragma unroll
    for (int i = 0; i < 16; ++i) t[i] = dd + (0.5f - (float)((i & 3) + 8 * (i >> 2))) * 1e30f;
    const int s4 = w == 7 ? 4 : 0, tile0 = 4 * g - 8 + s4;
    const bool loader = w >= 6 && tile0 >= 0;
    u32x4 hv[16]; f32x2 hn;
    if (loader) {
        wait_word(p, flagp(p, 1 + 5 * lyr, g - 2 + (s4 >> 2), r), tag);
        TS(tb + 1);
        const int kvo = KV_OFF + lyr * 4 * MB + (tile0 * 16 + r) * 4096;
#pragma unroll
        for (int i = 0; i < 16; ++i) hv[i] = ldw(rs, lane * 16 + (i & 3) * 1024, kvo + (i >> 2) * 65536);
        hn = ldst(rs, (lane & 3) * 128, KN_OFF + ((lyr * 64 + tile0) * 16 + r) * 8);
        SB();
    }
    if (fast) {
        f32x16 m;
#pragma unroll
        for (int i = 0; i < 16; ++i) m[i] = fminf(t[i], 0.f);
        attn_seg(kb + (8 + tw) * 4096, vb + (8 + tw) * vstep, -4096, -vstep, tw + 1, qf, m, o);
    }
    TS(tb);
    if (loader) {
        LAS char* kvl = lds + L_KV + s4 * 4096 + lane * 16;
#pragma unroll
        for (int i = 0; i < 16; ++i) *(LAS u32x4*)(kvl + i * 1024) = hv[i];
        if (lane < 4) *(LAS f32x2*)(lds + L_KN + (s4 + lane) * 8) = hn;
    }
    BAR();
    TS(tb + 2);
    const int s0 = T >= 8 ? tw : 8 - 4 * g;
    if (s0 < 8) {
        float kh = 0.f;
#pragma unroll
        for (int j = 0; j < 8; ++j) kh = fmaxf(kh, j >= 8 - 4 * g ? kn[2 * j + hd] : 0.f);
        fast = fast && __builtin_amdgcn_readfirstlane(qn * kh <= BOUND2);
        if (fast) {
            f32x16 m;
#pragma unroll
            for (int i = 0; i < 16; ++i) m[i] = T >= 8 ? fminf(-t[i], 0.f) : 0.f;
            attn_seg(kb + s0 * 4096, vb + s0 * vstep, 4096, vstep, 8 - s0, qf, m, o);
        }
    }
    if (!fast) o = attn_exact(lds, tw, hd, s0, T >= 8, qf, lane);
    attn_finish(rs, o, hd, T, r, lyr, lane);
}
DI void ln_stats(const f32x2 (&sv)[8], float& mean, float& rstd) {
    float S = 0.f, SS = 0.f;
#pragma unroll
    for (int i = 0; i < 8; ++i) { S += sv[i].x; SS += sv[i].y; }
    S = sum_xor32(S); SS = sum_xor32(SS);
    mean = S * (1.f / DM); const float var = fmaxf(SS * (1.f / DM) - mean * mean, 0.f); rstd = __builtin_amdgcn_rsqf(var + EPS);
}
DI unsigned read_tag(const Params& p) {
    return (unsigned)__builtin_amdgcn_readfirstlane(__hip_atomic_load((gu32*)(p.ws + EPOCH_OFF), __ATOMIC_RELAXED, __HIP_MEMORY_SCOPE_AGENT)) + 1u;
}
#define LDA(arr, s, base) arr[s] = ldw(rs, lane * 16 + ((s) & 3) * 1024, (base) + ((s) >> 2) * 4096)

#define WODELAY 36
#define PD0 4
#define PDV 12
constexpr int a_issued(int s, int total) { int v = PD0 + 2 * (s + 1); const int cap = s + 1 + PDV; v = v < cap ? v : cap; return v < total ? v : total; }

DI void main_path(const Params& p, LAS char* lds, rsrc_t rs, const int w, const int g_, const int r_) {
    unsigned tag = 0;
    TS_DECL();
    TS(0);
    {
        const int layer = 0; PHASE_VARS();
        const int T = 4 * g + (r & 3), u = (r >> 2) * 4 + w;
        const uint16_t* row = p.x + (size_t)(T * 32 + tok) * DM + 32 * u + 4 * h;
        u32x2 a0 = *(const GAS u32x2*)(row), a1 = *(const GAS u32x2*)(row + 8), a2 = *(const GAS u32x2*)(row + 16), a3 = *(const GAS u32x2*)(row + 24);
        tag = read_tag(p); asm volatile("" :: "s"(tag) : "memory");
        stw(rs, lane * 16, XF_OFF + (T * 32 + 2 * u) * 1024, u32x4{a0.x, a0.y, a1.x, a1.y});
        stw(rs, lane * 16, XF_OFF + (T * 32 + 2 * u + 1) * 1024, u32x4{a2.x, a2.y, a3.x, a3.y});
        const unsigned c[8] = {a0.x, a0.y, a1.x, a1.y, a2.x, a2.y, a3.x, a3.y};
        stats_store(rs, 0, T, u, lane, c);
        DRAIN();
        BAR();
        if (w == 0 && lane == 0) { set_flag(p, 0, g, r, tag); __hip_atomic_store((gu32*)(p.ws + ACK_OFF) + blockIdx.x * 32, tag, __ATOMIC_RELAXED, __HIP_MEMORY_SCOPE_AGENT); }
    }
    TS(1);
    for (int layer = 0; layer < 2; ++layer) {
        const int tb = 2 + layer * 24; (void)tb;
        {
            PHASE_VARS();
            if (w == 0) {
                if (lyr == 0) {
                    const int j = lane & 15;
                    wait_word(p, (lane & 16) ? flagp(p, 0, j, r) : flagp(p, 0, g, j), tag);
                } else wait_group(p, 5, g, tag, lane);
            }
            BAR();
            TS(tb + 0);
            const int T = 4 * g + w;
            const LAS char* wb = lds + L_WQ + lane * 16;
            LAS float* csq = (LAS float*)(lds + L_CSQ);
            LAS float* stt = (LAS float*)(lds + L_STT);
            f32x16 ak = {}, av = {};
            u32x4 xa[32];
            f32x2 sv[8];
            const int sto = ST_OFF + 2 * lyr * ST_BUF + T * 4096, xo = XF_OFF + lyr * 2 * MB + T * 32768;
#pragma unroll
            for (int s = 0; s < PD0; ++s) { LDA(xa, s, xo); SB(); }
            if (lyr == 0) BAR();
            bf16x8 wf[4][2];
#pragma unroll
            for (int s = 0; s < 3; ++s) { wf[s][0] = ldsfrag(wb + s * 1024); wf[s][1] = ldsfrag(wb + (32 + s) * 1024); }
#pragma unroll
            for (int s = 0; s < 32; ++s) {
                if ((s == 5 || s == 13 || s == 21) && lyr == 0) BAR();
#pragma unroll
                for (int j = (s == 0 ? PD0 : a_issued(s - 1, 32)); j < a_issued(s, 32); ++j) LDA(xa, j, xo);
                if (s >= 12 && s < 20) sv[s - 12] = ldst(rs, (8 * h * 32 + tok) * 8 + (s - 12) * 256, sto);
                if (s + 3 < 32) { wf[(s + 3) & 3][0] = ldsfrag(wb + (s + 3) * 1024); wf[(s + 3) & 3][1] = ldsfrag(wb + (32 + s + 3) * 1024); }
                const bf16x8 x = asfrag(xa[s]);
                ak = mfma32(wf[s & 3][0], x, ak); av = mfma32(x, wf[s & 3][1], av);
                SGB(0x100, 1); SGB(0x8, 1); SGB(0x20, 2); SGB(0x100, 1); SGB(0x8, 1);
                SB();
            }
            float mean, rstd; ln_stats(sv, mean, rstd);
            if (h == 0) { stt[w * 64 + tok] = rstd; stt[w * 64 + 32 + tok] = mean * rstd; }
            TS(tb + 1);
            BAR();
            TS(tb + 2);
            const float mr = mean * rstd;
            {
                float k[16], v[16];
                const float cv = csq[64 + tok];
#pragma unroll
                for (int g4 = 0; g4 < 4; ++g4) {
                    const f32x4 ck = *(LAS f32x4*)(csq + 32 + 8 * g4 + 4 * h);
                    const f32x4 rsv = *(LAS f32x4*)(stt + w * 64 + 8 * g4 + 4 * h), ms = *(LAS f32x4*)(stt + w * 64 + 32 + 8 * g4 + 4 * h);
#pragma unroll
                    for (int i = 0; i < 4; ++i) {
                        k[4 * g4 + i] = ak[4 * g4 + i] * rstd - mr * ck[i];
                        v[4 * g4 + i] = av[4 * g4 + i] * rsv[i] - ms[i] * cv;
                    }
                }
                const u32x4 k0 = {pk(k[0], k[1]), pk(k[2], k[3]), pk(k[4], k[5]), pk(k[6], k[7])};
                const u32x4 k1 = {pk(k[8], k[9]), pk(k[10], k[11]), pk(k[12], k[13]), pk(k[14], k[15])};
                const u32x4 v0 = {pk(v[0], v[1]), pk(v[2], v[3]), pk(v[4], v[5]), pk(v[6], v[7])};
                const u32x4 v1 = {pk(v[8], v[9]), pk(v[10], v[11]), pk(v[12], v[13]), pk(v[14], v[15])};
                const int kvo = KV_OFF + lyr * 4 * MB + (T * 16 + r) * 4096;
                stw(rs, lane * 16, kvo, k0); stw(rs, lane * 16 + 1024, kvo, k1); stw(rs, lane * 16 + 2048, kvo, v0); stw(rs, lane * 16 + 3072, kvo, v1);
                float na = 0.f, nb = 0.f;
#pragma unroll
                for (int i = 0; i < 8; ++i) { na = __builtin_fmaf(k[i], k[i], na); nb = __builtin_fmaf(k[8 + i], k[8 + i], nb); }
                const f32x2 nn = {wave32_max(sum_xor32(na)), wave32_max(sum_xor32(nb))};
                if (lane == 0) { stst(rs, 0, KN_OFF + ((lyr * 64 + T) * 16 + r) * 8, nn); *(LAS f32x2*)(lds + L_KN + (8 + w) * 8) = nn; }
                LAS char* kvl = lds + L_KV + (8 + w) * 4096 + lane * 16;
                *(LAS u32x4*)(kvl) = k0; *(LAS u32x4*)(kvl + 1024) = k1; *(LAS u32x4*)(kvl + 2048) = v0; *(LAS u32x4*)(kvl + 3072) = v1;
            }
            SB();
            f32x16 aq = {};
            bf16x8 wq[4];
#pragma unroll
            for (int s = 0; s < 3; ++s) wq[s] = ldsfrag(wb + (64 + s) * 1024);
#pragma unroll
            for (int s = 0; s < 32; ++s) {
                if (s == 22) {
                    DRAIN();
                    BAR();
                    if (w == 0 && lane == 0) set_flag(p, 1 + 5 * lyr, g, r, tag);
                    TS(tb + 3);
                }
                if (s + 3 < 32) wq[(s + 3) & 3] = ldsfrag(wb + (64 + s + 3) * 1024);
                aq = mfma32(wq[s & 3], asfrag(xa[s]), aq);
                SGB(0x100, 1); SGB(0x8, 1);
                SB();
            }
            {
                float q[16];
#pragma unroll
                for (int g4 = 0; g4 < 4; ++g4) {
                    const f32x4 cq = *(LAS f32x4*)(csq + 8 * g4 + 4 * h);
#pragma unroll
                    for (int i = 0; i < 4; ++i) q[4 * g4 + i] = (aq[4 * g4 + i] * rstd - mr * cq[i]) * QSCALE;
                }
                LAS char* ql = lds + L_Q + w * 2048 + lane * 16;
                *(LAS u32x4*)(ql) = u32x4{pk(q[0], q[1]), pk(q[2], q[3]), pk(q[4], q[5]), pk(q[6], q[7])};
                *(LAS u32x4*)(ql + 1024) = u32x4{pk(q[8], q[9]), pk(q[10], q[11]), pk(q[12], q[13]), pk(q[14], q[15])};
                float na = 0.f, nb = 0.f;
#pragma unroll
                for (int i = 0; i < 8; ++i) { na = __builtin_fmaf(q[i], q[i], na); nb = __builtin_fmaf(q[8 + i], q[8 + i], nb); }
                const f32x2 nn = {wave32_max(sum_xor32(na)), wave32_max(sum_xor32(nb))};
                if (lane == 0) *(LAS f32x2*)(lds + L_QN + w * 8) = nn;
            }
            BAR();
        }
        {
            PHASE_VARS();
            attn_phase(p, lds, rs, w, g, r, lyr, lane, tag TS_ARG(layer == 0 ? 22 : 48));
            TS(tb + 4); TS(tb + 5); TS(tb + 6);
            DRAIN();
            BAR();
            if (w == 0 && lane == 0) set_flag(p, 2 + 5 * lyr, g, r, tag);
            TS(tb + 7);
        }
        {
            PHASE_VARS();
            const int mt = w & 1, kh = (w >> 1) & 1, T = 4 * g + 2 * m + mt, u = 2 * n + kh;
            const int xo = XF_OFF + lyr * 2 * MB + (T * 32 + 2 * u) * 1024;
            const u32x4 r0 = ldw(rs, lane * 16, xo), r1 = ldw(rs, lane * 16 + 1024, xo);
            if (w == 0) wait_group(p, 2 + 5 * lyr, g, tag, lane);
            BAR();
            TS(tb + 8);
            f32x16 acc[2] = {};
            u32x4 xa[16];
            const int oo = O_OFF + lyr * 2 * MB + (T * 32 + 16 * kh) * 1024;
#pragma unroll
            for (int s = 0; s < 16; ++s) { LDA(xa, s, oo); SB(); }
            const LAS char* wb = lds + L_WO + kh * 32768 + lane * 16;
            bf16x8 wf[4][2];
#pragma unroll
            for (int s = 0; s < 3; ++s) { wf[s][0] = ldsfrag(wb + s * 2048); wf[s][1] = ldsfrag(wb + s * 2048 + 1024); }
#pragma unroll
            for (int s = 0; s < 16; ++s) {
                if (s + 3 < 16) { wf[(s + 3) & 3][0] = ldsfrag(wb + (s + 3) * 2048); wf[(s + 3) & 3][1] = ldsfrag(wb + (s + 3) * 2048 + 1024); }
                const bf16x8 x = asfrag(xa[s]);
                acc[0] = mfma32(wf[s & 3][0], x, acc[0]); acc[1] = mfma32(wf[s & 3][1], x, acc[1]);
                SGB(0x100, 1); SGB(0x8, 1); SGB(0x20, 2); SGB(0x100, 1); SGB(0x8, 1);
                SB();
            }
            const f32x16 snd = kh ? acc[0] : acc[1];
#pragma unroll
            for (int g4 = 0; g4 < 4; ++g4) *(LAS f32x4*)(lds + L_RED2 + ((w * 4 + g4) * 64 + lane) * 16) = f32x4{snd[4 * g4], snd[4 * g4 + 1], snd[4 * g4 + 2], snd[4 * g4 + 3]};
            TS(tb + 9);
            BAR();
            const f32x16 own = kh ? acc[1] : acc[0];
            const int pw = w ^ 2;
            float v[16];
#pragma unroll
            for (int g4 = 0; g4 < 4; ++g4) { f32x4 t = *(LAS f32x4*)(lds + L_RED2 + ((pw * 4 + g4) * 64 + lane) * 16);
#pragma unroll
                for (int i = 0; i < 4; ++i) v[4 * g4 + i] = own[4 * g4 + i] + t[i]; }
            const unsigned rr[8] = {r0.x, r0.y, r0.z, r0.w, r1.x, r1.y, r1.z, r1.w};
            unsigned c[8];
#pragma unroll
            for (int i = 0; i < 8; ++i) c[i] = pk(v[2 * i] + bflo(rr[i]), v[2 * i + 1] + bfhi(rr[i]));
            const int yo = X2_OFF + lyr * 2 * MB + (T * 32 + 2 * u) * 1024;
            stw(rs, lane * 16, yo, u32x4{c[0], c[1], c[2], c[3]});
            stw(rs, lane * 16 + 1024, yo, u32x4{c[4], c[5], c[6], c[7]});
            stats_store(rs, 2 * lyr + 1, T, u, lane, c);
            DRAIN();
            BAR();
            if (w == 0 && lane == 0) set_flag(p, 3 + 5 * lyr, g, r, tag);
            TS(tb + 10);
        }
        {
            PHASE_VARS();
            if (w == 0) wait_group(p, 3 + 5 * lyr, g, tag, lane);
            BAR();
            TS(tb + 11);
            const int T = 4 * g + w;
            const LAS char* wbA = lds + lane * 16;
            const LAS char* wbB = lds + 3072 + (h * 16 + (lane & 15)) * 16;
            LAS float* cs1 = (LAS float*)(lds + L_CS1);
            f32x16 acc[4] = {};
            u32x4 xa[32];
            f32x2 sv[8];
            const int sto = ST_OFF + (2 * lyr + 1) * ST_BUF + T * 4096, xo = X2_OFF + lyr * 2 * MB + T * 32768;
#pragma unroll
            for (int s = 0; s < PD0; ++s) { LDA(xa, s, xo); SB(); }
            auto ldW = [&](int s, int t) { return t < 3 ? ldsfrag(wbA + w1_off(s) + t * 1024) : ldsfrag(wbB + w1_off(s)); };
            bf16x8 wf[2][4];
#pragma unroll
            for (int t = 0; t < 4; ++t) wf[0][t] = ldW(0, t);
#pragma unroll
            for (int s = 0; s < 32; ++s) {
                if (s == 21) BAR();
#pragma unroll
                for (int j = (s == 0 ? PD0 : a_issued(s - 1, 32)); j < a_issued(s, 32); ++j) LDA(xa, j, xo);
                if (s >= 16 && s < 24) sv[s - 16] = ldst(rs, (8 * h * 32 + tok) * 8 + (s - 16) * 256, sto);
                const bf16x8 x = asfrag(xa[s]);
#pragma unroll
                for (int t = 0; t < 4; ++t) {
                    if (s + 1 < 32) wf[(s + 1) & 1][t] = ldW(s + 1, t);
                    acc[t] = mfma32(wf[s & 1][t], x, acc[t]);
                }
                SGB(0x100, 1); SGB(0x8, 1); SGB(0x20, 1); SGB(0x100, 1); SGB(0x8, 1); SGB(0x20, 1); SGB(0x100, 1); SGB(0x8, 1); SGB(0x100, 1); SGB(0x8, 1);
                SB();
            }
            float mean, rstd; ln_stats(sv, mean, rstd);
            TS(tb + 12);
            BAR();
            TS(tb + 13);
            const int ho = H_OFF + lyr * 8 * MB + (T * 112 + 7 * r) * 1024;
            const f32x2 nm2 = {-mean, -mean}, rs2 = {rstd, rstd}, ry2 = {-LOG2E * rstd, -LOG2E * rstd}, one2 = {1.f, 1.f};
#pragma unroll
            for (int t = 0; t < 4; ++t) {
                constexpr int dummy = 0; (void)dummy;
                const int np = t < 3 ? 8 : 4;
                f32x2 tt[8], z[8], e[8];
#pragma unroll
                for (int g4 = 0; g4 < 4; ++g4) {
                    if (2 * g4 < np) {
                        const f32x4 c4 = *(LAS f32x4*)(cs1 + t * 32 + 8 * g4 + 4 * h);
                        tt[2 * g4] = __builtin_elementwise_fma(nm2, f32x2{c4[0], c4[1]}, f32x2{acc[t][4 * g4], acc[t][4 * g4 + 1]});
                        tt[2 * g4 + 1] = __builtin_elementwise_fma(nm2, f32x2{c4[2], c4[3]}, f32x2{acc[t][4 * g4 + 2], acc[t][4 * g4 + 3]});
                    }
                }
#pragma unroll
                for (int i = 0; i < 8; ++i) if (i < np) e[i] = tt[i] * ry2;
                SB();
#pragma unroll
                for (int i = 0; i < 8; ++i) if (i < np) e[i] = f32x2{__builtin_amdgcn_exp2f(e[i].x), __builtin_amdgcn_exp2f(e[i].y)};
#pragma unroll
                for (int i = 0; i < 8; ++i) if (i < np) z[i] = tt[i] * rs2;
                SB();
#pragma unroll
                for (int i = 0; i < 8; ++i) if (i < np) e[i] = e[i] + one2;
                SB();
#pragma unroll
                for (int i = 0; i < 8; ++i) if (i < np) e[i] = f32x2{__builtin_amdgcn_rcpf(e[i].x), __builtin_amdgcn_rcpf(e[i].y)};
                SB();
#pragma unroll
                for (int i = 0; i < 8; ++i) if (i < np) z[i] = z[i] * e[i];
                stw(rs, lane * 16 + (2 * t) * 1024, ho, u32x4{pk(z[0].x, z[0].y), pk(z[1].x, z[1].y), pk(z[2].x, z[2].y), pk(z[3].x, z[3].y)});
                if (t < 3) stw(rs, lane * 16 + (2 * t + 1) * 1024, ho, u32x4{pk(z[4].x, z[4].y), pk(z[5].x, z[5].y), pk(z[6].x, z[6].y), pk(z[7].x, z[7].y)});
                SB();
            }
            DRAIN();
            BAR();
            if (w == 0 && lane == 0) set_flag(p, 4 + 5 * lyr, g, r, tag);
            TS(tb + 14);
        }
        {
            PHASE_VARS();
            const int mt = w & 1, kh = (w >> 1) & 1, T = 4 * g + 2 * m + mt, u = 2 * n + kh;
            const int yo = X2_OFF + lyr * 2 * MB + (T * 32 + 2 * u) * 1024;
            const u32x4 r0 = ldw(rs, lane * 16, yo), r1 = ldw(rs, lane * 16 + 1024, yo);
            if (w == 0) wait_group(p, 4 + 5 * lyr, g, tag, lane);
            BAR();
            TS(tb + 15);
            const LAS char* wb = lds + kh * 2048 + lane * 16;
            f32x16 acc[2] = {};
            const int ho = H_OFF + lyr * 8 * MB + (T * 112 + 56 * kh) * 1024;
            u32x4 xa[56];
#pragma unroll
            for (int s = 0; s < PD0; ++s) { LDA(xa, s, ho); SB(); }
            auto woff = [](int s) { return w2_slot(s); };
            bf16x8 wf[4][2];
#pragma unroll
            for (int s = 0; s < 3; ++s) { wf[s][0] = ldsfrag(wb + woff(s)); wf[s][1] = ldsfrag(wb + woff(s) + 1024); }
#pragma unroll
            for (int s = 0; s < 56; ++s) {
                if (s == 18) { BAR(); TS(tb + 16); }
                if (s == 35) { BAR(); TS(tb + 17); }
#pragma unroll
                for (int j = (s == 0 ? PD0 : a_issued(s - 1, 56)); j < a_issued(s, 56); ++j) LDA(xa, j, ho);
                if (s + 3 < 56) { wf[(s + 3) & 3][0] = ldsfrag(wb + woff(s + 3)); wf[(s + 3) & 3][1] = ldsfrag(wb + woff(s + 3) + 1024); }
                const bf16x8 x = asfrag(xa[s]);
                acc[0] = mfma32(wf[s & 3][0], x, acc[0]); acc[1] = mfma32(wf[s & 3][1], x, acc[1]);
                SGB(0x100, 1); SGB(0x8, 1); SGB(0x20, 2); SGB(0x100, 1); SGB(0x8, 1);
                SB();
            }
            const f32x16 snd = kh ? acc[0] : acc[1];
#pragma unroll
            for (int g4 = 0; g4 < 4; ++g4) *(LAS f32x4*)(lds + L_RED4 + ((w * 4 + g4) * 64 + lane) * 16) = f32x4{snd[4 * g4], snd[4 * g4 + 1], snd[4 * g4 + 2], snd[4 * g4 + 3]};
            TS(tb + 18);
            BAR();
            const f32x16 own = kh ? acc[1] : acc[0];
            const int pw = w ^ 2;
            float v[16];
#pragma unroll
            for (int g4 = 0; g4 < 4; ++g4) { f32x4 t = *(LAS f32x4*)(lds + L_RED4 + ((pw * 4 + g4) * 64 + lane) * 16);
#pragma unroll
                for (int i = 0; i < 4; ++i) v[4 * g4 + i] = own[4 * g4 + i] + t[i]; }
            const unsigned rr[8] = {r0.x, r0.y, r0.z, r0.w, r1.x, r1.y, r1.z, r1.w};
            unsigned c[8];
#pragma unroll
            for (int i = 0; i < 8; ++i) c[i] = pk(v[2 * i] + bflo(rr[i]), v[2 * i + 1] + bfhi(rr[i]));
            if (lyr == 0) {
                stw(rs, lane * 16, XF_OFF + 2 * MB + (T * 32 + 2 * u) * 1024, u32x4{c[0], c[1], c[2], c[3]});
                stw(rs, lane * 16 + 1024, XF_OFF + 2 * MB + (T * 32 + 2 * u) * 1024, u32x4{c[4], c[5], c[6], c[7]});
                stats_store(rs, 2, T, u, lane, c);
                DRAIN();
                BAR();
                if (w == 0 && lane == 0) set_flag(p, 5, g, r, tag);
            } else {
                auto s0 = __builtin_amdgcn_permlane32_swap(c[0], c[2], false, false);
                auto s1 = __builtin_amdgcn_permlane32_swap(c[1], c[3], false, false);
                auto s2 = __builtin_amdgcn_permlane32_swap(c[4], c[6], false, false);
                auto s3 = __builtin_amdgcn_permlane32_swap(c[5], c[7], false, false);
                uint16_t* orow = p.out + (size_t)(T * 32 + (lane & 31)) * DM + 32 * u + 8 * h;
                *(GAS u32x4*)(orow) = u32x4{s0[0], s1[0], s0[1], s1[1]};
                *(GAS u32x4*)(orow + 16) = u32x4{s2[0], s3[0], s2[1], s3[1]};
            }
            TS(tb + 19);
        }
    }
    asm volatile("s_waitcnt lgkmcnt(0)" ::: "memory");
    if (w == 0) TS_DUMP();
}

template <int PART> DI void pretile(const Params& p, LAS char* lds, unsigned lds0, rsrc_t rs, int hw, int g, int r, int lane) {
    const int h = lane >> 5, a = (lane & 15) >> 2, pp = lane & 3, ch = (lane >> 4) & 1;
    const int n = r >> 1, j = 2 * g + (r & 1);
    const unsigned scr0 = lds0 + L_SCR + hw * 8192;
    const LAS char* scr = lds + L_SCR + hw * 8192;
    if (hw == 0) {
        const char* base = (const char*)(p.wqkv + (size_t)32 * g * 1536 + 32 * r);
        if (PART == 0) {
#pragma unroll
            for (int i = 0; i < 6; ++i) { const int L = 64 * i + lane, k = L / 12, c = L % 12; glds16s<false>(base, (unsigned)(k * 1536 + (c >> 2) * 512 + (c & 3) * 8) * 2, scr0 + i * 1024); }
            return;
        }
        DRAIN();
        const LAS char* wb = scr + (4 * h + a) * 192 + ch * 32 + pp * 8;
#pragma unroll
        for (int s = 0; s < 2; ++s)
#pragma unroll
            for (int t = 0; t < 3; ++t) stw(rs, lane * 16, WQT_OFF + (r * 96 + ((t + 2) % 3) * 32 + 2 * g + s) * 1024, wfrag_tr(wb + s * 16 * 192 + t * 64, 8 * 192));
    } else if (hw == 1) {
        const char* base = (const char*)(p.w1 + (size_t)32 * g * FF + 112 * r);
        if (PART == 0) {
#pragma unroll
            for (int b = 0; b < 7; ++b) {
                const int L = 64 * b + lane, G = L / 56, x = L - G * 56;
                const int aa = x < 48 ? ((x >> 2) & 3) : ((x - 48) >> 1);
                const int c = x < 48 ? ((x >> 4) * 4 + (x & 3)) : (12 + (x & 1));
                glds16s<false>(base, (unsigned)((4 * G + aa) * FF + c * 8) * 2, scr0 + b * 1024);
            }
            return;
        }
        DRAIN();
        const LAS char* wbA = scr + h * 896 + a * 64 + ch * 32 + pp * 8;
        const LAS char* wbB = scr + h * 896 + 768 + a * 32 + pp * 8;
#pragma unroll
        for (int s = 0; s < 2; ++s) {
            const int dst = W1T_OFF + r * 114688 + (2 * g + s) * 3584;
#pragma unroll
            for (int t = 0; t < 3; ++t) stw(rs, lane * 16, dst + t * 1024, wfrag_tr(wbA + s * 3584 + t * 256, 1792));
            const u32x4 f3 = wfrag_tr(wbB + s * 3584, 1792);
            if (ch == 0) stw(rs, (h * 16 + (lane & 15)) * 16, dst + 3072, f3);
        }
    } else {
        const int rr = lane >> 3, slot = lane & 7, chunk = slot ^ (((rr >> 1) & 1) << 2);
        const unsigned vo = (unsigned)(rr * DM + chunk * 8) * 2;
        const int s0 = hw == 3 ? j : (j < 16 ? 4 * j : 64 + 3 * (j - 16)), ns = hw == 3 ? 1 : (j < 16 ? 4 : 3);
        const char* base = (const char*)((hw == 3 ? p.wo : p.w2) + (size_t)16 * s0 * DM + 64 * n);
        if (PART == 0) {
            for (int i = 0; i < 2 * ns; ++i) glds16s<false>(base + (size_t)i * 8 * DM * 2, vo, scr0 + i * 1024);
            return;
        }
        DRAIN();
        const int sw = ((a >> 1) & 1) << 6;
        const LAS char* wb0 = scr + (4 * h + a) * 128 + (0 ^ sw) + ch * 32 + pp * 8;
        const LAS char* wb1 = scr + (4 * h + a) * 128 + (64 ^ sw) + ch * 32 + pp * 8;
        for (int i = 0; i < ns; ++i) {
            const int s = s0 + i;
            const int kh = s >= 56 ? 1 : 0, sl = s - 56 * kh;
            const int dst = hw == 3 ? WOT_OFF + n * 65536 + s * 2048 : W2T_OFF + (n * 56 + sl) * 4096 + kh * 2048;
            stw(rs, lane * 16, dst, wfrag_tr(wb0 + i * 2048, 1024));
            stw(rs, lane * 16, dst + 1024, wfrag_tr(wb1 + i * 2048, 1024));
        }
    }
    DRAIN();
}

constexpr int w2_regsp(int j) { return j < 9 ? j : j < 29 ? j + 7 : j + 9; }
DI void helper_path(const Params& p, LAS char* lds, unsigned lds0_, rsrc_t rs, const int w, const int g_, const int r_) {
    unsigned tag = 0;
    TS_DECL();
    {   const int layer = 0; PHASE_VARS(); HELPER_VARS();
        tag = read_tag(p); asm volatile("" :: "s"(tag) : "memory");
        if (hw == 3) {
            unsigned keep;
            const unsigned vo = (unsigned)(lane < 44 ? lane : 43) << 20;
            asm volatile("s_mov_b32 %0, m0\n\ts_mov_b32 m0, %3\n\ts_nop 0\n\tglobal_load_lds_dword %1, %2\n\ts_mov_b32 m0, %0" : "=&s"(keep) : "v"(vo), "s"(p.ws), "s"(lds0 + 140 * KB) : "memory");
            const unsigned vo2 = (unsigned)(lane & 1) << 20;
            asm volatile("s_mov_b32 %0, m0\n\ts_mov_b32 m0, %3\n\ts_nop 0\n\tglobal_load_lds_dword %1, %2\n\ts_mov_b32 m0, %0" : "=&s"(keep) : "v"(vo2), "s"(p.out), "s"(lds0 + 141 * KB) : "memory");
        }
        pretile<0>(p, lds, lds0, rs, hw, g, r, lane);
        if (hw == 0) { pretile<1>(p, lds, lds0, rs, hw, g, r, lane); BAR(); }
        else { BAR(); pretile<1>(p, lds, lds0, rs, hw, g, r, lane); }
    }
    for (int layer = 0; layer < 2; ++layer) {
        {
            PHASE_VARS(); HELPER_VARS();
            HTS(layer * 20 + 0);
            BAR();
            if (w == 7 && lane < 2) *(LAS u32x4*)(lds + L_ONES + lane * 1024) = u32x4{0x3f803f80u, 0x3f803f80u, 0x3f803f80u, 0x3f803f80u};
            float accq = 0.f;
            if (lyr == 0) {
                if (w == 6 && lane == 0) set_flag(p, 10, g, r, tag);
                {
                    const char* src = wsb + WQT_OFF + r * 98304;
                    for (int j = hw; j < 96; j += 4) { const int off = (j < 64 ? (j & 1) * 32 + (j >> 1) : j) * 1024; glds16s<true>(src + off, lane * 16, lds0 + L_WQ + off); }
                }
                const LAS char* wbq = lds + L_WQ + (hw < 2 ? hw : 0) * 32768 + lane * 16;
                float acc = 0.f;
                WAITV(20); BAR();
                acc = colsum_frags<8>(wbq, [](int i) { return (0 + i) * 1024; }, acc);
                WAITV(16); BAR();
                acc = colsum_frags<8>(wbq, [](int i) { return (8 + i) * 1024; }, acc);
                WAITV(12); BAR();
                acc = colsum_frags<8>(wbq, [](int i) { return (16 + i) * 1024; }, acc);
                WAITV(8); BAR();
                acc = colsum_frags<8>(wbq, [](int i) { return (24 + i) * 1024; }, acc);
                acc = sum_xor32(acc);
                if (h == 0 && hw < 2) ((LAS float*)(lds + L_CSQ))[32 + hw * 32 + tok] = acc;
                wait_word(p, flagp(p, 10, lane & 15, (lane & 16) ? (r ^ 1) : r), tag);
                DRAIN();
            }
            BAR();
            HTS(layer * 20 + 2);
            stage_lin(wsb, lds0, WOT_OFF + n * 65536, L_WO, 64, hw, lane);
            if (lyr == 0 && hw == 2) {
                const LAS char* wbq = lds + L_WQ + 65536 + lane * 16;
#pragma unroll
                for (int q4 = 0; q4 < 4; ++q4) accq = colsum_frags<8>(wbq + q4 * 8192, [](int i) { return i * 1024; }, accq);
                accq = sum_xor32(accq);
                if (h == 0) ((LAS float*)(lds + L_CSQ))[tok] = accq;
            }
            HTS(layer * 20 + 3);
            BAR();
            BAR();
        }
        u32x4 buf[47];
        {   PHASE_VARS(); HELPER_VARS();

            attn_phase(p, lds, rs, w, g, r, lyr, lane, tag TS_ARG(layer == 0 ? 40 : 44));
            HTS(layer * 20 + 4);
            DRAIN();
            HTS(layer * 20 + 5);
            BAR();
            stage_lin(wsb, lds0, W1T_OFF + r * 114688, 64 * KB, 32, hw, lane);
            stage_lin(wsb, lds0, W1T_OFF + r * 114688 + 32256, 96 * KB, 46, hw, lane);
            HTS(layer * 20 + 6);
        }
        {
            PHASE_VARS(); HELPER_VARS();
            const int wsrc = W2T_OFF + n * 56 * 4096 + hw * 1024;
            BAR();
#pragma unroll
            for (int j = 0; j < 12; ++j) { buf[j] = ldw(rs, lane * 16, wsrc + w2_regsp(j) * 4096); SB(); }
            BAR();
#pragma unroll
            for (int j = 12; j < 22; ++j) { buf[j] = ldw(rs, lane * 16, wsrc + w2_regsp(j) * 4096); SB(); }
            BAR();
            stage_lin(wsb, lds0, W1T_OFF + r * 114688 + 77 * KB, 0, 35, hw, lane);
        }
        {
            PHASE_VARS(); HELPER_VARS();
            HTS(layer * 20 + 8);
            WAITV(30);
            BAR();
            const int wsrc = W2T_OFF + n * 56 * 4096 + hw * 1024;
            stage_lin(wsb, lds0, W2T_OFF + (n * 56 + 9) * 4096, 36 * KB, 28, hw, lane);
            stage_lin(wsb, lds0, W2T_OFF + (n * 56 + 36) * 4096, 144 * KB, 8, hw, lane);
#pragma unroll
            for (int j = 22; j < 34; ++j) { buf[j] = ldw(rs, lane * 16, wsrc + w2_regsp(j) * 4096); SB(); }
            WAITV(21);
            HTS(layer * 20 + 10);
            BAR();
#pragma unroll
            for (int j = 34; j < 47; ++j) { buf[j] = ldw(rs, lane * 16, wsrc + w2_regsp(j) * 4096); SB(); }
            if (lyr == 0) {
                LAS float* cs1 = (LAS float*)(lds + L_CS1);
                const LAS char* wb = hw < 3 ? lds + hw * 1024 + lane * 16 : lds + 3072 + (h * 16 + (lane & 15)) * 16;
                float cs_part = colsum_frags<11>(wb, [](int i) { return w1_off(i); }, 0.f);
                cs_part = colsum_frags<11>(wb, [](int i) { return w1_off(11 + i); }, cs_part);
                cs_part = colsum_frags<10>(wb, [](int i) { return w1_off(22 + i); }, cs_part);
                const float s = sum_xor32(cs_part);
                if (h == 0 && (hw < 3 || tok < 16)) cs1[hw * 32 + tok] = s;
            }
            WAITV(13);
            BAR();
            HTS(layer * 20 + 11);
#pragma unroll
            for (int j = 0; j < 29; ++j) *(LAS u32x4*)(lds + w2_slot(w2_regsp(j)) + hw * 1024 + lane * 16) = buf[j];
            if (lyr == 0) {
#pragma unroll
                for (int j = 0; j < 24; ++j) { buf[j] = ldw(rs, lane * 16, WQT_OFF + r * 98304 + (4 * j + hw) * 1024); SB(); }
            }
            HTS(layer * 20 + 12);
            BAR();
        }
        {
            PHASE_VARS(); HELPER_VARS();
            BAR();
            BAR();
#pragma unroll
            for (int j = 29; j < 47; ++j) *(LAS u32x4*)(lds + w2_slot(w2_regsp(j)) + hw * 1024 + lane * 16) = buf[j];
            if (lyr == 1 && blockIdx.x == 0 && w == 7) {
                bool ok = false;
                for (unsigned spins = 0; spins < (1u << 16) && !ok; ++spins) {
                    bool mine = true;
#pragma unroll
                    for (int j = 0; j < 4; ++j) mine &= __hip_atomic_load((gu32*)(p.ws + ACK_OFF) + (lane + 64 * j) * 32, __ATOMIC_RELAXED, __HIP_MEMORY_SCOPE_AGENT) == tag;
                    ok = __all(mine);
                }
                unsigned tg = tag; asm volatile("" : "+s"(tg));
                if (lane == 0) __hip_atomic_store((gu32*)(p.ws + EPOCH_OFF), tg, __ATOMIC_RELAXED, __HIP_MEMORY_SCOPE_AGENT);
            }
            HTS(layer * 20 + 15);
            BAR();
            BAR();
            if (lyr == 0) {
#pragma unroll
                for (int j = 0; j < 24; ++j) *(LAS u32x4*)(lds + L_WQ + (4 * j + hw) * 1024 + lane * 16) = buf[j];
                HTS(17);
                BAR();
            }
        }
    }
    asm volatile("s_waitcnt lgkmcnt(0)" ::: "memory");
    if (w == 4) HTS_DUMP();
}

__global__ __launch_bounds__(512, 2) void k_mega(Params p) {
    extern __shared__ __attribute__((aligned(16))) char smem_[];
    LAS char* lds = (LAS char*)smem_;
    const unsigned lds0 = (unsigned)(uintptr_t)smem_;
    const int b = blockIdx.x, idx = b >> 3;
    const int g_ = ((b & 7) >> 1) * 4 + (idx >> 3), r_ = (b & 1) * 8 + (idx & 7);
    const int w = __builtin_amdgcn_readfirstlane(threadIdx.x >> 6);
    const rsrc_t rs = __builtin_amdgcn_make_buffer_rsrc((void*)p.ws, 0, WS_USED, 0x00020000);
    if (w < 4) main_path(p, lds, rs, w, g_, r_);
    else helper_path(p, lds, lds0, rs, w, g_, r_);
}
}

extern "C" void kernel_launch(void* const* d_in, const int* in_sizes, int n_in, void* d_out, int out_size, void* d_ws, size_t ws_size, hipStream_t stream) {
    Params p;
    p.x = (const uint16_t*)d_in[0]; p.wqkv = (const uint16_t*)d_in[1]; p.wo = (const uint16_t*)d_in[2];
    p.w1 = (const uint16_t*)d_in[4]; p.w2 = (const uint16_t*)d_in[5];
    p.out = (uint16_t*)d_out; p.ws = (char*)d_ws;
    (void)hipFuncSetAttribute((const void*)k_mega, hipFuncAttributeMaxDynamicSharedMemorySize, LDS_BYTES);
    k_mega<<<256, 512, LDS_BYTES, stream>>>(p);
}
```

```cpp
#include <hip/hip_runtime.h>
#include <hip/hip_bf16.h>
#include <stdint.h>

#define GAS __attribute__((address_space(1)))
#define LAS __attribute__((address_space(3)))
#define DI __device__ __forceinline__

typedef __bf16 bf16x8 __attribute__((ext_vector_type(8)));
typedef __bf16 bf16x2 __attribute__((ext_vector_type(2)));
typedef short s16x4 __attribute__((ext_vector_type(4)));
typedef float f32x16 __attribute__((ext_vector_type(16)));
typedef float f32x4 __attribute__((ext_vector_type(4)));
typedef float f32x2 __attribute__((ext_vector_type(2)));
typedef unsigned u32x4 __attribute__((ext_vector_type(4)));
typedef unsigned u32x2 __attribute__((ext_vector_type(2)));
typedef GAS unsigned gu32;

#define TS_DECL()
#define TS_PARAM
#define TS_ARG(tb)
#define TS(i)
#define TS_DUMP()
#define HTS(i)
#define HTS_DUMP()
#define MAPMODE 3
namespace {
constexpr int DM = 512, FF = 1792;
constexpr float EPS = 1e-5f;
constexpr float LOG2E = 1.4426950408889634f;
constexpr float QSCALE = 0.25f * LOG2E;

constexpr int MB = 1 << 20, KB = 1024;
constexpr int XF_OFF = 0;
constexpr int O_OFF = 4 * MB;
constexpr int X2_OFF = 8 * MB;
constexpr int H_OFF = 12 * MB;
constexpr int KV_OFF = 28 * MB;
constexpr int WQT_OFF = 36 * MB;
constexpr int WOT_OFF = WQT_OFF + 1536 * KB;
constexpr int W1T_OFF = 38 * MB;
constexpr int W2T_OFF = 40 * MB;
constexpr int ST_OFF = 42 * MB;
constexpr int ST_BUF = 262144;
constexpr int KN_OFF = 43 * MB;
constexpr int FLAG_OFF = 43 * MB + 65536;
constexpr int FLAG_BYTES = 11 * 256 * 128;
constexpr int TMO_OFF = FLAG_OFF + FLAG_BYTES;
constexpr int EPOCH_OFF = TMO_OFF + 256;
constexpr int ACK_OFF = EPOCH_OFF + 256;
constexpr int WS_USED = 44 * MB;

constexpr int L_WQ = 0;
constexpr int L_Q = 0, L_KV = 8 * KB, L_ONES = 91 * KB;
constexpr int L_RED2 = 40 * KB;
constexpr int wo_off(int s) { return s < 31 ? 96 * KB + s * 2048 : 94 * KB; }
constexpr int L_RED4 = 96 * KB;
constexpr int L_SCR = 100 * KB;
constexpr int L_TAB = 158 * KB, L_CSQ = L_TAB, L_CS1 = L_TAB + 512, L_STT = L_TAB + 1024;
constexpr int L_KN = L_TAB + 384, L_QN = L_TAB + 384 + 96;
constexpr int LDS_BYTES = 160 * KB;
constexpr int w1_off(int s) { return s < 10 ? 56 * KB + s * 3584 : s < 21 ? (s - 10) * 3584 : 94 * KB + (s - 21) * 3584; }
constexpr int w2_slot(int sp) { return (sp < 38 ? sp : sp - 38) * 4096; }
constexpr int AUX_SC1 = 16;

struct Params {
    const uint16_t* x; const uint16_t* wqkv; const uint16_t* wo; const uint16_t* w1; const uint16_t* w2;
    uint16_t* out; char* ws;
};

DI int lane_id() { return (int)__builtin_amdgcn_mbcnt_hi(~0u, __builtin_amdgcn_mbcnt_lo(~0u, 0u)); }
DI f32x16 mfma32(bf16x8 a, bf16x8 b, f32x16 c) { return __builtin_amdgcn_mfma_f32_32x32x16_bf16(a, b, c, 0, 0, 0); }
DI unsigned pk(float a, float b) { bf16x2 v = {(__bf16)a, (__bf16)b}; return __builtin_bit_cast(unsigned, v); }
DI float bflo(unsigned u) { return __uint_as_float(u << 16); }
DI float bfhi(unsigned u) { return __uint_as_float(u & 0xffff0000u); }
DI bf16x8 asfrag(u32x4 v) { return __builtin_bit_cast(bf16x8, v); }
DI s16x4 trd(const LAS char* p) { return __builtin_amdgcn_ds_read_tr16_b64_v4i16((LAS s16x4*)p); }
DI u32x4 wfrag_tr(const LAS char* p, int off2) {
    const u32x2 lo = __builtin_bit_cast(u32x2, trd(p)), hi = __builtin_bit_cast(u32x2, trd(p + off2));
    return u32x4{lo.x, lo.y, hi.x, hi.y};
}
DI bf16x8 ldsfrag(const LAS char* p) { return *(const LAS bf16x8*)p; }
template <int N, class F> DI float colsum_frags(const LAS char* p, F off, float acc) {
    u32x4 f[N];
#pragma unroll
    for (int i = 0; i < N; ++i) f[i] = *(const LAS u32x4*)(p + off(i));
    float a1 = 0.f;
#pragma unroll
    for (int i = 0; i < N; ++i) {
        unsigned w0 = f[i].x, w1 = f[i].y, w2 = f[i].z, w3 = f[i].w;
        asm("v_dot2c_f32_bf16 %0, %2, %6\n\tv_dot2c_f32_bf16 %1, %3, %6\n\tv_dot2c_f32_bf16 %0, %4, %6\n\tv_dot2c_f32_bf16 %1, %5, %6"
            : "+v"(acc), "+v"(a1) : "v"(w0), "v"(w1), "v"(w2), "v"(w3), "v"(0x3f803f80u));
    }
    asm volatile("s_nop 2" : "+v"(acc), "+v"(a1));
    return acc + a1;
}
DI float sum_xor32(float v) { auto r = __builtin_amdgcn_permlane32_swap(__float_as_uint(v), __float_as_uint(v), false, false); return __uint_as_float(r[0]) + __uint_as_float(r[1]); }
DI float max_xor32(float v) { auto r = __builtin_amdgcn_permlane32_swap(__float_as_uint(v), __float_as_uint(v), false, false); return fmaxf(__uint_as_float(r[0]), __uint_as_float(r[1])); }
template <bool SC1> DI void glds16s(const void* sbase, unsigned voff, unsigned lds_addr) {
    unsigned keep;
    if (SC1) asm volatile("s_mov_b32 %0, m0\n\ts_mov_b32 m0, %3\n\ts_nop 0\n\tglobal_load_lds_dwordx4 %1, %2 sc1\n\ts_mov_b32 m0, %0" : "=&s"(keep) : "v"(voff), "s"(sbase), "s"(lds_addr) : "memory");
    else asm volatile("s_mov_b32 %0, m0\n\ts_mov_b32 m0, %3\n\ts_nop 0\n\tglobal_load_lds_dwordx4 %1, %2\n\ts_mov_b32 m0, %0" : "=&s"(keep) : "v"(voff), "s"(sbase), "s"(lds_addr) : "memory");
}
#define BAR() asm volatile("s_waitcnt lgkmcnt(0)\n\ts_barrier" ::: "memory")
#define DRAIN() asm volatile("s_waitcnt vmcnt(0)" ::: "memory")
#define WAITV(n) asm volatile("s_waitcnt vmcnt(" #n ")" ::: "memory")
#define SB() __builtin_amdgcn_sched_barrier(0)
#define SGB(mask, n) __builtin_amdgcn_sched_group_barrier(mask, n, 0)

typedef __amdgpu_buffer_rsrc_t rsrc_t;
DI u32x4 ldw(rsrc_t rs, int voff, int soff) { return __builtin_amdgcn_raw_buffer_load_b128(rs, voff, soff, AUX_SC1); }
DI void stw(rsrc_t rs, int voff, int soff, u32x4 v) { __builtin_amdgcn_raw_buffer_store_b128(v, rs, voff + soff, 0, AUX_SC1); }
DI f32x2 ldst(rsrc_t rs, int voff, int soff) { return __builtin_bit_cast(f32x2, __builtin_amdgcn_raw_buffer_load_b64(rs, voff, soff, AUX_SC1)); }
DI void stst(rsrc_t rs, int voff, int soff, f32x2 v) { __builtin_amdgcn_raw_buffer_store_b64(__builtin_bit_cast(u32x2, v), rs, voff, soff, AUX_SC1); }

#define FSTRIDE 32
DI gu32* flagp(const Params& p, int ph, int g, int r) { return (gu32*)(p.ws + FLAG_OFF) + (ph * 16 + g) * 16 * 32 + r * FSTRIDE; }
DI void set_flag(const Params& p, int ph, int g, int r, unsigned tag) { __hip_atomic_store(flagp(p, ph, g, r), tag, __ATOMIC_RELAXED, __HIP_MEMORY_SCOPE_AGENT); }
DI void wait_word(const Params& p, gu32* a, unsigned tag) {
    for (unsigned spins = 0;; ++spins) {
        const unsigned v = __hip_atomic_load(a, __ATOMIC_RELAXED, __HIP_MEMORY_SCOPE_AGENT);
        if (__all(v == tag)) break;
        if (spins > (1u << 20)) { __hip_atomic_store((gu32*)(p.ws + TMO_OFF), 1u, __ATOMIC_RELAXED, __HIP_MEMORY_SCOPE_AGENT); break; }
        __builtin_amdgcn_s_sleep(1);
    }
    __builtin_amdgcn_fence(__ATOMIC_ACQUIRE, "wavefront");
}
DI void wait_group(const Params& p, int ph, int g, unsigned tag, int lane) { wait_word(p, flagp(p, ph, g, lane & 15), tag); }

DI void stats_store(rsrc_t rs, int buf, int T, int u, int lane, const unsigned (&c)[8]) {
    float s = 0.f, ss = 0.f;
#pragma unroll
    for (int i = 0; i < 8; ++i) { float lo = bflo(c[i]), hi = bfhi(c[i]); s += lo + hi; ss += lo * lo + hi * hi; }
    s = sum_xor32(s); ss = sum_xor32(ss);
    if (lane < 32) stst(rs, lane * 8, ST_OFF + buf * ST_BUF + (T * 16 + u) * 256, f32x2{s, ss});
}

DI void stage_lin(const char* wsb, unsigned lds0, int src, int dst, int n, int hw, int lane, int nw = 4) {
    const unsigned vo = lane * 16;
    for (int i = hw; i < n; i += nw) glds16s<true>(wsb + src + i * 1024, vo, lds0 + dst + i * 1024);
}

#define HELPER_VARS() \
    int hw = w - 4; asm volatile("" : "+s"(hw)); \
    const char* wsb = p.ws; asm volatile("" : "+s"(wsb)); \
    unsigned lds0 = lds0_; asm volatile("" : "+s"(lds0)); (void)hw; (void)wsb; (void)lds0;
#define PHASE_VARS() \
    int lane = lane_id(); asm volatile("" : "+v"(lane)); \
    int lyr = layer; asm volatile("" : "+s"(lyr)); \
    int g = g_, r = r_; asm volatile("" : "+s"(g), "+s"(r)); \
    const int h = lane >> 5, tok = lane & 31; const int m = r & 1, n = r >> 1; (void)h; (void)tok; (void)m; (void)n; (void)lyr;

DI void attn_finish(rsrc_t rs, const f32x16& o, int hd, int T, int r, int lyr, int lane) {
    float ov[8]; float l;
    if (hd == 0) { l = o[8];
#pragma unroll
        for (int i = 0; i < 8; ++i) ov[i] = o[i];
    } else { l = o[0];
#pragma unroll
        for (int i = 0; i < 8; ++i) ov[i] = o[8 + i];
    }
    const float inv = __builtin_amdgcn_rcpf(l);
    stw(rs, lane * 16, O_OFF + lyr * 2 * MB + (T * 32 + 2 * r + hd) * 1024,
        u32x4{pk(ov[0] * inv, ov[1] * inv), pk(ov[2] * inv, ov[3] * inv), pk(ov[4] * inv, ov[5] * inv), pk(ov[6] * inv, ov[7] * inv)});
}
DI float max16(float m, const f32x16& s) {
#pragma unroll
    for (int i = 0; i < 16; i += 2) m = fmaxf(fmaxf(m, s[i]), s[i + 1]);
    return m;
}
DI void max16x2(float& m0, float& m1, const f32x16& s) {
#pragma unroll
    for (int i = 0; i < 16; i += 4) { m0 = fmaxf(fmaxf(m0, s[i]), s[i + 1]); m1 = fmaxf(fmaxf(m1, s[i + 2]), s[i + 3]); }
}
DI void exp_pack(const f32x16& s, u32x4& p0, u32x4& p1) {
    unsigned pw[8];
#pragma unroll
    for (int i = 0; i < 8; ++i) pw[i] = pk(__builtin_amdgcn_exp2f(s[2 * i]), __builtin_amdgcn_exp2f(s[2 * i + 1]));
    p0 = u32x4{pw[0], pw[1], pw[2], pw[3]}; p1 = u32x4{pw[4], pw[5], pw[6], pw[7]};
}
DI float wave32_max(float v) {
    int x = __float_as_int(v);
    x = max(x, __builtin_amdgcn_update_dpp(x, x, 0x111, 0xf, 0xf, false));
    x = max(x, __builtin_amdgcn_update_dpp(x, x, 0x112, 0xf, 0xf, false));
    x = max(x, __builtin_amdgcn_update_dpp(x, x, 0x114, 0xf, 0xf, false));
    x = max(x, __builtin_amdgcn_update_dpp(x, x, 0x118, 0xf, 0xf, false));
    x = max(x, __builtin_amdgcn_update_dpp(x, x, 0x142, 0xa, 0xf, false));
    return __int_as_float(__builtin_amdgcn_readlane(x, 31));
}
DI void attn_seg(unsigned ka, unsigned va, int ks, int vs, int n, const bf16x8 qf, const f32x16 c0, f32x16& o) {
    int d;
    asm volatile(
        "ds_read_b128 v[248:251], %[ka]\n\tv_add_u32 %[ka], %[ks], %[ka]\n\tds_read_b128 v[236:239], %[ka]\n\tv_add_u32 %[ka], %[ks], %[ka]\n\t"
        "ds_read_b128 v[240:243], %[va]\n\tds_read_b128 v[244:247], %[va] offset:1024\n\tv_add_u32 %[va], %[vs], %[va]\n\t"
        "s_lshr_b32 %[d], %[n], 1\n\ts_waitcnt lgkmcnt(3)\n\t"
        "v_mfma_f32_32x32x16_bf16 v[204:219], v[248:251], %[q], %[c0]\n\t"
        "s_cmp_eq_u32 %[d], 0\n\ts_nop 10\n\ts_cbranch_scc1 Ltail_%=\n"
        "Lloop_%=:\n\t"
        "v_exp_f32 v204, v204\n\t"
        "v_exp_f32 v205, v205\n\t"
        "v_exp_f32 v206, v206\n\t"
        "v_exp_f32 v207, v207\n\t"
        "v_exp_f32 v208, v208\n\t"
        "v_exp_f32 v209, v209\n\t"
        "v_exp_f32 v210, v210\n\t"
        "v_exp_f32 v211, v211\n\t"
        "v_cvt_pk_bf16_f32 v204, v204, v205\n\t"
        "v_cvt_pk_bf16_f32 v205, v206, v207\n\t"
        "v_cvt_pk_bf16_f32 v206, v208, v209\n\t"
        "v_cvt_pk_bf16_f32 v207, v210, v211\n\t"
        "s_waitcnt lgkmcnt(1)\n\ts_nop 0\n\t"
        "v_mfma_f32_32x32x16_bf16 %[o], v[240:243], v[204:207], %[o]\n\t"
        "v_exp_f32 v212, v212\n\t"
        "v_exp_f32 v213, v213\n\t"
        "v_exp_f32 v214, v214\n\t"
        "v_exp_f32 v215, v215\n\t"
        "v_mfma_f32_32x32x16_bf16 v[220:235], v[236:239], %[q], 0\n\t"
        "ds_read_b128 v[236:239], %[ka]\n\tds_read_b128 v[248:251], %[va]\n\tds_read_b128 v[252:255], %[va] offset:1024\n\t"
        "v_add_u32 %[ka], %[ks], %[ka]\n\tv_add_u32 %[va], %[vs], %[va]\n\t"
        "v_exp_f32 v216, v216\n\t"
        "v_exp_f32 v217, v217\n\t"
        "v_exp_f32 v218, v218\n\t"
        "v_exp_f32 v219, v219\n\t"
        "v_cvt_pk_bf16_f32 v212, v212, v213\n\t"
        "v_cvt_pk_bf16_f32 v213, v214, v215\n\t"
        "v_cvt_pk_bf16_f32 v214, v216, v217\n\t"
        "v_cvt_pk_bf16_f32 v215, v218, v219\n\t"
        "s_waitcnt lgkmcnt(3)\n\ts_nop 0\n\t"
        "v_mfma_f32_32x32x16_bf16 %[o], v[244:247], v[212:215], %[o]\n\t"
        "v_exp_f32 v220, v220\n\t"
        "v_exp_f32 v221, v221\n\t"
        "v_exp_f32 v222, v222\n\t"
        "v_exp_f32 v223, v223\n\t"
        "v_exp_f32 v224, v224\n\t"
        "v_exp_f32 v225, v225\n\t"
        "v_exp_f32 v226, v226\n\t"
        "v_exp_f32 v227, v227\n\t"
        "v_cvt_pk_bf16_f32 v220, v220, v221\n\t"
        "v_cvt_pk_bf16_f32 v221, v222, v223\n\t"
        "v_cvt_pk_bf16_f32 v222, v224, v225\n\t"
        "v_cvt_pk_bf16_f32 v223, v226, v227\n\t"
        "s_waitcnt lgkmcnt(1)\n\ts_nop 0\n\t"
        "v_mfma_f32_32x32x16_bf16 %[o], v[248:251], v[220:223], %[o]\n\t"
        "v_exp_f32 v228, v228\n\t"
        "v_exp_f32 v229, v229\n\t"
        "v_exp_f32 v230, v230\n\t"
        "v_exp_f32 v231, v231\n\t"
        "v_mfma_f32_32x32x16_bf16 v[204:219], v[236:239], %[q], 0\n\t"
        "ds_read_b128 v[236:239], %[ka]\n\tds_read_b128 v[240:243], %[va]\n\tds_read_b128 v[244:247], %[va] offset:1024\n\t"
        "v_add_u32 %[ka], %[ks], %[ka]\n\tv_add_u32 %[va], %[vs], %[va]\n\t"
        "v_exp_f32 v232, v232\n\t"
        "v_exp_f32 v233, v233\n\t"
        "v_exp_f32 v234, v234\n\t"
        "v_exp_f32 v235, v235\n\t"
        "v_cvt_pk_bf16_f32 v228, v228, v229\n\t"
        "v_cvt_pk_bf16_f32 v229, v230, v231\n\t"
        "v_cvt_pk_bf16_f32 v230, v232, v233\n\t"
        "v_cvt_pk_bf16_f32 v231, v234, v235\n\t"
        "s_waitcnt lgkmcnt(3)\n\ts_nop 0\n\t"
        "v_mfma_f32_32x32x16_bf16 %[o], v[252:255], v[228:231], %[o]\n\t"
        "s_sub_u32 %[d], %[d], 1\n\ts_cmp_lg_u32 %[d], 0\n\ts_cbranch_scc1 Lloop_%=\n"
        "Ltail_%=:\n\t"
        "s_bitcmp1_b32 %[n], 0\n\ts_waitcnt lgkmcnt(0)\n\ts_cbranch_scc0 Lend_%=\n\t"
        "v_exp_f32 v204, v204\n\t"
        "v_exp_f32 v205, v205\n\t"
        "v_exp_f32 v206, v206\n\t"
        "v_exp_f32 v207, v207\n\t"
        "v_exp_f32 v208, v208\n\t"
        "v_exp_f32 v209, v209\n\t"
        "v_exp_f32 v210, v210\n\t"
        "v_exp_f32 v211, v211\n\t"
        "v_cvt_pk_bf16_f32 v204, v204, v205\n\t"
        "v_cvt_pk_bf16_f32 v205, v206, v207\n\t"
        "v_cvt_pk_bf16_f32 v206, v208, v209\n\t"
        "v_cvt_pk_bf16_f32 v207, v210, v211\n\t"
        "s_nop 1\n\t"
        "v_mfma_f32_32x32x16_bf16 %[o], v[240:243], v[204:207], %[o]\n\t"
        "v_exp_f32 v212, v212\n\t"
        "v_exp_f32 v213, v213\n\t"
        "v_exp_f32 v214, v214\n\t"
        "v_exp_f32 v215, v215\n\t"
        "v_exp_f32 v216, v216\n\t"
        "v_exp_f32 v217, v217\n\t"
        "v_exp_f32 v218, v218\n\t"
        "v_exp_f32 v219, v219\n\t"
        "v_cvt_pk_bf16_f32 v212, v212, v213\n\t"
        "v_cvt_pk_bf16_f32 v213, v214, v215\n\t"
        "v_cvt_pk_bf16_f32 v214, v216, v217\n\t"
        "v_cvt_pk_bf16_f32 v215, v218, v219\n\t"
        "s_nop 1\n\t"
        "v_mfma_f32_32x32x16_bf16 %[o], v[244:247], v[212:215], %[o]\n"
        "Lend_%=:\n\ts_nop 11"
        : [o] "+v"(o), [ka] "+v"(ka), [va] "+v"(va), [d] "=&s"(d)
        : [q] "v"(qf), [c0] "v"(c0), [ks] "s"(ks), [vs] "v"(vs), [n] "s"(n)
        : "memory", "scc", "v204", "v205", "v206", "v207", "v208", "v209", "v210", "v211", "v212", "v213", "v214", "v215", "v216", "v217", "v218", "v219", "v220", "v221", "v222", "v223", "v224", "v225", "v226", "v227", "v228", "v229", "v230", "v231", "v232", "v233", "v234", "v235", "v236", "v237", "v238", "v239", "v240", "v241", "v242", "v243", "v244", "v245", "v246", "v247", "v248", "v249", "v250", "v251", "v252", "v253", "v254", "v255");
}
constexpr float BOUND2 = 4096.f;
DI f32x16 attn_exact(LAS char* lds, int tw, int hd, int s0, bool first_masked, const bf16x8 qf, int lane) {
    const int h = lane >> 5, tok = lane & 31;
    const LAS char* kb = lds + L_KV + hd * 1024 + lane * 16;
    const bool isdata = ((lane >> 4) & 1) == hd;
    const LAS char* vb = isdata ? (lds + L_KV + 2048 + lane * 16) : (lds + L_ONES);
    const int vstep = isdata ? 4096 : 0;
    const f32x16 zero = {};
    f32x16 o = {};
    float mx = -INFINITY;
    for (int pass = 0; pass < 2; ++pass) {
        for (int sl = s0; sl <= 8 + tw; ++sl) {
            f32x16 s = mfma32(*(const LAS bf16x8*)(kb + sl * 4096), qf, zero);
            const bool diag = sl == 8 + tw, first = first_masked && sl == s0;
#pragma unroll
            for (int i = 0; i < 16; ++i) { const int kk = (i & 3) + 8 * (i >> 2) + 4 * h; if ((diag && kk > tok) || (first && kk <= tok)) s[i] = -INFINITY; }
            if (pass == 0) mx = max16(mx, s);
            else {
#pragma unroll
                for (int i = 0; i < 16; ++i) s[i] -= mx;
                u32x4 p0, p1; exp_pack(s, p0, p1);
                o = mfma32(*(const LAS bf16x8*)(vb + sl * vstep), asfrag(p0), o);
                o = mfma32(*(const LAS bf16x8*)(vb + sl * vstep + 1024), asfrag(p1), o);
            }
        }
        if (pass == 0) mx = max_xor32(mx);
    }
    return o;
}
DI void attn_phase(const Params& p, LAS char* lds, rsrc_t rs, int w, int g, int r, int lyr, int lane, unsigned tag TS_PARAM) {
    const int h = lane >> 5, tok = lane & 31;
    const int hd = w >> 2, tw = hd ? 3 - (w & 3) : (w & 3), T = 4 * g + tw;
    const bf16x8 qf = *(const LAS bf16x8*)(lds + L_Q + tw * 2048 + hd * 1024 + lane * 16);
    const unsigned kb = (unsigned)(uintptr_t)(lds + L_KV + hd * 1024 + lane * 16);
    const bool isdata = ((lane >> 4) & 1) == hd;
    const unsigned vb = (unsigned)(uintptr_t)(isdata ? (lds + L_KV + 2048 + lane * 16) : (lds + L_ONES));
    const int vstep = isdata ? 4096 : 0;
    const LAS float* kn = (const LAS float*)(lds + L_KN);
    const float qn = ((const LAS float*)(lds + L_QN))[tw * 2 + hd] * 1.05f;
    const float kown = fmaxf(fmaxf(kn[16 + hd], kn[18 + hd]), fmaxf(kn[20 + hd], kn[22 + hd]));
    bool fast = __builtin_amdgcn_readfirstlane(qn * kown <= BOUND2);
    f32x16 t, o = {};
#pragma unroll
    for (int i = 0; i < 16; ++i) t[i] = __builtin_fmaf((float)(tok - 4 * h), 1e30f, (0.5f - (float)((i & 3) + 8 * (i >> 2))) * 1e30f);
    const int s4 = 4 * hd, tile0 = 4 * g - 8 + s4;
    const bool loader = tw == 0 && tile0 >= 0;
    u32x4 hv[16]; f32x2 hn;
    if (loader) {
        wait_word(p, flagp(p, 1 + 5 * lyr, g - 2 + hd, r), tag);
        TS(tb + 1);
        const int kvo = KV_OFF + lyr * 4 * MB + (tile0 * 16 + r) * 4096;
#pragma unroll
        for (int i = 0; i < 16; ++i) hv[i] = ldw(rs, lane * 16 + (i & 3) * 1024, kvo + (i >> 2) * 65536);
        hn = ldst(rs, (lane & 3) * 128, KN_OFF + ((lyr * 64 + tile0) * 16 + r) * 8);
        SB();
    }
    if (fast) {
        f32x16 m;
#pragma unroll
        for (int i = 0; i < 16; ++i) m[i] = fminf(t[i], 0.f);
        attn_seg(kb + (8 + tw) * 4096, vb + (8 + tw) * vstep, -4096, -vstep, tw + 1, qf, m, o);
    }
    TS(tb);
    if (loader) {
        LAS char* kvl = lds + L_KV + s4 * 4096 + lane * 16;
#pragma unroll
        for (int i = 0; i < 16; ++i) *(LAS u32x4*)(kvl + i * 1024) = hv[i];
        if (lane < 4) *(LAS f32x2*)(lds + L_KN + (s4 + lane) * 8) = hn;
    }
    BAR();
    TS(tb + 2);
    const int s0 = T >= 8 ? tw : 8 - 4 * g;
    if (s0 < 8) {
        float kh = 0.f;
#pragma unroll
        for (int j = 0; j < 8; ++j) kh = fmaxf(kh, j >= 8 - 4 * g ? kn[2 * j + hd] : 0.f);
        fast = fast && __builtin_amdgcn_readfirstlane(qn * kh <= BOUND2);
        if (fast) {
            f32x16 m;
#pragma unroll
            for (int i = 0; i < 16; ++i) m[i] = T >= 8 ? fminf(-t[i], 0.f) : 0.f;
            attn_seg(kb + s0 * 4096, vb + s0 * vstep, 4096, vstep, 8 - s0, qf, m, o);
        }
    }
    if (!fast) o = attn_exact(lds, tw, hd, s0, T >= 8, qf, lane);
    attn_finish(rs, o, hd, T, r, lyr, lane);
}
DI void ln_stats(const f32x2 (&sv)[8], float& mean, float& rstd) {
    float S = 0.f, SS = 0.f;
#pragma unroll
    for (int i = 0; i < 8; ++i) { S += sv[i].x; SS += sv[i].y; }
    S = sum_xor32(S); SS = sum_xor32(SS);
    mean = S * (1.f / DM); const float var = fmaxf(SS * (1.f / DM) - mean * mean, 0.f); rstd = __builtin_amdgcn_rsqf(var + EPS);
}
DI unsigned read_tag(const Params& p) {
    return (unsigned)__builtin_amdgcn_readfirstlane(__hip_atomic_load((gu32*)(p.ws + EPOCH_OFF), __ATOMIC_RELAXED, __HIP_MEMORY_SCOPE_AGENT)) + 1u;
}
#define LDA(arr, s, base) arr[s] = ldw(rs, lane * 16 + ((s) & 3) * 1024, (base) + ((s) >> 2) * 4096)

#define WODELAY 36
#define PD0 4
#define PDV 12
constexpr int a_issued(int s, int total) { int v = PD0 + 2 * (s + 1); const int cap = s + 1 + PDV; v = v < cap ? v : cap; return v < total ? v : total; }

DI void main_path(const Params& p, LAS char* lds, rsrc_t rs, const int w, const int g_, const int r_) {
    unsigned tag = 0;
    TS_DECL();
    TS(0);
    {
        const int layer = 0; PHASE_VARS();
        const int T = 4 * g + (r & 3), u = (r >> 2) * 4 + w;
        const uint16_t* row = p.x + (size_t)(T * 32 + tok) * DM + 32 * u + 4 * h;
        u32x2 a0 = *(const GAS u32x2*)(row), a1 = *(const GAS u32x2*)(row + 8), a2 = *(const GAS u32x2*)(row + 16), a3 = *(const GAS u32x2*)(row + 24);
        tag = read_tag(p); asm volatile("" :: "s"(tag) : "memory");
        stw(rs, lane * 16, XF_OFF + (T * 32 + 2 * u) * 1024, u32x4{a0.x, a0.y, a1.x, a1.y});
        stw(rs, lane * 16, XF_OFF + (T * 32 + 2 * u + 1) * 1024, u32x4{a2.x, a2.y, a3.x, a3.y});
        const unsigned c[8] = {a0.x, a0.y, a1.x, a1.y, a2.x, a2.y, a3.x, a3.y};
        stats_store(rs, 0, T, u, lane, c);
        DRAIN();
        BAR();
        if (w == 0 && lane == 0) { set_flag(p, 0, g, r, tag); __hip_atomic_store((gu32*)(p.ws + ACK_OFF) + blockIdx.x * 32, tag, __ATOMIC_RELAXED, __HIP_MEMORY_SCOPE_AGENT); }
    }
    TS(1);
    for (int layer = 0; layer < 2; ++layer) {
        const int tb = 2 + layer * 24; (void)tb;
        {
            PHASE_VARS();
            if (w == 0) {
                if (lyr == 0) {
                    const int j = lane & 15;
                    wait_word(p, (lane & 16) ? flagp(p, 0, j, r) : flagp(p, 0, g, j), tag);
                } else wait_group(p, 5, g, tag, lane);
            }
            BAR();
            TS(tb + 0);
            const int T = 4 * g + w;
            const LAS char* wb = lds + L_WQ + lane * 16;
            LAS float* csq = (LAS float*)(lds + L_CSQ);
            LAS float* stt = (LAS float*)(lds + L_STT);
            f32x16 aq = {}, ak = {}, av = {};
            u32x4 xa[32];
            f32x2 sv[8];
            const int sto = ST_OFF + 2 * lyr * ST_BUF + T * 4096, xo = XF_OFF + lyr * 2 * MB + T * 32768;
#pragma unroll
            for (int s = 0; s < PD0; ++s) { LDA(xa, s, xo); SB(); }
            if (lyr == 0) BAR();
            bf16x8 wf[3][3];
#pragma unroll
            for (int s = 0; s < 2; ++s)
#pragma unroll
                for (int t = 0; t < 3; ++t) wf[s][t] = ldsfrag(wb + (s * 3 + t) * 1024);
#pragma unroll
            for (int s = 0; s < 32; ++s) {
                if ((s == 6 || s == 14 || s == 22) && lyr == 0) BAR();
#pragma unroll
                for (int j = (s == 0 ? PD0 : a_issued(s - 1, 32)); j < a_issued(s, 32); ++j) LDA(xa, j, xo);
                if (s >= 16 && s < 24) sv[s - 16] = ldst(rs, (8 * h * 32 + tok) * 8 + (s - 16) * 256, sto);
                if (s + 2 < 32) {
#pragma unroll
                    for (int t = 0; t < 3; ++t) wf[(s + 2) % 3][t] = ldsfrag(wb + ((s + 2) * 3 + t) * 1024);
                }
                const bf16x8 x = asfrag(xa[s]);
                aq = mfma32(wf[s % 3][0], x, aq); ak = mfma32(wf[s % 3][1], x, ak); av = mfma32(x, wf[s % 3][2], av);
                SGB(0x100, 1); SGB(0x8, 1); SGB(0x20, 1); SGB(0x100, 1); SGB(0x8, 1); SGB(0x20, 1); SGB(0x100, 1); SGB(0x8, 1);
                SB();
            }
            float mean, rstd; ln_stats(sv, mean, rstd);
            if (h == 0) { stt[w * 64 + tok] = rstd; stt[w * 64 + 32 + tok] = mean * rstd; }
            TS(tb + 1);
            BAR();
            TS(tb + 2);
            const float mr = mean * rstd;
            float q[16], k[16], v[16];
            const float cv = csq[64 + tok];
#pragma unroll
            for (int g4 = 0; g4 < 4; ++g4) {
                f32x4 cq = *(LAS f32x4*)(csq + 8 * g4 + 4 * h), ck = *(LAS f32x4*)(csq + 32 + 8 * g4 + 4 * h);
                f32x4 rsv = *(LAS f32x4*)(stt + w * 64 + 8 * g4 + 4 * h), ms = *(LAS f32x4*)(stt + w * 64 + 32 + 8 * g4 + 4 * h);
#pragma unroll
                for (int i = 0; i < 4; ++i) {
                    k[4 * g4 + i] = ak[4 * g4 + i] * rstd - mr * ck[i];
                    v[4 * g4 + i] = av[4 * g4 + i] * rsv[i] - ms[i] * cv;
                    q[4 * g4 + i] = (aq[4 * g4 + i] * rstd - mr * cq[i]) * QSCALE;
                }
            }
            const u32x4 k0 = {pk(k[0], k[1]), pk(k[2], k[3]), pk(k[4], k[5]), pk(k[6], k[7])};
            const u32x4 k1 = {pk(k[8], k[9]), pk(k[10], k[11]), pk(k[12], k[13]), pk(k[14], k[15])};
            const u32x4 v0 = {pk(v[0], v[1]), pk(v[2], v[3]), pk(v[4], v[5]), pk(v[6], v[7])};
            const u32x4 v1 = {pk(v[8], v[9]), pk(v[10], v[11]), pk(v[12], v[13]), pk(v[14], v[15])};
            const int kvo = KV_OFF + lyr * 4 * MB + (T * 16 + r) * 4096;
            stw(rs, lane * 16, kvo, k0); stw(rs, lane * 16 + 1024, kvo, k1); stw(rs, lane * 16 + 2048, kvo, v0); stw(rs, lane * 16 + 3072, kvo, v1);
            {
                float na = 0.f, nb = 0.f;
#pragma unroll
                for (int i = 0; i < 8; ++i) { na = __builtin_fmaf(k[i], k[i], na); nb = __builtin_fmaf(k[8 + i], k[8 + i], nb); }
                const f32x2 nn = {wave32_max(sum_xor32(na)), wave32_max(sum_xor32(nb))};
                if (lane == 0) { stst(rs, 0, KN_OFF + ((lyr * 64 + T) * 16 + r) * 8, nn); *(LAS f32x2*)(lds + L_KN + (8 + w) * 8) = nn; }
            }
            LAS char* kvl = lds + L_KV + (8 + w) * 4096 + lane * 16;
            *(LAS u32x4*)(kvl) = k0; *(LAS u32x4*)(kvl + 1024) = k1; *(LAS u32x4*)(kvl + 2048) = v0; *(LAS u32x4*)(kvl + 3072) = v1;
            LAS char* ql = lds + L_Q + w * 2048 + lane * 16;
            *(LAS u32x4*)(ql) = u32x4{pk(q[0], q[1]), pk(q[2], q[3]), pk(q[4], q[5]), pk(q[6], q[7])};
            *(LAS u32x4*)(ql + 1024) = u32x4{pk(q[8], q[9]), pk(q[10], q[11]), pk(q[12], q[13]), pk(q[14], q[15])};
            {
                float na = 0.f, nb = 0.f;
#pragma unroll
                for (int i = 0; i < 8; ++i) { na = __builtin_fmaf(q[i], q[i], na); nb = __builtin_fmaf(q[8 + i], q[8 + i], nb); }
                const f32x2 nn = {wave32_max(sum_xor32(na)), wave32_max(sum_xor32(nb))};
                if (lane == 0) *(LAS f32x2*)(lds + L_QN + w * 8) = nn;
            }
            DRAIN();
            BAR();
            if (w == 0 && lane == 0) set_flag(p, 1 + 5 * lyr, g, r, tag);
            TS(tb + 3);
        }
        {
            PHASE_VARS();
            attn_phase(p, lds, rs, w, g, r, lyr, lane, tag TS_ARG(layer == 0 ? 22 : 48));
            TS(tb + 4); TS(tb + 5); TS(tb + 6);
            DRAIN();
            BAR();
            if (w == 0 && lane == 0) set_flag(p, 2 + 5 * lyr, g, r, tag);
            TS(tb + 7);
        }
        {
            PHASE_VARS();
            const int mt = w & 1, kh = (w >> 1) & 1, T = 4 * g + 2 * m + mt, u = 2 * n + kh;
            const int xo = XF_OFF + lyr * 2 * MB + (T * 32 + 2 * u) * 1024;
            const u32x4 r0 = ldw(rs, lane * 16, xo), r1 = ldw(rs, lane * 16 + 1024, xo);
            if (w == 0) wait_group(p, 2 + 5 * lyr, g, tag, lane);
            BAR();
            TS(tb + 8);
            f32x16 acc[2] = {};
            u32x4 xa[16];
            const int oo = O_OFF + lyr * 2 * MB + (T * 32 + 16 * kh) * 1024;
#pragma unroll
            for (int s = 0; s < 16; ++s) { LDA(xa, s, oo); SB(); }
            const LAS char* wb = lds + 96 * KB + kh * 32768 + lane * 16;
            const LAS char* wlast = lds + (kh ? 94 * KB : 96 * KB + 15 * 2048) + lane * 16;
            bf16x8 wf[4][2];
#pragma unroll
            for (int s = 0; s < 3; ++s) { wf[s][0] = ldsfrag(wb + s * 2048); wf[s][1] = ldsfrag(wb + s * 2048 + 1024); }
#pragma unroll
            for (int s = 0; s < 16; ++s) {
                if (s + 3 < 15) { wf[(s + 3) & 3][0] = ldsfrag(wb + (s + 3) * 2048); wf[(s + 3) & 3][1] = ldsfrag(wb + (s + 3) * 2048 + 1024); }
                if (s + 3 == 15) { wf[3][0] = ldsfrag(wlast); wf[3][1] = ldsfrag(wlast + 1024); }
                const bf16x8 x = asfrag(xa[s]);
                acc[0] = mfma32(wf[s & 3][0], x, acc[0]); acc[1] = mfma32(wf[s & 3][1], x, acc[1]);
                SGB(0x100, 1); SGB(0x8, 1); SGB(0x20, 2); SGB(0x100, 1); SGB(0x8, 1);
                SB();
            }
            const f32x16 snd = kh ? acc[0] : acc[1];
#pragma unroll
            for (int g4 = 0; g4 < 4; ++g4) *(LAS f32x4*)(lds + L_RED2 + ((w * 4 + g4) * 64 + lane) * 16) = f32x4{snd[4 * g4], snd[4 * g4 + 1], snd[4 * g4 + 2], snd[4 * g4 + 3]};
            TS(tb + 9);
            BAR();
            const f32x16 own = kh ? acc[1] : acc[0];
            const int pw = w ^ 2;
            float v[16];
#pragma unroll
            for (int g4 = 0; g4 < 4; ++g4) { f32x4 t = *(LAS f32x4*)(lds + L_RED2 + ((pw * 4 + g4) * 64 + lane) * 16);
#pragma unroll
                for (int i = 0; i < 4; ++i) v[4 * g4 + i] = own[4 * g4 + i] + t[i]; }
            const unsigned rr[8] = {r0.x, r0.y, r0.z, r0.w, r1.x, r1.y, r1.z, r1.w};
            unsigned c[8];
#pragma unroll
            for (int i = 0; i < 8; ++i) c[i] = pk(v[2 * i] + bflo(rr[i]), v[2 * i + 1] + bfhi(rr[i]));
            const int yo = X2_OFF + lyr * 2 * MB + (T * 32 + 2 * u) * 1024;
            stw(rs, lane * 16, yo, u32x4{c[0], c[1], c[2], c[3]});
            stw(rs, lane * 16 + 1024, yo, u32x4{c[4], c[5], c[6], c[7]});
            stats_store(rs, 2 * lyr + 1, T, u, lane, c);
            DRAIN();
            BAR();
            if (w == 0 && lane == 0) set_flag(p, 3 + 5 * lyr, g, r, tag);
            TS(tb + 10);
        }
        {
            PHASE_VARS();
            if (w == 0) wait_group(p, 3 + 5 * lyr, g, tag, lane);
            BAR();
            TS(tb + 11);
            const int T = 4 * g + w;
            const LAS char* wbA = lds + lane * 16;
            const LAS char* wbB = lds + 3072 + (h * 16 + (lane & 15)) * 16;
            LAS float* cs1 = (LAS float*)(lds + L_CS1);
            f32x16 acc[4] = {};
            u32x4 xa[32];
            f32x2 sv[8];
            const int sto = ST_OFF + (2 * lyr + 1) * ST_BUF + T * 4096, xo = X2_OFF + lyr * 2 * MB + T * 32768;
#pragma unroll
            for (int s = 0; s < PD0; ++s) { LDA(xa, s, xo); SB(); }
            auto ldW = [&](int s, int t) { return t < 3 ? ldsfrag(wbA + w1_off(s) + t * 1024) : ldsfrag(wbB + w1_off(s)); };
            bf16x8 wf[2][4];
#pragma unroll
            for (int t = 0; t < 4; ++t) wf[0][t] = ldW(0, t);
#pragma unroll
            for (int s = 0; s < 32; ++s) {
                if (s == 20) BAR();
#pragma unroll
                for (int j = (s == 0 ? PD0 : a_issued(s - 1, 32)); j < a_issued(s, 32); ++j) LDA(xa, j, xo);
                if (s >= 16 && s < 24) sv[s - 16] = ldst(rs, (8 * h * 32 + tok) * 8 + (s - 16) * 256, sto);
                const bf16x8 x = asfrag(xa[s]);
#pragma unroll
                for (int t = 0; t < 4; ++t) {
                    if (s + 1 < 32) wf[(s + 1) & 1][t] = ldW(s + 1, t);
                    acc[t] = mfma32(wf[s & 1][t], x, acc[t]);
                }
                SGB(0x100, 1); SGB(0x8, 1); SGB(0x20, 1); SGB(0x100, 1); SGB(0x8, 1); SGB(0x20, 1); SGB(0x100, 1); SGB(0x8, 1); SGB(0x100, 1); SGB(0x8, 1);
                SB();
            }
            float mean, rstd; ln_stats(sv, mean, rstd);
            const float mr = mean * rstd;
            TS(tb + 12);
            BAR();
            TS(tb + 13);
            const int ho = H_OFF + lyr * 8 * MB + (T * 112 + 7 * r) * 1024;
            const f32x2 nm2 = {-mean, -mean}, rs2 = {rstd, rstd}, ry2 = {-LOG2E * rstd, -LOG2E * rstd}, one2 = {1.f, 1.f};
#pragma unroll
            for (int t = 0; t < 4; ++t) {
                constexpr int dummy = 0; (void)dummy;
                const int np = t < 3 ? 8 : 4;
                f32x2 tt[8], z[8], e[8];
#pragma unroll
                for (int g4 = 0; g4 < 4; ++g4) {
                    if (2 * g4 < np) {
                        const f32x4 c4 = *(LAS f32x4*)(cs1 + t * 32 + 8 * g4 + 4 * h);
                        tt[2 * g4] = __builtin_elementwise_fma(nm2, f32x2{c4[0], c4[1]}, f32x2{acc[t][4 * g4], acc[t][4 * g4 + 1]});
                        tt[2 * g4 + 1] = __builtin_elementwise_fma(nm2, f32x2{c4[2], c4[3]}, f32x2{acc[t][4 * g4 + 2], acc[t][4 * g4 + 3]});
                    }
                }
#pragma unroll
                for (int i = 0; i < 8; ++i) if (i < np) e[i] = tt[i] * ry2;
                SB();
#pragma unroll
                for (int i = 0; i < 8; ++i) if (i < np) e[i] = f32x2{__builtin_amdgcn_exp2f(e[i].x), __builtin_amdgcn_exp2f(e[i].y)};
#pragma unroll
                for (int i = 0; i < 8; ++i) if (i < np) z[i] = tt[i] * rs2;
                SB();
#pragma unroll
                for (int i = 0; i < 8; ++i) if (i < np) e[i] = e[i] + one2;
                SB();
#pragma unroll
                for (int i = 0; i < 8; ++i) if (i < np) e[i] = f32x2{__builtin_amdgcn_rcpf(e[i].x), __builtin_amdgcn_rcpf(e[i].y)};
                SB();
#pragma unroll
                for (int i = 0; i < 8; ++i) if (i < np) z[i] = z[i] * e[i];
                stw(rs, lane * 16 + (2 * t) * 1024, ho, u32x4{pk(z[0].x, z[0].y), pk(z[1].x, z[1].y), pk(z[2].x, z[2].y), pk(z[3].x, z[3].y)});
                if (t < 3) stw(rs, lane * 16 + (2 * t + 1) * 1024, ho, u32x4{pk(z[4].x, z[4].y), pk(z[5].x, z[5].y), pk(z[6].x, z[6].y), pk(z[7].x, z[7].y)});
                SB();
            }
            DRAIN();
            BAR();
            if (w == 0 && lane == 0) set_flag(p, 4 + 5 * lyr, g, r, tag);
            TS(tb + 14);
        }
        {
            PHASE_VARS();
            const int mt = w & 1, kh = (w >> 1) & 1, T = 4 * g + 2 * m + mt, u = 2 * n + kh;
            const int yo = X2_OFF + lyr * 2 * MB + (T * 32 + 2 * u) * 1024;
            const u32x4 r0 = ldw(rs, lane * 16, yo), r1 = ldw(rs, lane * 16 + 1024, yo);
            if (w == 0) wait_group(p, 4 + 5 * lyr, g, tag, lane);
            BAR();
            TS(tb + 15);
            const LAS char* wb = lds + kh * 2048 + lane * 16;
            f32x16 acc[2] = {};
            const int ho = H_OFF + lyr * 8 * MB + (T * 112 + 56 * kh) * 1024;
            u32x4 xa[56];
#pragma unroll
            for (int s = 0; s < PD0; ++s) { LDA(xa, s, ho); SB(); }
            auto woff = [](int s) { return w2_slot(s); };
            bf16x8 wf[4][2];
#pragma unroll
            for (int s = 0; s < 3; ++s) { wf[s][0] = ldsfrag(wb + woff(s)); wf[s][1] = ldsfrag(wb + woff(s) + 1024); }
#pragma unroll
            for (int s = 0; s < 56; ++s) {
                if (s == 18) { BAR(); TS(tb + 16); }
                if (s == 35) { BAR(); TS(tb + 17); }
#pragma unroll
                for (int j = (s == 0 ? PD0 : a_issued(s - 1, 56)); j < a_issued(s, 56); ++j) LDA(xa, j, ho);
                if (s + 3 < 56) { wf[(s + 3) & 3][0] = ldsfrag(wb + woff(s + 3)); wf[(s + 3) & 3][1] = ldsfrag(wb + woff(s + 3) + 1024); }
                const bf16x8 x = asfrag(xa[s]);
                acc[0] = mfma32(wf[s & 3][0], x, acc[0]); acc[1] = mfma32(wf[s & 3][1], x, acc[1]);
                SGB(0x100, 1); SGB(0x8, 1); SGB(0x20, 2); SGB(0x100, 1); SGB(0x8, 1);
                SB();
            }
            const f32x16 snd = kh ? acc[0] : acc[1];
#pragma unroll
            for (int g4 = 0; g4 < 4; ++g4) *(LAS f32x4*)(lds + L_RED4 + ((w * 4 + g4) * 64 + lane) * 16) = f32x4{snd[4 * g4], snd[4 * g4 + 1], snd[4 * g4 + 2], snd[4 * g4 + 3]};
            TS(tb + 18);
            BAR();
            const f32x16 own = kh ? acc[1] : acc[0];
            const int pw = w ^ 2;
            float v[16];
#pragma unroll
            for (int g4 = 0; g4 < 4; ++g4) { f32x4 t = *(LAS f32x4*)(lds + L_RED4 + ((pw * 4 + g4) * 64 + lane) * 16);
#pragma unroll
                for (int i = 0; i < 4; ++i) v[4 * g4 + i] = own[4 * g4 + i] + t[i]; }
            const unsigned rr[8] = {r0.x, r0.y, r0.z, r0.w, r1.x, r1.y, r1.z, r1.w};
            unsigned c[8];
#pragma unroll
            for (int i = 0; i < 8; ++i) c[i] = pk(v[2 * i] + bflo(rr[i]), v[2 * i + 1] + bfhi(rr[i]));
            if (lyr == 0) {
                stw(rs, lane * 16, XF_OFF + 2 * MB + (T * 32 + 2 * u) * 1024, u32x4{c[0], c[1], c[2], c[3]});
                stw(rs, lane * 16 + 1024, XF_OFF + 2 * MB + (T * 32 + 2 * u) * 1024, u32x4{c[4], c[5], c[6], c[7]});
                stats_store(rs, 2, T, u, lane, c);
                DRAIN();
                BAR();
                if (w == 0 && lane == 0) set_flag(p, 5, g, r, tag);
            } else {
                auto s0 = __builtin_amdgcn_permlane32_swap(c[0], c[2], false, false);
                auto s1 = __builtin_amdgcn_permlane32_swap(c[1], c[3], false, false);
                auto s2 = __builtin_amdgcn_permlane32_swap(c[4], c[6], false, false);
                auto s3 = __builtin_amdgcn_permlane32_swap(c[5], c[7], false, false);
                uint16_t* orow = p.out + (size_t)(T * 32 + (lane & 31)) * DM + 32 * u + 8 * h;
                *(GAS u32x4*)(orow) = u32x4{s0[0], s1[0], s0[1], s1[1]};
                *(GAS u32x4*)(orow + 16) = u32x4{s2[0], s3[0], s2[1], s3[1]};
            }
            TS(tb + 19);
        }
    }
    asm volatile("s_waitcnt lgkmcnt(0)" ::: "memory");
    if (w == 0) TS_DUMP();
}

template <int PART> DI void pretile(const Params& p, LAS char* lds, unsigned lds0, rsrc_t rs, int hw, int g, int r, int lane) {
    const int h = lane >> 5, a = (lane & 15) >> 2, pp = lane & 3, ch = (lane >> 4) & 1;
    const int n = r >> 1, j = 2 * g + (r & 1);
    const unsigned scr0 = lds0 + L_SCR + hw * 8192;
    const LAS char* scr = lds + L_SCR + hw * 8192;
    if (hw == 0) {
        const char* base = (const char*)(p.wqkv + (size_t)32 * g * 1536 + 32 * r);
        if (PART == 0) {
#pragma unroll
            for (int i = 0; i < 6; ++i) { const int L = 64 * i + lane, k = L / 12, c = L % 12; glds16s<false>(base, (unsigned)(k * 1536 + (c >> 2) * 512 + (c & 3) * 8) * 2, scr0 + i * 1024); }
            return;
        }
        DRAIN();
        const LAS char* wb = scr + (4 * h + a) * 192 + ch * 32 + pp * 8;
#pragma unroll
        for (int s = 0; s < 2; ++s)
#pragma unroll
            for (int t = 0; t < 3; ++t) stw(rs, lane * 16, WQT_OFF + ((r * 32 + 2 * g + s) * 3 + t) * 1024, wfrag_tr(wb + s * 16 * 192 + t * 64, 8 * 192));
    } else if (hw == 1) {
        const char* base = (const char*)(p.w1 + (size_t)32 * g * FF + 112 * r);
        if (PART == 0) {
#pragma unroll
            for (int b = 0; b < 7; ++b) {
                const int L = 64 * b + lane, G = L / 56, x = L - G * 56;
                const int aa = x < 48 ? ((x >> 2) & 3) : ((x - 48) >> 1);
                const int c = x < 48 ? ((x >> 4) * 4 + (x & 3)) : (12 + (x & 1));
                glds16s<false>(base, (unsigned)((4 * G + aa) * FF + c * 8) * 2, scr0 + b * 1024);
            }
            return;
        }
        DRAIN();
        const LAS char* wbA = scr + h * 896 + a * 64 + ch * 32 + pp * 8;
        const LAS char* wbB = scr + h * 896 + 768 + a * 32 + pp * 8;
#pragma unroll
        for (int s = 0; s < 2; ++s) {
            const int dst = W1T_OFF + r * 114688 + (2 * g + s) * 3584;
#pragma unroll
            for (int t = 0; t < 3; ++t) stw(rs, lane * 16, dst + t * 1024, wfrag_tr(wbA + s * 3584 + t * 256, 1792));
            const u32x4 f3 = wfrag_tr(wbB + s * 3584, 1792);
            if (ch == 0) stw(rs, (h * 16 + (lane & 15)) * 16, dst + 3072, f3);
        }
    } else {
        const int rr = lane >> 3, slot = lane & 7, chunk = slot ^ (((rr >> 1) & 1) << 2);
        const unsigned vo = (unsigned)(rr * DM + chunk * 8) * 2;
        const int s0 = hw == 3 ? j : (j < 16 ? 4 * j : 64 + 3 * (j - 16)), ns = hw == 3 ? 1 : (j < 16 ? 4 : 3);
        const char* base = (const char*)((hw == 3 ? p.wo : p.w2) + (size_t)16 * s0 * DM + 64 * n);
        if (PART == 0) {
            for (int i = 0; i < 2 * ns; ++i) glds16s<false>(base + (size_t)i * 8 * DM * 2, vo, scr0 + i * 1024);
            return;
        }
        DRAIN();
        const int sw = ((a >> 1) & 1) << 6;
        const LAS char* wb0 = scr + (4 * h + a) * 128 + (0 ^ sw) + ch * 32 + pp * 8;
        const LAS char* wb1 = scr + (4 * h + a) * 128 + (64 ^ sw) + ch * 32 + pp * 8;
        for (int i = 0; i < ns; ++i) {
            const int s = s0 + i;
            const int kh = s >= 56 ? 1 : 0, sl = s - 56 * kh;
            const int dst = hw == 3 ? WOT_OFF + n * 65536 + s * 2048 : W2T_OFF + (n * 56 + sl) * 4096 + kh * 2048;
            stw(rs, lane * 16, dst, wfrag_tr(wb0 + i * 2048, 1024));
            stw(rs, lane * 16, dst + 1024, wfrag_tr(wb1 + i * 2048, 1024));
        }
    }
    DRAIN();
}

constexpr int w2_regsp(int j) { return j < 10 ? j : j < 30 ? j + 4 : j + 8; }
DI void helper_path(const Params& p, LAS char* lds, unsigned lds0_, rsrc_t rs, const int w, const int g_, const int r_) {
    unsigned tag = 0;
    TS_DECL();
    {   const int layer = 0; PHASE_VARS(); HELPER_VARS();
        tag = read_tag(p); asm volatile("" :: "s"(tag) : "memory");
        if (hw == 3) {
            unsigned keep;
            const unsigned vo = (unsigned)(lane < 44 ? lane : 43) << 20;
            asm volatile("s_mov_b32 %0, m0\n\ts_mov_b32 m0, %3\n\ts_nop 0\n\tglobal_load_lds_dword %1, %2\n\ts_mov_b32 m0, %0" : "=&s"(keep) : "v"(vo), "s"(p.ws), "s"(lds0 + 140 * KB) : "memory");
            const unsigned vo2 = (unsigned)(lane & 1) << 20;
            asm volatile("s_mov_b32 %0, m0\n\ts_mov_b32 m0, %3\n\ts_nop 0\n\tglobal_load_lds_dword %1, %2\n\ts_mov_b32 m0, %0" : "=&s"(keep) : "v"(vo2), "s"(p.out), "s"(lds0 + 141 * KB) : "memory");
        }
        pretile<0>(p, lds, lds0, rs, hw, g, r, lane);
        if (hw == 0) { pretile<1>(p, lds, lds0, rs, hw, g, r, lane); BAR(); }
        else { BAR(); pretile<1>(p, lds, lds0, rs, hw, g, r, lane); }
    }
    for (int layer = 0; layer < 2; ++layer) {
        {
            PHASE_VARS(); HELPER_VARS();
            HTS(layer * 20 + 0);
            BAR();
            if (lyr == 0) {
                if (w == 6 && lane == 0) set_flag(p, 10, g, r, tag);
                stage_lin(wsb, lds0, WQT_OFF + r * 98304, L_WQ, 96, hw, lane);
                float acc = 0.f;
                const LAS char* wbq = lds + L_WQ + (hw < 3 ? hw : 0) * 1024 + lane * 16;
                WAITV(18); BAR();
                acc = colsum_frags<8>(wbq, [](int i) { return (0 + i) * 3072; }, acc);
                WAITV(12); BAR();
                acc = colsum_frags<8>(wbq, [](int i) { return (8 + i) * 3072; }, acc);
                WAITV(6); BAR();
                acc = colsum_frags<8>(wbq, [](int i) { return (16 + i) * 3072; }, acc);
                DRAIN(); BAR();
                acc = colsum_frags<8>(wbq, [](int i) { return (24 + i) * 3072; }, acc);
                acc = sum_xor32(acc);
                if (h == 0 && hw < 3) ((LAS float*)(lds + L_CSQ))[hw * 32 + tok] = acc;
                wait_word(p, flagp(p, 10, lane & 15, (lane & 16) ? (r ^ 1) : r), tag);
            } else {
                __builtin_amdgcn_s_sleep(WODELAY);
                for (int i = hw; i < 62; i += 4) glds16s<true>(wsb + WOT_OFF + n * 65536 + i * 1024, lane * 16, lds0 + 96 * KB + i * 1024);
            }
            BAR();
            if (w == 7 && lane < 2) *(LAS u32x4*)(lds + L_ONES + lane * 1024) = u32x4{0x3f803f80u, 0x3f803f80u, 0x3f803f80u, 0x3f803f80u};
            HTS(layer * 20 + 2);
            stage_lin(wsb, lds0, WOT_OFF + n * 65536 + 62 * KB, 94 * KB, 2, hw, lane);
            stage_lin(wsb, lds0, W1T_OFF + r * 114688, 56 * KB, 35, hw, lane);
            HTS(layer * 20 + 3);
            BAR();
            if (lyr == 0) stage_lin(wsb, lds0, WOT_OFF + n * 65536, 96 * KB, 62, hw, lane);
        }
        u32x4 buf[48];
        {   PHASE_VARS(); HELPER_VARS();

            attn_phase(p, lds, rs, w, g, r, lyr, lane, tag TS_ARG(layer == 0 ? 40 : 44));
            HTS(layer * 20 + 4);
            DRAIN();
            HTS(layer * 20 + 5);
            BAR();
            stage_lin(wsb, lds0, W1T_OFF + r * 114688 + 35 * KB, 0, 39, hw, lane);
            HTS(layer * 20 + 6);
        }
        {
            PHASE_VARS(); HELPER_VARS();
            const int wsrc = W2T_OFF + n * 56 * 4096 + hw * 1024;
            BAR();
#pragma unroll
            for (int j = 0; j < 12; ++j) { buf[j] = ldw(rs, lane * 16, wsrc + w2_regsp(j) * 4096); SB(); }
            BAR();
#pragma unroll
            for (int j = 12; j < 22; ++j) { buf[j] = ldw(rs, lane * 16, wsrc + w2_regsp(j) * 4096); SB(); }
            BAR();
            stage_lin(wsb, lds0, W1T_OFF + r * 114688 + 75264, 94 * KB, 39, hw, lane);
        }
        {
            PHASE_VARS(); HELPER_VARS();
            HTS(layer * 20 + 8);
            WAITV(31);
            BAR();
            const int wsrc = W2T_OFF + n * 56 * 4096 + hw * 1024;
            stage_lin(wsb, lds0, W2T_OFF + (n * 56 + 10) * 4096, 40 * KB, 16, hw, lane);
            stage_lin(wsb, lds0, W2T_OFF + (n * 56 + 34) * 4096, 136 * KB, 16, hw, lane);
#pragma unroll
            for (int j = 22; j < 34; ++j) { buf[j] = ldw(rs, lane * 16, wsrc + w2_regsp(j) * 4096); SB(); }
            WAITV(20);
            HTS(layer * 20 + 10);
            BAR();
#pragma unroll
            for (int j = 34; j < 48; ++j) { buf[j] = ldw(rs, lane * 16, wsrc + w2_regsp(j) * 4096); SB(); }
            if (lyr == 0) {
                LAS float* cs1 = (LAS float*)(lds + L_CS1);
                const LAS char* wb = hw < 3 ? lds + hw * 1024 + lane * 16 : lds + 3072 + (h * 16 + (lane & 15)) * 16;
                float cs_part = colsum_frags<11>(wb, [](int i) { return w1_off(i); }, 0.f);
                cs_part = colsum_frags<11>(wb, [](int i) { return w1_off(11 + i); }, cs_part);
                cs_part = colsum_frags<10>(wb, [](int i) { return w1_off(22 + i); }, cs_part);
                const float s = sum_xor32(cs_part);
                if (h == 0 && (hw < 3 || tok < 16)) cs1[hw * 32 + tok] = s;
            }
            WAITV(14);
            BAR();
            HTS(layer * 20 + 11);
#pragma unroll
            for (int j = 0; j < 30; ++j) *(LAS u32x4*)(lds + w2_slot(w2_regsp(j)) + hw * 1024 + lane * 16) = buf[j];
            if (lyr == 0) {
#pragma unroll
                for (int j = 0; j < 24; ++j) { buf[j] = ldw(rs, lane * 16, WQT_OFF + r * 98304 + (4 * j + hw) * 1024); SB(); }
            }
            HTS(layer * 20 + 12);
            BAR();
        }
        {
            PHASE_VARS(); HELPER_VARS();
            BAR();
            BAR();
#pragma unroll
            for (int j = 30; j < 48; ++j) *(LAS u32x4*)(lds + w2_slot(w2_regsp(j)) + hw * 1024 + lane * 16) = buf[j];
            if (lyr == 1 && blockIdx.x == 0 && w == 7) {
                bool ok = false;
                for (unsigned spins = 0; spins < (1u << 16) && !ok; ++spins) {
                    bool mine = true;
#pragma unroll
                    for (int j = 0; j < 4; ++j) mine &= __hip_atomic_load((gu32*)(p.ws + ACK_OFF) + (lane + 64 * j) * 32, __ATOMIC_RELAXED, __HIP_MEMORY_SCOPE_AGENT) == tag;
                    ok = __all(mine);
                }
                if (lane == 0) __hip_atomic_store((gu32*)(p.ws + EPOCH_OFF), tag, __ATOMIC_RELAXED, __HIP_MEMORY_SCOPE_AGENT);
            }
            HTS(layer * 20 + 15);
            BAR();
            BAR();
            if (lyr == 0) {
#pragma unroll
                for (int j = 0; j < 24; ++j) *(LAS u32x4*)(lds + L_WQ + (4 * j + hw) * 1024 + lane * 16) = buf[j];
                HTS(17);
                BAR();
            }
        }
    }
    asm volatile("s_waitcnt lgkmcnt(0)" ::: "memory");
    if (w == 4) HTS_DUMP();
}

__global__ __launch_bounds__(512, 2) void k_mega(Params p) {
    extern __shared__ __attribute__((aligned(16))) char smem_[];
    LAS char* lds = (LAS char*)smem_;
    const unsigned lds0 = (unsigned)(uintptr_t)smem_;
    const int b = blockIdx.x, idx = b >> 3;
    const int g_ = (b & 3) * 4 + (idx >> 3), r_ = ((b >> 2) & 1) * 8 + (idx & 7);
    const int w = __builtin_amdgcn_readfirstlane(threadIdx.x >> 6);
    const rsrc_t rs = __builtin_amdgcn_make_buffer_rsrc((void*)p.ws, 0, WS_USED, 0x00020000);
    if (w < 4) main_path(p, lds, rs, w, g_, r_);
    else helper_path(p, lds, lds0, rs, w, g_, r_);
}
}

extern "C" void kernel_launch(void* const* d_in, const int* in_sizes, int n_in, void* d_out, int out_size, void* d_ws, size_t ws_size, hipStream_t stream) {
    Params p;
    p.x = (const uint16_t*)d_in[0]; p.wqkv = (const uint16_t*)d_in[1]; p.wo = (const uint16_t*)d_in[2];
    p.w1 = (const uint16_t*)d_in[4]; p.w2 = (const uint16_t*)d_in[5];
    p.out = (uint16_t*)d_out; p.ws = (char*)d_ws;
    (void)hipFuncSetAttribute((const void*)k_mega, hipFuncAttributeMaxDynamicSharedMemorySize, LDS_BYTES);
    k_mega<<<256, 512, LDS_BYTES, stream>>>(p);
}
```

```cpp
#include <hip/hip_runtime.h>
#include <hip/hip_bf16.h>
#include <stdint.h>

#define GAS __attribute__((address_space(1)))
#define LAS __attribute__((address_space(3)))
#define DI __device__ __forceinline__

typedef __bf16 bf16x8 __attribute__((ext_vector_type(8)));
typedef __bf16 bf16x2 __attribute__((ext_vector_type(2)));
typedef short s16x4 __attribute__((ext_vector_type(4)));
typedef float f32x16 __attribute__((ext_vector_type(16)));
typedef float f32x4 __attribute__((ext_vector_type(4)));
typedef float f32x2 __attribute__((ext_vector_type(2)));
typedef unsigned u32x4 __attribute__((ext_vector_type(4)));
typedef unsigned u32x2 __attribute__((ext_vector_type(2)));
typedef GAS unsigned gu32;

#define TS_DECL()
#define TS_PARAM
#define TS_ARG(tb)
#define TS(i)
#define TS_DUMP()
#define HTS(i)
#define HTS_DUMP()
#define MAPMODE 3
namespace {
constexpr int DM = 512, FF = 1792;
constexpr float EPS = 1e-5f;
constexpr float LOG2E = 1.4426950408889634f;
constexpr float QSCALE = 0.25f * LOG2E;

constexpr int MB = 1 << 20, KB = 1024;
constexpr int XF_OFF = 0;
constexpr int O_OFF = 4 * MB;
constexpr int X2_OFF = 8 * MB;
constexpr int H_OFF = 12 * MB;
constexpr int KV_OFF = 28 * MB;
constexpr int WQT_OFF = 36 * MB;
constexpr int WOT_OFF = WQT_OFF + 1536 * KB;
constexpr int W1T_OFF = 38 * MB;
constexpr int W2T_OFF = 40 * MB;
constexpr int ST_OFF = 42 * MB;
constexpr int ST_BUF = 262144;
constexpr int KN_OFF = 43 * MB;
constexpr int FLAG_OFF = 43 * MB + 65536;
constexpr int FLAG_BYTES = 11 * 256 * 128;
constexpr int TMO_OFF = FLAG_OFF + FLAG_BYTES;
constexpr int EPOCH_OFF = TMO_OFF + 256;
constexpr int ACK_OFF = EPOCH_OFF + 256;
constexpr int WS_USED = 44 * MB;

constexpr int L_WQ = 0;
constexpr int L_Q = 0, L_KV = 8 * KB, L_ONES = 91 * KB;
constexpr int L_RED2 = 40 * KB;
constexpr int wo_off(int s) { return s < 31 ? 96 * KB + s * 2048 : 94 * KB; }
constexpr int L_RED4 = 96 * KB;
constexpr int L_SCR = 100 * KB;
constexpr int L_TAB = 158 * KB, L_CSQ = L_TAB, L_CS1 = L_TAB + 512, L_STT = L_TAB + 1024;
constexpr int L_KN = L_TAB + 384, L_QN = L_TAB + 384 + 96;
constexpr int LDS_BYTES = 160 * KB;
constexpr int w1_off(int s) { return s < 10 ? 56 * KB + s * 3584 : s < 21 ? (s - 10) * 3584 : 94 * KB + (s - 21) * 3584; }
constexpr int w2_slot(int sp) { return (sp < 38 ? sp : sp - 38) * 4096; }
constexpr int AUX_SC1 = 16;

struct Params {
    const uint16_t* x; const uint16_t* wqkv; const uint16_t* wo; const uint16_t* w1; const uint16_t* w2;
    uint16_t* out; char* ws;
};

DI int lane_id() { return (int)__builtin_amdgcn_mbcnt_hi(~0u, __builtin_amdgcn_mbcnt_lo(~0u, 0u)); }
DI f32x16 mfma32(bf16x8 a, bf16x8 b, f32x16 c) { return __builtin_amdgcn_mfma_f32_32x32x16_bf16(a, b, c, 0, 0, 0); }
DI unsigned pk(float a, float b) { bf16x2 v = {(__bf16)a, (__bf16)b}; return __builtin_bit_cast(unsigned, v); }
DI float bflo(unsigned u) { return __uint_as_float(u << 16); }
DI float bfhi(unsigned u) { return __uint_as_float(u & 0xffff0000u); }
DI bf16x8 asfrag(u32x4 v) { return __builtin_bit_cast(bf16x8, v); }
DI s16x4 trd(const LAS char* p) { return __builtin_amdgcn_ds_read_tr16_b64_v4i16((LAS s16x4*)p); }
DI u32x4 wfrag_tr(const LAS char* p, int off2) {
    const u32x2 lo = __builtin_bit_cast(u32x2, trd(p)), hi = __builtin_bit_cast(u32x2, trd(p + off2));
    return u32x4{lo.x, lo.y, hi.x, hi.y};
}
DI bf16x8 ldsfrag(const LAS char* p) { return *(const LAS bf16x8*)p; }
template <int N, class F> DI float colsum_frags(const LAS char* p, F off, float acc) {
    u32x4 f[N];
#pragma unroll
    for (int i = 0; i < N; ++i) f[i] = *(const LAS u32x4*)(p + off(i));
    float a1 = 0.f;
#pragma unroll
    for (int i = 0; i < N; ++i) {
        unsigned w0 = f[i].x, w1 = f[i].y, w2 = f[i].z, w3 = f[i].w;
        asm("v_dot2c_f32_bf16 %0, %2, %6\n\tv_dot2c_f32_bf16 %1, %3, %6\n\tv_dot2c_f32_bf16 %0, %4, %6\n\tv_dot2c_f32_bf16 %1, %5, %6"
            : "+v"(acc), "+v"(a1) : "v"(w0), "v"(w1), "v"(w2), "v"(w3), "v"(0x3f803f80u));
    }
    asm volatile("s_nop 2" : "+v"(acc), "+v"(a1));
    return acc + a1;
}
DI float sum_xor32(float v) { auto r = __builtin_amdgcn_permlane32_swap(__float_as_uint(v), __float_as_uint(v), false, false); return __uint_as_float(r[0]) + __uint_as_float(r[1]); }
DI float max_xor32(float v) { auto r = __builtin_amdgcn_permlane32_swap(__float_as_uint(v), __float_as_uint(v), false, false); return fmaxf(__uint_as_float(r[0]), __uint_as_float(r[1])); }
template <bool SC1> DI void glds16s(const void* sbase, unsigned voff, unsigned lds_addr) {
    unsigned keep;
    if (SC1) asm volatile("s_mov_b32 %0, m0\n\ts_mov_b32 m0, %3\n\ts_nop 0\n\tglobal_load_lds_dwordx4 %1, %2 sc1\n\ts_mov_b32 m0, %0" : "=&s"(keep) : "v"(voff), "s"(sbase), "s"(lds_addr) : "memory");
    else asm volatile("s_mov_b32 %0, m0\n\ts_mov_b32 m0, %3\n\ts_nop 0\n\tglobal_load_lds_dwordx4 %1, %2\n\ts_mov_b32 m0, %0" : "=&s"(keep) : "v"(voff), "s"(sbase), "s"(lds_addr) : "memory");
}
#define BAR() asm volatile("s_waitcnt lgkmcnt(0)\n\ts_barrier" ::: "memory")
#define DRAIN() asm volatile("s_waitcnt vmcnt(0)" ::: "memory")
#define WAITV(n) asm volatile("s_waitcnt vmcnt(" #n ")" ::: "memory")
#define SB() __builtin_amdgcn_sched_barrier(0)
#define SGB(mask, n) __builtin_amdgcn_sched_group_barrier(mask, n, 0)

typedef __amdgpu_buffer_rsrc_t rsrc_t;
DI u32x4 ldw(rsrc_t rs, int voff, int soff) { return __builtin_amdgcn_raw_buffer_load_b128(rs, voff, soff, AUX_SC1); }
DI void stw(rsrc_t rs, int voff, int soff, u32x4 v) { __builtin_amdgcn_raw_buffer_store_b128(v, rs, voff + soff, 0, AUX_SC1); }
DI f32x2 ldst(rsrc_t rs, int voff, int soff) { return __builtin_bit_cast(f32x2, __builtin_amdgcn_raw_buffer_load_b64(rs, voff, soff, AUX_SC1)); }
DI void stst(rsrc_t rs, int voff, int soff, f32x2 v) { __builtin_amdgcn_raw_buffer_store_b64(__builtin_bit_cast(u32x2, v), rs, voff, soff, AUX_SC1); }

#define FSTRIDE 32
DI gu32* flagp(const Params& p, int ph, int g, int r) { return (gu32*)(p.ws + FLAG_OFF) + (ph * 16 + g) * 16 * 32 + r * FSTRIDE; }
DI void set_flag(const Params& p, int ph, int g, int r, unsigned tag) { __hip_atomic_store(flagp(p, ph, g, r), tag, __ATOMIC_RELAXED, __HIP_MEMORY_SCOPE_AGENT); }
DI void wait_word(const Params& p, gu32* a, unsigned tag) {
    for (unsigned spins = 0;; ++spins) {
        const unsigned v = __hip_atomic_load(a, __ATOMIC_RELAXED, __HIP_MEMORY_SCOPE_AGENT);
        if (__all(v == tag)) break;
        if (spins > (1u << 20)) { __hip_atomic_store((gu32*)(p.ws + TMO_OFF), 1u, __ATOMIC_RELAXED, __HIP_MEMORY_SCOPE_AGENT); break; }
        __builtin_amdgcn_s_sleep(1);
    }
    __builtin_amdgcn_fence(__ATOMIC_ACQUIRE, "wavefront");
}
DI void wait_group(const Params& p, int ph, int g, unsigned tag, int lane) { wait_word(p, flagp(p, ph, g, lane & 15), tag); }

DI void stats_store(rsrc_t rs, int buf, int T, int u, int lane, const unsigned (&c)[8]) {
    float s = 0.f, s1 = 0.f, ss = 0.f, ss1 = 0.f;
#pragma unroll
    for (int i = 0; i < 8; i += 2) {
        unsigned w0 = c[i], w1 = c[i + 1];
        asm("v_dot2c_f32_bf16 %0, %4, %6\n\tv_dot2c_f32_bf16 %1, %5, %6\n\tv_dot2c_f32_bf16 %2, %4, %4\n\tv_dot2c_f32_bf16 %3, %5, %5"
            : "+v"(s), "+v"(s1), "+v"(ss), "+v"(ss1) : "v"(w0), "v"(w1), "v"(0x3f803f80u));
    }
    asm volatile("s_nop 2" : "+v"(s), "+v"(s1), "+v"(ss), "+v"(ss1));
    s = sum_xor32(s + s1); ss = sum_xor32(ss + ss1);
    if (lane < 32) stst(rs, lane * 8, ST_OFF + buf * ST_BUF + (T * 16 + u) * 256, f32x2{s, ss});
}

DI void stage_lin(const char* wsb, unsigned lds0, int src, int dst, int n, int hw, int lane, int nw = 4) {
    const unsigned vo = lane * 16;
    for (int i = hw; i < n; i += nw) glds16s<true>(wsb + src + i * 1024, vo, lds0 + dst + i * 1024);
}

#define HELPER_VARS() \
    int hw = w - 4; asm volatile("" : "+s"(hw)); \
    const char* wsb = p.ws; asm volatile("" : "+s"(wsb)); \
    unsigned lds0 = lds0_; asm volatile("" : "+s"(lds0)); (void)hw; (void)wsb; (void)lds0;
#define PHASE_VARS() \
    int lane = lane_id(); asm volatile("" : "+v"(lane)); \
    int lyr = layer; asm volatile("" : "+s"(lyr)); \
    int g = g_, r = r_; asm volatile("" : "+s"(g), "+s"(r)); \
    const int h = lane >> 5, tok = lane & 31; const int m = r & 1, n = r >> 1; (void)h; (void)tok; (void)m; (void)n; (void)lyr;

DI void attn_finish(rsrc_t rs, const f32x16& o, int hd, int T, int r, int lyr, int lane) {
    float ov[8]; float l;
    if (hd == 0) { l = o[8];
#pragma unroll
        for (int i = 0; i < 8; ++i) ov[i] = o[i];
    } else { l = o[0];
#pragma unroll
        for (int i = 0; i < 8; ++i) ov[i] = o[8 + i];
    }
    const float inv = __builtin_amdgcn_rcpf(l);
    stw(rs, lane * 16, O_OFF + lyr * 2 * MB + (T * 32 + 2 * r + hd) * 1024,
        u32x4{pk(ov[0] * inv, ov[1] * inv), pk(ov[2] * inv, ov[3] * inv), pk(ov[4] * inv, ov[5] * inv), pk(ov[6] * inv, ov[7] * inv)});
}
DI float max16(float m, const f32x16& s) {
#pragma unroll
    for (int i = 0; i < 16; i += 2) m = fmaxf(fmaxf(m, s[i]), s[i + 1]);
    return m;
}
DI void max16x2(float& m0, float& m1, const f32x16& s) {
#pragma unroll
    for (int i = 0; i < 16; i += 4) { m0 = fmaxf(fmaxf(m0, s[i]), s[i + 1]); m1 = fmaxf(fmaxf(m1, s[i + 2]), s[i + 3]); }
}
DI void exp_pack(const f32x16& s, u32x4& p0, u32x4& p1) {
    unsigned pw[8];
#pragma unroll
    for (int i = 0; i < 8; ++i) pw[i] = pk(__builtin_amdgcn_exp2f(s[2 * i]), __builtin_amdgcn_exp2f(s[2 * i + 1]));
    p0 = u32x4{pw[0], pw[1], pw[2], pw[3]}; p1 = u32x4{pw[4], pw[5], pw[6], pw[7]};
}
DI float wave32_max(float v) {
    int x = __float_as_int(v);
    x = max(x, __builtin_amdgcn_update_dpp(x, x, 0x111, 0xf, 0xf, false));
    x = max(x, __builtin_amdgcn_update_dpp(x, x, 0x112, 0xf, 0xf, false));
    x = max(x, __builtin_amdgcn_update_dpp(x, x, 0x114, 0xf, 0xf, false));
    x = max(x, __builtin_amdgcn_update_dpp(x, x, 0x118, 0xf, 0xf, false));
    x = max(x, __builtin_amdgcn_update_dpp(x, x, 0x142, 0xa, 0xf, false));
    return __int_as_float(__builtin_amdgcn_readlane(x, 31));
}
DI void attn_seg(unsigned ka, unsigned va, int ks, int vs, int n, const bf16x8 qf, const f32x16 c0, f32x16& o) {
    int d;
    asm volatile(
        "ds_read_b128 v[248:251], %[ka]\n\tv_add_u32 %[ka], %[ks], %[ka]\n\tds_read_b128 v[236:239], %[ka]\n\tv_add_u32 %[ka], %[ks], %[ka]\n\t"
        "ds_read_b128 v[240:243], %[va]\n\tds_read_b128 v[244:247], %[va] offset:1024\n\tv_add_u32 %[va], %[vs], %[va]\n\t"
        "s_lshr_b32 %[d], %[n], 1\n\ts_waitcnt lgkmcnt(3)\n\t"
        "v_mfma_f32_32x32x16_bf16 v[204:219], v[248:251], %[q], %[c0]\n\t"
        "s_cmp_eq_u32 %[d], 0\n\ts_nop 10\n\ts_cbranch_scc1 Ltail_%=\n"
        "Lloop_%=:\n\t"
        "v_exp_f32 v204, v204\n\t"
        "v_exp_f32 v205, v205\n\t"
        "v_exp_f32 v206, v206\n\t"
        "v_exp_f32 v207, v207\n\t"
        "v_exp_f32 v208, v208\n\t"
        "v_exp_f32 v209, v209\n\t"
        "v_exp_f32 v210, v210\n\t"
        "v_exp_f32 v211, v211\n\t"
        "v_cvt_pk_bf16_f32 v204, v204, v205\n\t"
        "v_cvt_pk_bf16_f32 v205, v206, v207\n\t"
        "v_cvt_pk_bf16_f32 v206, v208, v209\n\t"
        "v_cvt_pk_bf16_f32 v207, v210, v211\n\t"
        "s_waitcnt lgkmcnt(1)\n\ts_nop 0\n\t"
        "v_mfma_f32_32x32x16_bf16 %[o], v[240:243], v[204:207], %[o]\n\t"
        "v_exp_f32 v212, v212\n\t"
        "v_exp_f32 v213, v213\n\t"
        "v_exp_f32 v214, v214\n\t"
        "v_exp_f32 v215, v215\n\t"
        "v_mfma_f32_32x32x16_bf16 v[220:235], v[236:239], %[q], 0\n\t"
        "ds_read_b128 v[236:239], %[ka]\n\tds_read_b128 v[248:251], %[va]\n\tds_read_b128 v[252:255], %[va] offset:1024\n\t"
        "v_add_u32 %[ka], %[ks], %[ka]\n\tv_add_u32 %[va], %[vs], %[va]\n\t"
        "v_exp_f32 v216, v216\n\t"
        "v_exp_f32 v217, v217\n\t"
        "v_exp_f32 v218, v218\n\t"
        "v_exp_f32 v219, v219\n\t"
        "v_cvt_pk_bf16_f32 v212, v212, v213\n\t"
        "v_cvt_pk_bf16_f32 v213, v214, v215\n\t"
        "v_cvt_pk_bf16_f32 v214, v216, v217\n\t"
        "v_cvt_pk_bf16_f32 v215, v218, v219\n\t"
        "s_waitcnt lgkmcnt(3)\n\ts_nop 0\n\t"
        "v_mfma_f32_32x32x16_bf16 %[o], v[244:247], v[212:215], %[o]\n\t"
        "v_exp_f32 v220, v220\n\t"
        "v_exp_f32 v221, v221\n\t"
        "v_exp_f32 v222, v222\n\t"
        "v_exp_f32 v223, v223\n\t"
        "v_exp_f32 v224, v224\n\t"
        "v_exp_f32 v225, v225\n\t"
        "v_exp_f32 v226, v226\n\t"
        "v_exp_f32 v227, v227\n\t"
        "v_cvt_pk_bf16_f32 v220, v220, v221\n\t"
        "v_cvt_pk_bf16_f32 v221, v222, v223\n\t"
        "v_cvt_pk_bf16_f32 v222, v224, v225\n\t"
        "v_cvt_pk_bf16_f32 v223, v226, v227\n\t"
        "s_waitcnt lgkmcnt(1)\n\ts_nop 0\n\t"
        "v_mfma_f32_32x32x16_bf16 %[o], v[248:251], v[220:223], %[o]\n\t"
        "v_exp_f32 v228, v228\n\t"
        "v_exp_f32 v229, v229\n\t"
        "v_exp_f32 v230, v230\n\t"
        "v_exp_f32 v231, v231\n\t"
        "v_mfma_f32_32x32x16_bf16 v[204:219], v[236:239], %[q], 0\n\t"
        "ds_read_b128 v[236:239], %[ka]\n\tds_read_b128 v[240:243], %[va]\n\tds_read_b128 v[244:247], %[va] offset:1024\n\t"
        "v_add_u32 %[ka], %[ks], %[ka]\n\tv_add_u32 %[va], %[vs], %[va]\n\t"
        "v_exp_f32 v232, v232\n\t"
        "v_exp_f32 v233, v233\n\t"
        "v_exp_f32 v234, v234\n\t"
        "v_exp_f32 v235, v235\n\t"
        "v_cvt_pk_bf16_f32 v228, v228, v229\n\t"
        "v_cvt_pk_bf16_f32 v229, v230, v231\n\t"
        "v_cvt_pk_bf16_f32 v230, v232, v233\n\t"
        "v_cvt_pk_bf16_f32 v231, v234, v235\n\t"
        "s_waitcnt lgkmcnt(3)\n\ts_nop 0\n\t"
        "v_mfma_f32_32x32x16_bf16 %[o], v[252:255], v[228:231], %[o]\n\t"
        "s_sub_u32 %[d], %[d], 1\n\ts_cmp_lg_u32 %[d], 0\n\ts_cbranch_scc1 Lloop_%=\n"
        "Ltail_%=:\n\t"
        "s_bitcmp1_b32 %[n], 0\n\ts_waitcnt lgkmcnt(0)\n\ts_cbranch_scc0 Lend_%=\n\t"
        "v_exp_f32 v204, v204\n\t"
        "v_exp_f32 v205, v205\n\t"
        "v_exp_f32 v206, v206\n\t"
        "v_exp_f32 v207, v207\n\t"
        "v_exp_f32 v208, v208\n\t"
        "v_exp_f32 v209, v209\n\t"
        "v_exp_f32 v210, v210\n\t"
        "v_exp_f32 v211, v211\n\t"
        "v_cvt_pk_bf16_f32 v204, v204, v205\n\t"
        "v_cvt_pk_bf16_f32 v205, v206, v207\n\t"
        "v_cvt_pk_bf16_f32 v206, v208, v209\n\t"
        "v_cvt_pk_bf16_f32 v207, v210, v211\n\t"
        "s_nop 1\n\t"
        "v_mfma_f32_32x32x16_bf16 %[o], v[240:243], v[204:207], %[o]\n\t"
        "v_exp_f32 v212, v212\n\t"
        "v_exp_f32 v213, v213\n\t"
        "v_exp_f32 v214, v214\n\t"
        "v_exp_f32 v215, v215\n\t"
        "v_exp_f32 v216, v216\n\t"
        "v_exp_f32 v217, v217\n\t"
        "v_exp_f32 v218, v218\n\t"
        "v_exp_f32 v219, v219\n\t"
        "v_cvt_pk_bf16_f32 v212, v212, v213\n\t"
        "v_cvt_pk_bf16_f32 v213, v214, v215\n\t"
        "v_cvt_pk_bf16_f32 v214, v216, v217\n\t"
        "v_cvt_pk_bf16_f32 v215, v218, v219\n\t"
        "s_nop 1\n\t"
        "v_mfma_f32_32x32x16_bf16 %[o], v[244:247], v[212:215], %[o]\n"
        "Lend_%=:\n\ts_nop 11"
        : [o] "+v"(o), [ka] "+v"(ka), [va] "+v"(va), [d] "=&s"(d)
        : [q] "v"(qf), [c0] "v"(c0), [ks] "s"(ks), [vs] "v"(vs), [n] "s"(n)
        : "memory", "scc", "v204", "v205", "v206", "v207", "v208", "v209", "v210", "v211", "v212", "v213", "v214", "v215", "v216", "v217", "v218", "v219", "v220", "v221", "v222", "v223", "v224", "v225", "v226", "v227", "v228", "v229", "v230", "v231", "v232", "v233", "v234", "v235", "v236", "v237", "v238", "v239", "v240", "v241", "v242", "v243", "v244", "v245", "v246", "v247", "v248", "v249", "v250", "v251", "v252", "v253", "v254", "v255");
}
constexpr float BOUND2 = 4096.f;
DI f32x16 attn_exact(LAS char* lds, int tw, int hd, int s0, bool first_masked, const bf16x8 qf, int lane) {
    const int h = lane >> 5, tok = lane & 31;
    const LAS char* kb = lds + L_KV + hd * 1024 + lane * 16;
    const bool isdata = ((lane >> 4) & 1) == hd;
    const LAS char* vb = isdata ? (lds + L_KV + 2048 + lane * 16) : (lds + L_ONES);
    const int vstep = isdata ? 4096 : 0;
    const f32x16 zero = {};
    f32x16 o = {};
    float mx = -INFINITY;
    for (int pass = 0; pass < 2; ++pass) {
        for (int sl = s0; sl <= 8 + tw; ++sl) {
            f32x16 s = mfma32(*(const LAS bf16x8*)(kb + sl * 4096), qf, zero);
            const bool diag = sl == 8 + tw, first = first_masked && sl == s0;
#pragma unroll
            for (int i = 0; i < 16; ++i) { const int kk = (i & 3) + 8 * (i >> 2) + 4 * h; if ((diag && kk > tok) || (first && kk <= tok)) s[i] = -INFINITY; }
            if (pass == 0) mx = max16(mx, s);
            else {
#pragma unroll
                for (int i = 0; i < 16; ++i) s[i] -= mx;
                u32x4 p0, p1; exp_pack(s, p0, p1);
                o = mfma32(*(const LAS bf16x8*)(vb + sl * vstep), asfrag(p0), o);
                o = mfma32(*(const LAS bf16x8*)(vb + sl * vstep + 1024), asfrag(p1), o);
            }
        }
        if (pass == 0) mx = max_xor32(mx);
    }
    return o;
}
DI void attn_phase(const Params& p, LAS char* lds, rsrc_t rs, int w, int g, int r, int lyr, int lane, unsigned tag TS_PARAM) {
    const int h = lane >> 5, tok = lane & 31;
    const int hd = w >> 2, tw = hd ? 3 - (w & 3) : (w & 3), T = 4 * g + tw;
    const bf16x8 qf = *(const LAS bf16x8*)(lds + L_Q + tw * 2048 + hd * 1024 + lane * 16);
    const unsigned kb = (unsigned)(uintptr_t)(lds + L_KV + hd * 1024 + lane * 16);
    const bool isdata = ((lane >> 4) & 1) == hd;
    const unsigned vb = (unsigned)(uintptr_t)(isdata ? (lds + L_KV + 2048 + lane * 16) : (lds + L_ONES));
    const int vstep = isdata ? 4096 : 0;
    const LAS float* kn = (const LAS float*)(lds + L_KN);
    const float qn = ((const LAS float*)(lds + L_QN))[tw * 2 + hd] * 1.05f;
    const float kown = fmaxf(fmaxf(kn[16 + hd], kn[18 + hd]), fmaxf(kn[20 + hd], kn[22 + hd]));
    bool fast = __builtin_amdgcn_readfirstlane(qn * kown <= BOUND2);
    f32x16 t, o = {};
#pragma unroll
    for (int i = 0; i < 16; ++i) t[i] = __builtin_fmaf((float)(tok - 4 * h), 1e30f, (0.5f - (float)((i & 3) + 8 * (i >> 2))) * 1e30f);
    const int s4 = 4 * hd, tile0 = 4 * g - 8 + s4;
    const bool loader = tw == 0 && tile0 >= 0;
    u32x4 hv[16]; f32x2 hn;
    if (loader) {
        wait_word(p, flagp(p, 1 + 5 * lyr, g - 2 + hd, r), tag);
        TS(tb + 1);
        const int kvo = KV_OFF + lyr * 4 * MB + (tile0 * 16 + r) * 4096;
#pragma unroll
        for (int i = 0; i < 16; ++i) hv[i] = ldw(rs, lane * 16 + (i & 3) * 1024, kvo + (i >> 2) * 65536);
        hn = ldst(rs, (lane & 3) * 128, KN_OFF + ((lyr * 64 + tile0) * 16 + r) * 8);
        SB();
    }
    if (fast) {
        f32x16 m;
#pragma unroll
        for (int i = 0; i < 16; ++i) m[i] = fminf(t[i], 0.f);
        attn_seg(kb + (8 + tw) * 4096, vb + (8 + tw) * vstep, -4096, -vstep, tw + 1, qf, m, o);
    }
    TS(tb);
    if (loader) {
        LAS char* kvl = lds + L_KV + s4 * 4096 + lane * 16;
#pragma unroll
        for (int i = 0; i < 16; ++i) *(LAS u32x4*)(kvl + i * 1024) = hv[i];
        if (lane < 4) *(LAS f32x2*)(lds + L_KN + (s4 + lane) * 8) = hn;
    }
    BAR();
    TS(tb + 2);
    const int s0 = T >= 8 ? tw : 8 - 4 * g;
    if (s0 < 8) {
        float kh = 0.f;
#pragma unroll
        for (int j = 0; j < 8; ++j) kh = fmaxf(kh, j >= 8 - 4 * g ? kn[2 * j + hd] : 0.f);
        fast = fast && __builtin_amdgcn_readfirstlane(qn * kh <= BOUND2);
        if (fast) {
            f32x16 m;
#pragma unroll
            for (int i = 0; i < 16; ++i) m[i] = T >= 8 ? fminf(-t[i], 0.f) : 0.f;
            attn_seg(kb + s0 * 4096, vb + s0 * vstep, 4096, vstep, 8 - s0, qf, m, o);
        }
    }
    if (!fast) o = attn_exact(lds, tw, hd, s0, T >= 8, qf, lane);
    attn_finish(rs, o, hd, T, r, lyr, lane);
}
DI void ln_stats(const f32x2 (&sv)[8], float& mean, float& rstd) {
    float S = 0.f, SS = 0.f;
#pragma unroll
    for (int i = 0; i < 8; ++i) { S += sv[i].x; SS += sv[i].y; }
    S = sum_xor32(S); SS = sum_xor32(SS);
    mean = S * (1.f / DM); const float var = fmaxf(SS * (1.f / DM) - mean * mean, 0.f); rstd = __builtin_amdgcn_rsqf(var + EPS);
}
DI unsigned read_tag(const Params& p) {
    return (unsigned)__builtin_amdgcn_readfirstlane(__hip_atomic_load((gu32*)(p.ws + EPOCH_OFF), __ATOMIC_RELAXED, __HIP_MEMORY_SCOPE_AGENT)) + 1u;
}
#define LDA(arr, s, base) arr[s] = ldw(rs, lane * 16 + ((s) & 3) * 1024, (base) + ((s) >> 2) * 4096)

#define WODELAY 36
#define PD0 4
#define PDV 12
constexpr int a_issued(int s, int total) { int v = PD0 + 2 * (s + 1); const int cap = s + 1 + PDV; v = v < cap ? v : cap; return v < total ? v : total; }

DI void main_path(const Params& p, LAS char* lds, rsrc_t rs, const int w, const int g_, const int r_) {
    unsigned tag = 0;
    TS_DECL();
    TS(0);
    {
        const int layer = 0; PHASE_VARS();
        const int T = 4 * g + (r & 3), u = (r >> 2) * 4 + w;
        const uint16_t* row = p.x + (size_t)(T * 32 + tok) * DM + 32 * u + 4 * h;
        u32x2 a0 = *(const GAS u32x2*)(row), a1 = *(const GAS u32x2*)(row + 8), a2 = *(const GAS u32x2*)(row + 16), a3 = *(const GAS u32x2*)(row + 24);
        tag = read_tag(p); asm volatile("" :: "s"(tag) : "memory");
        stw(rs, lane * 16, XF_OFF + (T * 32 + 2 * u) * 1024, u32x4{a0.x, a0.y, a1.x, a1.y});
        stw(rs, lane * 16, XF_OFF + (T * 32 + 2 * u + 1) * 1024, u32x4{a2.x, a2.y, a3.x, a3.y});
        const unsigned c[8] = {a0.x, a0.y, a1.x, a1.y, a2.x, a2.y, a3.x, a3.y};
        stats_store(rs, 0, T, u, lane, c);
        DRAIN();
        BAR();
        if (w == 0 && lane == 0) { set_flag(p, 0, g, r, tag); __hip_atomic_store((gu32*)(p.ws + ACK_OFF) + blockIdx.x * 32, tag, __ATOMIC_RELAXED, __HIP_MEMORY_SCOPE_AGENT); }
    }
    TS(1);
    for (int layer = 0; layer < 2; ++layer) {
        const int tb = 2 + layer * 24; (void)tb;
        {
            PHASE_VARS();
            if (w == 0) {
                if (lyr == 0) {
                    const int j = lane & 15;
                    wait_word(p, (lane & 16) ? flagp(p, 0, j, r) : flagp(p, 0, g, j), tag);
                } else wait_group(p, 5, g, tag, lane);
            }
            BAR();
            TS(tb + 0);
            const int T = 4 * g + w;
            const LAS char* wb = lds + L_WQ + lane * 16;
            LAS float* csq = (LAS float*)(lds + L_CSQ);
            LAS float* stt = (LAS float*)(lds + L_STT);
            f32x16 aq = {}, ak = {}, av = {};
            u32x4 xa[32];
            f32x2 sv[8];
            const int sto = ST_OFF + 2 * lyr * ST_BUF + T * 4096, xo = XF_OFF + lyr * 2 * MB + T * 32768;
#pragma unroll
            for (int s = 0; s < PD0; ++s) { LDA(xa, s, xo); SB(); }
            if (lyr == 0) BAR();
            bf16x8 wf[3][3];
#pragma unroll
            for (int s = 0; s < 2; ++s)
#pragma unroll
                for (int t = 0; t < 3; ++t) wf[s][t] = ldsfrag(wb + (s * 3 + t) * 1024);
#pragma unroll
            for (int s = 0; s < 32; ++s) {
                if ((s == 6 || s == 14 || s == 22) && lyr == 0) BAR();
#pragma unroll
                for (int j = (s == 0 ? PD0 : a_issued(s - 1, 32)); j < a_issued(s, 32); ++j) LDA(xa, j, xo);
                if (s >= 16 && s < 24) sv[s - 16] = ldst(rs, (8 * h * 32 + tok) * 8 + (s - 16) * 256, sto);
                if (s + 2 < 32) {
#pragma unroll
                    for (int t = 0; t < 3; ++t) wf[(s + 2) % 3][t] = ldsfrag(wb + ((s + 2) * 3 + t) * 1024);
                }
                const bf16x8 x = asfrag(xa[s]);
                aq = mfma32(wf[s % 3][0], x, aq); ak = mfma32(wf[s % 3][1], x, ak); av = mfma32(x, wf[s % 3][2], av);
                SGB(0x100, 1); SGB(0x8, 1); SGB(0x20, 1); SGB(0x100, 1); SGB(0x8, 1); SGB(0x20, 1); SGB(0x100, 1); SGB(0x8, 1);
                SB();
            }
            float mean, rstd; ln_stats(sv, mean, rstd);
            if (h == 0) { stt[w * 64 + tok] = rstd; stt[w * 64 + 32 + tok] = mean * rstd; }
            TS(tb + 1);
            BAR();
            TS(tb + 2);
            const float mr = mean * rstd;
            float q[16], k[16], v[16];
            const float cv = csq[64 + tok];
#pragma unroll
            for (int g4 = 0; g4 < 4; ++g4) {
                f32x4 cq = *(LAS f32x4*)(csq + 8 * g4 + 4 * h), ck = *(LAS f32x4*)(csq + 32 + 8 * g4 + 4 * h);
                f32x4 rsv = *(LAS f32x4*)(stt + w * 64 + 8 * g4 + 4 * h), ms = *(LAS f32x4*)(stt + w * 64 + 32 + 8 * g4 + 4 * h);
#pragma unroll
                for (int i = 0; i < 4; ++i) {
                    k[4 * g4 + i] = ak[4 * g4 + i] * rstd - mr * ck[i];
                    v[4 * g4 + i] = av[4 * g4 + i] * rsv[i] - ms[i] * cv;
                    q[4 * g4 + i] = (aq[4 * g4 + i] * rstd - mr * cq[i]) * QSCALE;
                }
            }
            const u32x4 k0 = {pk(k[0], k[1]), pk(k[2], k[3]), pk(k[4], k[5]), pk(k[6], k[7])};
            const u32x4 k1 = {pk(k[8], k[9]), pk(k[10], k[11]), pk(k[12], k[13]), pk(k[14], k[15])};
            const u32x4 v0 = {pk(v[0], v[1]), pk(v[2], v[3]), pk(v[4], v[5]), pk(v[6], v[7])};
            const u32x4 v1 = {pk(v[8], v[9]), pk(v[10], v[11]), pk(v[12], v[13]), pk(v[14], v[15])};
            const int kvo = KV_OFF + lyr * 4 * MB + (T * 16 + r) * 4096;
            stw(rs, lane * 16, kvo, k0); stw(rs, lane * 16 + 1024, kvo, k1); stw(rs, lane * 16 + 2048, kvo, v0); stw(rs, lane * 16 + 3072, kvo, v1);
            {
                float na = 0.f, nb = 0.f;
#pragma unroll
                for (int i = 0; i < 8; ++i) { na = __builtin_fmaf(k[i], k[i], na); nb = __builtin_fmaf(k[8 + i], k[8 + i], nb); }
                const f32x2 nn = {wave32_max(sum_xor32(na)), wave32_max(sum_xor32(nb))};
                if (lane == 0) { stst(rs, 0, KN_OFF + ((lyr * 64 + T) * 16 + r) * 8, nn); *(LAS f32x2*)(lds + L_KN + (8 + w) * 8) = nn; }
            }
            LAS char* kvl = lds + L_KV + (8 + w) * 4096 + lane * 16;
            *(LAS u32x4*)(kvl) = k0; *(LAS u32x4*)(kvl + 1024) = k1; *(LAS u32x4*)(kvl + 2048) = v0; *(LAS u32x4*)(kvl + 3072) = v1;
            LAS char* ql = lds + L_Q + w * 2048 + lane * 16;
            *(LAS u32x4*)(ql) = u32x4{pk(q[0], q[1]), pk(q[2], q[3]), pk(q[4], q[5]), pk(q[6], q[7])};
            *(LAS u32x4*)(ql + 1024) = u32x4{pk(q[8], q[9]), pk(q[10], q[11]), pk(q[12], q[13]), pk(q[14], q[15])};
            {
                float na = 0.f, nb = 0.f;
#pragma unroll
                for (int i = 0; i < 8; ++i) { na = __builtin_fmaf(q[i], q[i], na); nb = __builtin_fmaf(q[8 + i], q[8 + i], nb); }
                const f32x2 nn = {wave32_max(sum_xor32(na)), wave32_max(sum_xor32(nb))};
                if (lane == 0) *(LAS f32x2*)(lds + L_QN + w * 8) = nn;
            }
            DRAIN();
            BAR();
            if (w == 0 && lane == 0) set_flag(p, 1 + 5 * lyr, g, r, tag);
            TS(tb + 3);
        }
        {
            PHASE_VARS();
            attn_phase(p, lds, rs, w, g, r, lyr, lane, tag TS_ARG(layer == 0 ? 22 : 48));
            TS(tb + 4); TS(tb + 5); TS(tb + 6);
            DRAIN();
            BAR();
            if (w == 0 && lane == 0) set_flag(p, 2 + 5 * lyr, g, r, tag);
            TS(tb + 7);
        }
        {
            PHASE_VARS();
            const int mt = w & 1, kh = (w >> 1) & 1, T = 4 * g + 2 * m + mt, u = 2 * n + kh;
            const int xo = XF_OFF + lyr * 2 * MB + (T * 32 + 2 * u) * 1024;
            const u32x4 r0 = ldw(rs, lane * 16, xo), r1 = ldw(rs, lane * 16 + 1024, xo);
            if (w == 0) wait_group(p, 2 + 5 * lyr, g, tag, lane);
            BAR();
            TS(tb + 8);
            f32x16 acc[2] = {};
            u32x4 xa[16];
            const int oo = O_OFF + lyr * 2 * MB + (T * 32 + 16 * kh) * 1024;
#pragma unroll
            for (int s = 0; s < 16; ++s) { LDA(xa, s, oo); SB(); }
            const LAS char* wb = lds + 96 * KB + kh * 32768 + lane * 16;
            const LAS char* wlast = lds + (kh ? 94 * KB : 96 * KB + 15 * 2048) + lane * 16;
            bf16x8 wf[4][2];
            const int fo = kh * 1024, fs = 1024 - fo;
#pragma unroll
            for (int s = 0; s < 3; ++s) { wf[s][0] = ldsfrag(wb + fo + s * 2048); wf[s][1] = ldsfrag(wb + fs + s * 2048); }
#pragma unroll
            for (int s = 0; s < 16; ++s) {
                if (s + 3 < 15) { wf[(s + 3) & 3][0] = ldsfrag(wb + fo + (s + 3) * 2048); wf[(s + 3) & 3][1] = ldsfrag(wb + fs + (s + 3) * 2048); }
                if (s + 3 == 15) { wf[3][0] = ldsfrag(wlast + fo); wf[3][1] = ldsfrag(wlast + fs); }
                const bf16x8 x = asfrag(xa[s]);
                acc[0] = mfma32(wf[s & 3][0], x, acc[0]); acc[1] = mfma32(wf[s & 3][1], x, acc[1]);
                SGB(0x100, 1); SGB(0x8, 1); SGB(0x20, 2); SGB(0x100, 1); SGB(0x8, 1);
                SB();
            }
            const f32x16 snd = acc[1];
#pragma unroll
            for (int g4 = 0; g4 < 4; ++g4) *(LAS f32x4*)(lds + L_RED2 + ((w * 4 + g4) * 64 + lane) * 16) = f32x4{snd[4 * g4], snd[4 * g4 + 1], snd[4 * g4 + 2], snd[4 * g4 + 3]};
            TS(tb + 9);
            BAR();
            const f32x16 own = acc[0];
            const int pw = w ^ 2;
            float v[16];
#pragma unroll
            for (int g4 = 0; g4 < 4; ++g4) { f32x4 t = *(LAS f32x4*)(lds + L_RED2 + ((pw * 4 + g4) * 64 + lane) * 16);
#pragma unroll
                for (int i = 0; i < 4; ++i) v[4 * g4 + i] = own[4 * g4 + i] + t[i]; }
            const unsigned rr[8] = {r0.x, r0.y, r0.z, r0.w, r1.x, r1.y, r1.z, r1.w};
            unsigned c[8];
#pragma unroll
            for (int i = 0; i < 8; ++i) c[i] = pk(v[2 * i] + bflo(rr[i]), v[2 * i + 1] + bfhi(rr[i]));
            const int yo = X2_OFF + lyr * 2 * MB + (T * 32 + 2 * u) * 1024;
            stw(rs, lane * 16, yo, u32x4{c[0], c[1], c[2], c[3]});
            stw(rs, lane * 16 + 1024, yo, u32x4{c[4], c[5], c[6], c[7]});
            stats_store(rs, 2 * lyr + 1, T, u, lane, c);
            DRAIN();
            BAR();
            if (w == 0 && lane == 0) set_flag(p, 3 + 5 * lyr, g, r, tag);
            TS(tb + 10);
        }
        {
            PHASE_VARS();
            if (w == 0) wait_group(p, 3 + 5 * lyr, g, tag, lane);
            BAR();
            TS(tb + 11);
            const int T = 4 * g + w;
            const LAS char* wbA = lds + lane * 16;
            const LAS char* wbB = lds + 3072 + (h * 16 + (lane & 15)) * 16;
            LAS float* cs1 = (LAS float*)(lds + L_CS1);
            f32x16 acc[4] = {};
            u32x4 xa[32];
            f32x2 sv[8];
            const int sto = ST_OFF + (2 * lyr + 1) * ST_BUF + T * 4096, xo = X2_OFF + lyr * 2 * MB + T * 32768;
#pragma unroll
            for (int s = 0; s < PD0; ++s) { LDA(xa, s, xo); SB(); }
            auto ldW = [&](int s, int t) { return t < 3 ? ldsfrag(wbA + w1_off(s) + t * 1024) : ldsfrag(wbB + w1_off(s)); };
            bf16x8 wf[2][4];
#pragma unroll
            for (int t = 0; t < 4; ++t) wf[0][t] = ldW(0, t);
#pragma unroll
            for (int s = 0; s < 32; ++s) {
                if (s == 20) BAR();
#pragma unroll
                for (int j = (s == 0 ? PD0 : a_issued(s - 1, 32)); j < a_issued(s, 32); ++j) LDA(xa, j, xo);
                if (s >= 16 && s < 24) sv[s - 16] = ldst(rs, (8 * h * 32 + tok) * 8 + (s - 16) * 256, sto);
                const bf16x8 x = asfrag(xa[s]);
#pragma unroll
                for (int t = 0; t < 4; ++t) {
                    if (s + 1 < 32) wf[(s + 1) & 1][t] = ldW(s + 1, t);
                    acc[t] = mfma32(wf[s & 1][t], x, acc[t]);
                }
                SGB(0x100, 1); SGB(0x8, 1); SGB(0x20, 1); SGB(0x100, 1); SGB(0x8, 1); SGB(0x20, 1); SGB(0x100, 1); SGB(0x8, 1); SGB(0x100, 1); SGB(0x8, 1);
                SB();
            }
            float mean, rstd; ln_stats(sv, mean, rstd);
            const float mr = mean * rstd;
            TS(tb + 12);
            BAR();
            TS(tb + 13);
            const int ho = H_OFF + lyr * 8 * MB + (T * 112 + 7 * r) * 1024;
            const f32x2 nm2 = {-mean, -mean}, rs2 = {rstd, rstd}, ry2 = {-LOG2E * rstd, -LOG2E * rstd}, one2 = {1.f, 1.f};
#pragma unroll
            for (int t = 0; t < 4; ++t) {
                constexpr int dummy = 0; (void)dummy;
                const int np = t < 3 ? 8 : 4;
                f32x2 tt[8], z[8], e[8];
#pragma unroll
                for (int g4 = 0; g4 < 4; ++g4) {
                    if (2 * g4 < np) {
                        const f32x4 c4 = *(LAS f32x4*)(cs1 + t * 32 + 8 * g4 + 4 * h);
                        tt[2 * g4] = __builtin_elementwise_fma(nm2, f32x2{c4[0], c4[1]}, f32x2{acc[t][4 * g4], acc[t][4 * g4 + 1]});
                        tt[2 * g4 + 1] = __builtin_elementwise_fma(nm2, f32x2{c4[2], c4[3]}, f32x2{acc[t][4 * g4 + 2], acc[t][4 * g4 + 3]});
                    }
                }
#pragma unroll
                for (int i = 0; i < 8; ++i) if (i < np) e[i] = tt[i] * ry2;
                SB();
#pragma unroll
                for (int i = 0; i < 8; ++i) if (i < np) e[i] = f32x2{__builtin_amdgcn_exp2f(e[i].x), __builtin_amdgcn_exp2f(e[i].y)};
#pragma unroll
                for (int i = 0; i < 8; ++i) if (i < np) z[i] = tt[i] * rs2;
                SB();
#pragma unroll
                for (int i = 0; i < 8; ++i) if (i < np) e[i] = e[i] + one2;
                SB();
#pragma unroll
                for (int i = 0; i < 8; ++i) if (i < np) e[i] = f32x2{__builtin_amdgcn_rcpf(e[i].x), __builtin_amdgcn_rcpf(e[i].y)};
                SB();
#pragma unroll
                for (int i = 0; i < 8; ++i) if (i < np) z[i] = z[i] * e[i];
                stw(rs, lane * 16 + (2 * t) * 1024, ho, u32x4{pk(z[0].x, z[0].y), pk(z[1].x, z[1].y), pk(z[2].x, z[2].y), pk(z[3].x, z[3].y)});
                if (t < 3) stw(rs, lane * 16 + (2 * t + 1) * 1024, ho, u32x4{pk(z[4].x, z[4].y), pk(z[5].x, z[5].y), pk(z[6].x, z[6].y), pk(z[7].x, z[7].y)});
                SB();
            }
            DRAIN();
            BAR();
            if (w == 0 && lane == 0) set_flag(p, 4 + 5 * lyr, g, r, tag);
            TS(tb + 14);
        }
        {
            PHASE_VARS();
            const int mt = w & 1, kh = (w >> 1) & 1, T = 4 * g + 2 * m + mt, u = 2 * n + kh;
            const int yo = X2_OFF + lyr * 2 * MB + (T * 32 + 2 * u) * 1024;
            const u32x4 r0 = ldw(rs, lane * 16, yo), r1 = ldw(rs, lane * 16 + 1024, yo);
            if (w == 0) wait_group(p, 4 + 5 * lyr, g, tag, lane);
            BAR();
            TS(tb + 15);
            const LAS char* wb = lds + kh * 2048 + lane * 16;
            f32x16 acc[2] = {};
            const int ho = H_OFF + lyr * 8 * MB + (T * 112 + 56 * kh) * 1024;
            u32x4 xa[56];
#pragma unroll
            for (int s = 0; s < PD0; ++s) { LDA(xa, s, ho); SB(); }
            auto woff = [](int s) { return w2_slot(s); };
            bf16x8 wf[4][2];
            const int fo = kh * 1024, fs = 1024 - fo;
#pragma unroll
            for (int s = 0; s < 3; ++s) { wf[s][0] = ldsfrag(wb + fo + woff(s)); wf[s][1] = ldsfrag(wb + fs + woff(s)); }
#pragma unroll
            for (int s = 0; s < 56; ++s) {
                if (s == 18) { BAR(); TS(tb + 16); }
                if (s == 35) { BAR(); TS(tb + 17); }
#pragma unroll
                for (int j = (s == 0 ? PD0 : a_issued(s - 1, 56)); j < a_issued(s, 56); ++j) LDA(xa, j, ho);
                if (s + 3 < 56) { wf[(s + 3) & 3][0] = ldsfrag(wb + fo + woff(s + 3)); wf[(s + 3) & 3][1] = ldsfrag(wb + fs + woff(s + 3)); }
                const bf16x8 x = asfrag(xa[s]);
                acc[0] = mfma32(wf[s & 3][0], x, acc[0]); acc[1] = mfma32(wf[s & 3][1], x, acc[1]);
                SGB(0x100, 1); SGB(0x8, 1); SGB(0x20, 2); SGB(0x100, 1); SGB(0x8, 1);
                SB();
            }
            const f32x16 snd = acc[1];
#pragma unroll
            for (int g4 = 0; g4 < 4; ++g4) *(LAS f32x4*)(lds + L_RED4 + ((w * 4 + g4) * 64 + lane) * 16) = f32x4{snd[4 * g4], snd[4 * g4 + 1], snd[4 * g4 + 2], snd[4 * g4 + 3]};
            TS(tb + 18);
            BAR();
            const f32x16 own = acc[0];
            const int pw = w ^ 2;
            float v[16];
#pragma unroll
            for (int g4 = 0; g4 < 4; ++g4) { f32x4 t = *(LAS f32x4*)(lds + L_RED4 + ((pw * 4 + g4) * 64 + lane) * 16);
#pragma unroll
                for (int i = 0; i < 4; ++i) v[4 * g4 + i] = own[4 * g4 + i] + t[i]; }
            const unsigned rr[8] = {r0.x, r0.y, r0.z, r0.w, r1.x, r1.y, r1.z, r1.w};
            unsigned c[8];
#pragma unroll
            for (int i = 0; i < 8; ++i) c[i] = pk(v[2 * i] + bflo(rr[i]), v[2 * i + 1] + bfhi(rr[i]));
            if (lyr == 0) {
                stw(rs, lane * 16, XF_OFF + 2 * MB + (T * 32 + 2 * u) * 1024, u32x4{c[0], c[1], c[2], c[3]});
                stw(rs, lane * 16 + 1024, XF_OFF + 2 * MB + (T * 32 + 2 * u) * 1024, u32x4{c[4], c[5], c[6], c[7]});
                stats_store(rs, 2, T, u, lane, c);
                DRAIN();
                BAR();
                if (w == 0 && lane == 0) set_flag(p, 5, g, r, tag);
            } else {
                auto s0 = __builtin_amdgcn_permlane32_swap(c[0], c[2], false, false);
                auto s1 = __builtin_amdgcn_permlane32_swap(c[1], c[3], false, false);
                auto s2 = __builtin_amdgcn_permlane32_swap(c[4], c[6], false, false);
                auto s3 = __builtin_amdgcn_permlane32_swap(c[5], c[7], false, false);
                uint16_t* orow = p.out + (size_t)(T * 32 + (lane & 31)) * DM + 32 * u + 8 * h;
                *(GAS u32x4*)(orow) = u32x4{s0[0], s1[0], s0[1], s1[1]};
                *(GAS u32x4*)(orow + 16) = u32x4{s2[0], s3[0], s2[1], s3[1]};
            }
            TS(tb + 19);
        }
    }
    asm volatile("s_waitcnt lgkmcnt(0)" ::: "memory");
    if (w == 0) TS_DUMP();
}

template <int PART> DI void pretile(const Params& p, LAS char* lds, unsigned lds0, rsrc_t rs, int hw, int g, int r, int lane) {
    const int h = lane >> 5, a = (lane & 15) >> 2, pp = lane & 3, ch = (lane >> 4) & 1;
    const int n = r >> 1, j = 2 * g + (r & 1);
    const unsigned scr0 = lds0 + L_SCR + hw * 8192;
    const LAS char* scr = lds + L_SCR + hw * 8192;
    if (hw == 0) {
        const char* base = (const char*)(p.wqkv + (size_t)32 * g * 1536 + 32 * r);
        if (PART == 0) {
#pragma unroll
            for (int i = 0; i < 6; ++i) { const int L = 64 * i + lane, k = L / 12, c = L % 12; glds16s<false>(base, (unsigned)(k * 1536 + (c >> 2) * 512 + (c & 3) * 8) * 2, scr0 + i * 1024); }
            return;
        }
        DRAIN();
        const LAS char* wb = scr + (4 * h + a) * 192 + ch * 32 + pp * 8;
#pragma unroll
        for (int s = 0; s < 2; ++s)
#pragma unroll
            for (int t = 0; t < 3; ++t) stw(rs, lane * 16, WQT_OFF + ((r * 32 + 2 * g + s) * 3 + t) * 1024, wfrag_tr(wb + s * 16 * 192 + t * 64, 8 * 192));
    } else if (hw == 1) {
        const char* base = (const char*)(p.w1 + (size_t)32 * g * FF + 112 * r);
        if (PART == 0) {
#pragma unroll
            for (int b = 0; b < 7; ++b) {
                const int L = 64 * b + lane, G = L / 56, x = L - G * 56;
                const int aa = x < 48 ? ((x >> 2) & 3) : ((x - 48) >> 1);
                const int c = x < 48 ? ((x >> 4) * 4 + (x & 3)) : (12 + (x & 1));
                glds16s<false>(base, (unsigned)((4 * G + aa) * FF + c * 8) * 2, scr0 + b * 1024);
            }
            return;
        }
        DRAIN();
        const LAS char* wbA = scr + h * 896 + a * 64 + ch * 32 + pp * 8;
        const LAS char* wbB = scr + h * 896 + 768 + a * 32 + pp * 8;
#pragma unroll
        for (int s = 0; s < 2; ++s) {
            const int dst = W1T_OFF + r * 114688 + (2 * g + s) * 3584;
#pragma unroll
            for (int t = 0; t < 3; ++t) stw(rs, lane * 16, dst + t * 1024, wfrag_tr(wbA + s * 3584 + t * 256, 1792));
            const u32x4 f3 = wfrag_tr(wbB + s * 3584, 1792);
            if (ch == 0) stw(rs, (h * 16 + (lane & 15)) * 16, dst + 3072, f3);
        }
    } else {
        const int rr = lane >> 3, slot = lane & 7, chunk = slot ^ (((rr >> 1) & 1) << 2);
        const unsigned vo = (unsigned)(rr * DM + chunk * 8) * 2;
        const int s0 = hw == 3 ? j : (j < 16 ? 4 * j : 64 + 3 * (j - 16)), ns = hw == 3 ? 1 : (j < 16 ? 4 : 3);
        const char* base = (const char*)((hw == 3 ? p.wo : p.w2) + (size_t)16 * s0 * DM + 64 * n);
        if (PART == 0) {
            for (int i = 0; i < 2 * ns; ++i) glds16s<false>(base + (size_t)i * 8 * DM * 2, vo, scr0 + i * 1024);
            return;
        }
        DRAIN();
        const int sw = ((a >> 1) & 1) << 6;
        const LAS char* wb0 = scr + (4 * h + a) * 128 + (0 ^ sw) + ch * 32 + pp * 8;
        const LAS char* wb1 = scr + (4 * h + a) * 128 + (64 ^ sw) + ch * 32 + pp * 8;
        for (int i = 0; i < ns; ++i) {
            const int s = s0 + i;
            const int kh = s >= 56 ? 1 : 0, sl = s - 56 * kh;
            const int dst = hw == 3 ? WOT_OFF + n * 65536 + s * 2048 : W2T_OFF + (n * 56 + sl) * 4096 + kh * 2048;
            stw(rs, lane * 16, dst, wfrag_tr(wb0 + i * 2048, 1024));
            stw(rs, lane * 16, dst + 1024, wfrag_tr(wb1 + i * 2048, 1024));
        }
    }
    DRAIN();
}

constexpr int w2_regsp(int j) { return j < 10 ? j : j < 30 ? j + 4 : j + 8; }
DI void helper_path(const Params& p, LAS char* lds, unsigned lds0_, rsrc_t rs, const int w, const int g_, const int r_) {
    unsigned tag = 0;
    TS_DECL();
    {   const int layer = 0; PHASE_VARS(); HELPER_VARS();
        tag = read_tag(p); asm volatile("" :: "s"(tag) : "memory");
        if (hw == 3) {
            unsigned keep;
            const unsigned vo = (unsigned)(lane < 44 ? lane : 43) << 20;
            asm volatile("s_mov_b32 %0, m0\n\ts_mov_b32 m0, %3\n\ts_nop 0\n\tglobal_load_lds_dword %1, %2\n\ts_mov_b32 m0, %0" : "=&s"(keep) : "v"(vo), "s"(p.ws), "s"(lds0 + 140 * KB) : "memory");
            const unsigned vo2 = (unsigned)(lane & 1) << 20;
            asm volatile("s_mov_b32 %0, m0\n\ts_mov_b32 m0, %3\n\ts_nop 0\n\tglobal_load_lds_dword %1, %2\n\ts_mov_b32 m0, %0" : "=&s"(keep) : "v"(vo2), "s"(p.out), "s"(lds0 + 141 * KB) : "memory");
        }
        pretile<0>(p, lds, lds0, rs, hw, g, r, lane);
        if (hw == 0) { pretile<1>(p, lds, lds0, rs, hw, g, r, lane); BAR(); }
        else { BAR(); pretile<1>(p, lds, lds0, rs, hw, g, r, lane); }
    }
    for (int layer = 0; layer < 2; ++layer) {
        {
            PHASE_VARS(); HELPER_VARS();
            HTS(layer * 20 + 0);
            BAR();
            if (lyr == 0) {
                if (w == 6 && lane == 0) set_flag(p, 10, g, r, tag);
                stage_lin(wsb, lds0, WQT_OFF + r * 98304, L_WQ, 96, hw, lane);
                float acc = 0.f;
                const LAS char* wbq = lds + L_WQ + (hw < 3 ? hw : 0) * 1024 + lane * 16;
                WAITV(18); BAR();
                acc = colsum_frags<8>(wbq, [](int i) { return (0 + i) * 3072; }, acc);
                WAITV(12); BAR();
                acc = colsum_frags<8>(wbq, [](int i) { return (8 + i) * 3072; }, acc);
                WAITV(6); BAR();
                wait_word(p, flagp(p, 10, lane & 15, (lane & 16) ? (r ^ 1) : r), tag);
                for (int i = hw; i < 64; i += 4) if (i < 62) glds16s<true>(wsb + WOT_OFF + n * 65536 + i * 1024, lane * 16, lds0 + 96 * KB + i * 1024); else glds16s<true>(wsb + WOT_OFF + n * 65536 + (i - 4) * 1024, lane * 16, lds0 + 96 * KB + (i - 4) * 1024);
                acc = colsum_frags<8>(wbq, [](int i) { return (16 + i) * 3072; }, acc);
                WAITV(16); BAR();
                acc = colsum_frags<8>(wbq, [](int i) { return (24 + i) * 3072; }, acc);
                acc = sum_xor32(acc);
                if (h == 0 && hw < 3) ((LAS float*)(lds + L_CSQ))[hw * 32 + tok] = acc;
            } else {
                __builtin_amdgcn_s_sleep(WODELAY);
                for (int i = hw; i < 62; i += 4) glds16s<true>(wsb + WOT_OFF + n * 65536 + i * 1024, lane * 16, lds0 + 96 * KB + i * 1024);
            }
            BAR();
            if (w == 7 && lane < 2) *(LAS u32x4*)(lds + L_ONES + lane * 1024) = u32x4{0x3f803f80u, 0x3f803f80u, 0x3f803f80u, 0x3f803f80u};
            HTS(layer * 20 + 2);
            stage_lin(wsb, lds0, WOT_OFF + n * 65536 + 62 * KB, 94 * KB, 2, hw, lane);
            stage_lin(wsb, lds0, W1T_OFF + r * 114688, 56 * KB, 35, hw, lane);
            HTS(layer * 20 + 3);
            BAR();
        }
        u32x4 buf[48];
        {   PHASE_VARS(); HELPER_VARS();

            attn_phase(p, lds, rs, w, g, r, lyr, lane, tag TS_ARG(layer == 0 ? 40 : 44));
            HTS(layer * 20 + 4);
            DRAIN();
            HTS(layer * 20 + 5);
            BAR();
            stage_lin(wsb, lds0, W1T_OFF + r * 114688 + 35 * KB, 0, 39, hw, lane);
            HTS(layer * 20 + 6);
        }
        {
            PHASE_VARS(); HELPER_VARS();
            const int wsrc = W2T_OFF + n * 56 * 4096 + hw * 1024;
            BAR();
#pragma unroll
            for (int j = 0; j < 12; ++j) { buf[j] = ldw(rs, lane * 16, wsrc + w2_regsp(j) * 4096); SB(); }
            BAR();
#pragma unroll
            for (int j = 12; j < 22; ++j) { buf[j] = ldw(rs, lane * 16, wsrc + w2_regsp(j) * 4096); SB(); }
            BAR();
            stage_lin(wsb, lds0, W1T_OFF + r * 114688 + 75264, 94 * KB, 39, hw, lane);
        }
        {
            PHASE_VARS(); HELPER_VARS();
            HTS(layer * 20 + 8);
            WAITV(31);
            BAR();
            const int wsrc = W2T_OFF + n * 56 * 4096 + hw * 1024;
            const LAS char* wbc = hw < 3 ? lds + hw * 1024 + lane * 16 : lds + 3072 + (h * 16 + (lane & 15)) * 16;
            float cs_part = 0.f;
            if (lyr == 0) {
                cs_part = colsum_frags<11>(wbc, [](int i) { return w1_off(i); }, 0.f);
                cs_part = colsum_frags<10>(wbc, [](int i) { return w1_off(11 + i); }, cs_part);
            }
            stage_lin(wsb, lds0, W2T_OFF + (n * 56 + 10) * 4096, 40 * KB, 16, hw, lane);
            stage_lin(wsb, lds0, W2T_OFF + (n * 56 + 34) * 4096, 136 * KB, 16, hw, lane);
#pragma unroll
            for (int j = 22; j < 34; ++j) { buf[j] = ldw(rs, lane * 16, wsrc + w2_regsp(j) * 4096); SB(); }
            WAITV(20);
            HTS(layer * 20 + 10);
            BAR();
#pragma unroll
            for (int j = 34; j < 48; ++j) { buf[j] = ldw(rs, lane * 16, wsrc + w2_regsp(j) * 4096); SB(); }
            if (lyr == 0) {
                LAS float* cs1 = (LAS float*)(lds + L_CS1);
                cs_part = colsum_frags<11>(wbc, [](int i) { return w1_off(21 + i); }, cs_part);
                const float s = sum_xor32(cs_part);
                if (h == 0 && (hw < 3 || tok < 16)) cs1[hw * 32 + tok] = s;
            }
            WAITV(14);
            BAR();
            HTS(layer * 20 + 11);
#pragma unroll
            for (int j = 0; j < 30; ++j) *(LAS u32x4*)(lds + w2_slot(w2_regsp(j)) + hw * 1024 + lane * 16) = buf[j];
            HTS(layer * 20 + 12);
            BAR();
            if (lyr == 0) {
#pragma unroll
                for (int j = 0; j < 24; ++j) { buf[j] = ldw(rs, lane * 16, WQT_OFF + r * 98304 + (4 * j + hw) * 1024); SB(); }
            }
        }
        {
            PHASE_VARS(); HELPER_VARS();
            BAR();
            BAR();
#pragma unroll
            for (int j = 30; j < 48; ++j) *(LAS u32x4*)(lds + w2_slot(w2_regsp(j)) + hw * 1024 + lane * 16) = buf[j];
            if (lyr == 1 && blockIdx.x == 0 && w == 7) {
                bool ok = false;
                for (unsigned spins = 0; spins < (1u << 16) && !ok; ++spins) {
                    bool mine = true;
#pragma unroll
                    for (int j = 0; j < 4; ++j) mine &= __hip_atomic_load((gu32*)(p.ws + ACK_OFF) + (lane + 64 * j) * 32, __ATOMIC_RELAXED, __HIP_MEMORY_SCOPE_AGENT) == tag;
                    ok = __all(mine);
                }
                if (lane == 0) __hip_atomic_store((gu32*)(p.ws + EPOCH_OFF), tag, __ATOMIC_RELAXED, __HIP_MEMORY_SCOPE_AGENT);
            }
            HTS(layer * 20 + 15);
            BAR();
            BAR();
            if (lyr == 0) {
                BAR();
#pragma unroll
                for (int j = 0; j < 24; ++j) *(LAS u32x4*)(lds + L_WQ + (4 * j + hw) * 1024 + lane * 16) = buf[j];
                HTS(17);
            }
        }
    }
    asm volatile("s_waitcnt lgkmcnt(0)" ::: "memory");
    if (w == 4) HTS_DUMP();
}

__global__ __launch_bounds__(512, 2) void k_mega(Params p) {
    extern __shared__ __attribute__((aligned(16))) char smem_[];
    LAS char* lds = (LAS char*)smem_;
    const unsigned lds0 = (unsigned)(uintptr_t)smem_;
    const int b = blockIdx.x, idx = b >> 3;
    const int g_ = (b & 3) * 4 + (idx >> 3), r_ = ((b >> 2) & 1) * 8 + (idx & 7);
    const int w = __builtin_amdgcn_readfirstlane(threadIdx.x >> 6);
    const rsrc_t rs = __builtin_amdgcn_make_buffer_rsrc((void*)p.ws, 0, WS_USED, 0x00020000);
    if (w < 4) main_path(p, lds, rs, w, g_, r_);
    else helper_path(p, lds, lds0, rs, w, g_, r_);
}
}

extern "C" void kernel_launch(void* const* d_in, const int* in_sizes, int n_in, void* d_out, int out_size, void* d_ws, size_t ws_size, hipStream_t stream) {
    Params p;
    p.x = (const uint16_t*)d_in[0]; p.wqkv = (const uint16_t*)d_in[1]; p.wo = (const uint16_t*)d_in[2];
    p.w1 = (const uint16_t*)d_in[4]; p.w2 = (const uint16_t*)d_in[5];
    p.out = (uint16_t*)d_out; p.ws = (char*)d_ws;
    (void)hipFuncSetAttribute((const void*)k_mega, hipFuncAttributeMaxDynamicSharedMemorySize, LDS_BYTES);
    k_mega<<<256, 512, LDS_BYTES, stream>>>(p);
}
```

```cpp
#include <hip/hip_runtime.h>
#include <hip/hip_bf16.h>
#include <stdint.h>

#define GAS __attribute__((address_space(1)))
#define LAS __attribute__((address_space(3)))
#define DI __device__ __forceinline__

typedef __bf16 bf16x8 __attribute__((ext_vector_type(8)));
typedef __bf16 bf16x2 __attribute__((ext_vector_type(2)));
typedef short s16x4 __attribute__((ext_vector_type(4)));
typedef float f32x16 __attribute__((ext_vector_type(16)));
typedef float f32x4 __attribute__((ext_vector_type(4)));
typedef float f32x2 __attribute__((ext_vector_type(2)));
typedef unsigned u32x4 __attribute__((ext_vector_type(4)));
typedef unsigned u32x2 __attribute__((ext_vector_type(2)));
typedef GAS unsigned gu32;

#define TS_DECL()
#define TS_PARAM
#define TS_ARG(tb)
#define TS(i)
#define TS_DUMP()
#define HTS(i)
#define HTS_DUMP()
#define MAPMODE 3
namespace {
constexpr int DM = 512, FF = 1792;
constexpr float EPS = 1e-5f;
constexpr float LOG2E = 1.4426950408889634f;
constexpr float QSCALE = 0.25f * LOG2E;

constexpr int MB = 1 << 20, KB = 1024;
constexpr int XF_OFF = 0;
constexpr int O_OFF = 4 * MB;
constexpr int X2_OFF = 8 * MB;
constexpr int H_OFF = 12 * MB;
constexpr int KV_OFF = 28 * MB;
constexpr int WQT_OFF = 36 * MB;
constexpr int WOT_OFF = WQT_OFF + 1536 * KB;
constexpr int W1T_OFF = 38 * MB;
constexpr int W2T_OFF = 40 * MB;
constexpr int ST_OFF = 42 * MB;
constexpr int ST_BUF = 262144;
constexpr int KN_OFF = 43 * MB;
constexpr int FLAG_OFF = 43 * MB + 65536;
constexpr int FLAG_BYTES = 11 * 256 * 128;
constexpr int TMO_OFF = FLAG_OFF + FLAG_BYTES;
constexpr int EPOCH_OFF = TMO_OFF + 256;
constexpr int ACK_OFF = EPOCH_OFF + 256;
constexpr int WS_USED = 44 * MB;

constexpr int L_WQ = 0;
constexpr int L_Q = 0, L_KV = 8 * KB, L_ONES = 91 * KB;
constexpr int L_RED2 = 40 * KB;
constexpr int wo_off(int s) { return s < 31 ? 96 * KB + s * 2048 : 94 * KB; }
constexpr int L_RED4 = 96 * KB;
constexpr int L_SCR = 100 * KB;
constexpr int L_TAB = 158 * KB, L_CSQ = L_TAB, L_CS1 = L_TAB + 512, L_STT = L_TAB + 1024;
constexpr int L_KN = L_TAB + 384, L_QN = L_TAB + 384 + 96;
constexpr int LDS_BYTES = 160 * KB;
constexpr int w1_off(int s) { return s < 10 ? 56 * KB + s * 3584 : s < 21 ? (s - 10) * 3584 : 94 * KB + (s - 21) * 3584; }
constexpr int w2_slot(int sp) { return (sp < 38 ? sp : sp - 38) * 4096; }
constexpr int AUX_SC1 = 16;

struct Params {
    const uint16_t* x; const uint16_t* wqkv; const uint16_t* wo; const uint16_t* w1; const uint16_t* w2;
    uint16_t* out; char* ws;
};

DI int lane_id() { return (int)__builtin_amdgcn_mbcnt_hi(~0u, __builtin_amdgcn_mbcnt_lo(~0u, 0u)); }
DI f32x16 mfma32(bf16x8 a, bf16x8 b, f32x16 c) { return __builtin_amdgcn_mfma_f32_32x32x16_bf16(a, b, c, 0, 0, 0); }
DI unsigned pk(float a, float b) { bf16x2 v = {(__bf16)a, (__bf16)b}; return __builtin_bit_cast(unsigned, v); }
DI float bflo(unsigned u) { return __uint_as_float(u << 16); }
DI float bfhi(unsigned u) { return __uint_as_float(u & 0xffff0000u); }
DI bf16x8 asfrag(u32x4 v) { return __builtin_bit_cast(bf16x8, v); }
DI s16x4 trd(const LAS char* p) { return __builtin_amdgcn_ds_read_tr16_b64_v4i16((LAS s16x4*)p); }
DI u32x4 wfrag_tr(const LAS char* p, int off2) {
    const u32x2 lo = __builtin_bit_cast(u32x2, trd(p)), hi = __builtin_bit_cast(u32x2, trd(p + off2));
    return u32x4{lo.x, lo.y, hi.x, hi.y};
}
DI bf16x8 ldsfrag(const LAS char* p) { return *(const LAS bf16x8*)p; }
template <int N, class F> DI float colsum_frags(const LAS char* p, F off, float acc) {
    u32x4 f[N];
#pragma unroll
    for (int i = 0; i < N; ++i) f[i] = *(const LAS u32x4*)(p + off(i));
    float a1 = 0.f;
#pragma unroll
    for (int i = 0; i < N; ++i) {
        unsigned w0 = f[i].x, w1 = f[i].y, w2 = f[i].z, w3 = f[i].w;
        asm("v_dot2c_f32_bf16 %0, %2, %6\n\tv_dot2c_f32_bf16 %1, %3, %6\n\tv_dot2c_f32_bf16 %0, %4, %6\n\tv_dot2c_f32_bf16 %1, %5, %6"
            : "+v"(acc), "+v"(a1) : "v"(w0), "v"(w1), "v"(w2), "v"(w3), "v"(0x3f803f80u));
    }
    asm volatile("s_nop 2" : "+v"(acc), "+v"(a1));
    return acc + a1;
}
DI float sum_xor32(float v) { auto r = __builtin_amdgcn_permlane32_swap(__float_as_uint(v), __float_as_uint(v), false, false); return __uint_as_float(r[0]) + __uint_as_float(r[1]); }
DI float max_xor32(float v) { auto r = __builtin_amdgcn_permlane32_swap(__float_as_uint(v), __float_as_uint(v), false, false); return fmaxf(__uint_as_float(r[0]), __uint_as_float(r[1])); }
template <bool SC1> DI void glds16s(const void* sbase, unsigned voff, unsigned lds_addr) {
    unsigned keep;
    if (SC1) asm volatile("s_mov_b32 %0, m0\n\ts_mov_b32 m0, %3\n\ts_nop 0\n\tglobal_load_lds_dwordx4 %1, %2 sc1\n\ts_mov_b32 m0, %0" : "=&s"(keep) : "v"(voff), "s"(sbase), "s"(lds_addr) : "memory");
    else asm volatile("s_mov_b32 %0, m0\n\ts_mov_b32 m0, %3\n\ts_nop 0\n\tglobal_load_lds_dwordx4 %1, %2\n\ts_mov_b32 m0, %0" : "=&s"(keep) : "v"(voff), "s"(sbase), "s"(lds_addr) : "memory");
}
#define BAR() asm volatile("s_waitcnt lgkmcnt(0)\n\ts_barrier" ::: "memory")
#define DRAIN() asm volatile("s_waitcnt vmcnt(0)" ::: "memory")
#define WAITV(n) asm volatile("s_waitcnt vmcnt(" #n ")" ::: "memory")
#define SB() __builtin_amdgcn_sched_barrier(0)
#define SGB(mask, n) __builtin_amdgcn_sched_group_barrier(mask, n, 0)

typedef __amdgpu_buffer_rsrc_t rsrc_t;
DI u32x4 ldw(rsrc_t rs, int voff, int soff) { return __builtin_amdgcn_raw_buffer_load_b128(rs, voff, soff, AUX_SC1); }
DI void stw(rsrc_t rs, int voff, int soff, u32x4 v) { __builtin_amdgcn_raw_buffer_store_b128(v, rs, voff + soff, 0, AUX_SC1); }
DI f32x2 ldst(rsrc_t rs, int voff, int soff) { return __builtin_bit_cast(f32x2, __builtin_amdgcn_raw_buffer_load_b64(rs, voff, soff, AUX_SC1)); }
DI void stst(rsrc_t rs, int voff, int soff, f32x2 v) { __builtin_amdgcn_raw_buffer_store_b64(__builtin_bit_cast(u32x2, v), rs, voff, soff, AUX_SC1); }

#define FSTRIDE 32
DI gu32* flagp(const Params& p, int ph, int g, int r) { return (gu32*)(p.ws + FLAG_OFF) + (ph * 16 + g) * 16 * 32 + r * FSTRIDE; }
DI void set_flag(const Params& p, int ph, int g, int r, unsigned tag) { __hip_atomic_store(flagp(p, ph, g, r), tag, __ATOMIC_RELAXED, __HIP_MEMORY_SCOPE_AGENT); }
DI void wait_word(const Params& p, gu32* a, unsigned tag) {
#pragma nounroll
    for (unsigned spins = 0;; ++spins) {
        const unsigned v = __hip_atomic_load(a, __ATOMIC_RELAXED, __HIP_MEMORY_SCOPE_AGENT);
        if (__all(v == tag)) break;
        if (spins > (1u << 20)) { __hip_atomic_store((gu32*)(p.ws + TMO_OFF), 1u, __ATOMIC_RELAXED, __HIP_MEMORY_SCOPE_AGENT); break; }
        __builtin_amdgcn_s_sleep(1);
    }
    __builtin_amdgcn_fence(__ATOMIC_ACQUIRE, "wavefront");
}
DI void wait_group(const Params& p, int ph, int g, unsigned tag, int lane) { wait_word(p, flagp(p, ph, g, lane & 15), tag); }

DI void stats_store(rsrc_t rs, int buf, int T, int u, int lane, const unsigned (&c)[8]) {
    float s = 0.f, s1 = 0.f, ss = 0.f, ss1 = 0.f;
#pragma unroll
    for (int i = 0; i < 8; i += 2) {
        unsigned w0 = c[i], w1 = c[i + 1];
        asm("v_dot2c_f32_bf16 %0, %4, %6\n\tv_dot2c_f32_bf16 %1, %5, %6\n\tv_dot2c_f32_bf16 %2, %4, %4\n\tv_dot2c_f32_bf16 %3, %5, %5"
            : "+v"(s), "+v"(s1), "+v"(ss), "+v"(ss1) : "v"(w0), "v"(w1), "v"(0x3f803f80u));
    }
    asm volatile("s_nop 2" : "+v"(s), "+v"(s1), "+v"(ss), "+v"(ss1));
    s = sum_xor32(s + s1); ss = sum_xor32(ss + ss1);
    if (lane < 32) stst(rs, lane * 8, ST_OFF + buf * ST_BUF + (T * 16 + u) * 256, f32x2{s, ss});
}

DI void stage_lin(const char* wsb, unsigned lds0, int src, int dst, int n, int hw, int lane, int nw = 4) {
    const unsigned vo = lane * 16;
    for (int i = hw; i < n; i += nw) glds16s<true>(wsb + src + i * 1024, vo, lds0 + dst + i * 1024);
}

#define HELPER_VARS() \
    int hw = w - 4; asm volatile("" : "+s"(hw)); \
    const char* wsb = p.ws; asm volatile("" : "+s"(wsb)); \
    unsigned lds0 = lds0_; asm volatile("" : "+s"(lds0)); (void)hw; (void)wsb; (void)lds0;
#define PHASE_VARS() \
    int lane = lane_id(); asm volatile("" : "+v"(lane)); \
    int lyr = layer; asm volatile("" : "+s"(lyr)); \
    int g = g_, r = r_; asm volatile("" : "+s"(g), "+s"(r)); \
    const int h = lane >> 5, tok = lane & 31; const int m = r & 1, n = r >> 1; (void)h; (void)tok; (void)m; (void)n; (void)lyr;

DI void attn_finish(rsrc_t rs, const f32x16& o, int hd, int T, int r, int lyr, int lane) {
    float ov[8]; float l;
    if (hd == 0) { l = o[8];
#pragma unroll
        for (int i = 0; i < 8; ++i) ov[i] = o[i];
    } else { l = o[0];
#pragma unroll
        for (int i = 0; i < 8; ++i) ov[i] = o[8 + i];
    }
    const float inv = __builtin_amdgcn_rcpf(l);
    stw(rs, lane * 16, O_OFF + lyr * 2 * MB + (T * 32 + 2 * r + hd) * 1024,
        u32x4{pk(ov[0] * inv, ov[1] * inv), pk(ov[2] * inv, ov[3] * inv), pk(ov[4] * inv, ov[5] * inv), pk(ov[6] * inv, ov[7] * inv)});
}
DI float max16(float m, const f32x16& s) {
#pragma unroll
    for (int i = 0; i < 16; i += 2) m = fmaxf(fmaxf(m, s[i]), s[i + 1]);
    return m;
}
DI void max16x2(float& m0, float& m1, const f32x16& s) {
#pragma unroll
    for (int i = 0; i < 16; i += 4) { m0 = fmaxf(fmaxf(m0, s[i]), s[i + 1]); m1 = fmaxf(fmaxf(m1, s[i + 2]), s[i + 3]); }
}
DI void exp_pack(const f32x16& s, u32x4& p0, u32x4& p1) {
    unsigned pw[8];
#pragma unroll
    for (int i = 0; i < 8; ++i) pw[i] = pk(__builtin_amdgcn_exp2f(s[2 * i]), __builtin_amdgcn_exp2f(s[2 * i + 1]));
    p0 = u32x4{pw[0], pw[1], pw[2], pw[3]}; p1 = u32x4{pw[4], pw[5], pw[6], pw[7]};
}
DI float wave32_max(float v) {
    int x = __float_as_int(v);
    x = max(x, __builtin_amdgcn_update_dpp(x, x, 0x111, 0xf, 0xf, false));
    x = max(x, __builtin_amdgcn_update_dpp(x, x, 0x112, 0xf, 0xf, false));
    x = max(x, __builtin_amdgcn_update_dpp(x, x, 0x114, 0xf, 0xf, false));
    x = max(x, __builtin_amdgcn_update_dpp(x, x, 0x118, 0xf, 0xf, false));
    x = max(x, __builtin_amdgcn_update_dpp(x, x, 0x142, 0xa, 0xf, false));
    return __int_as_float(__builtin_amdgcn_readlane(x, 31));
}
DI void attn_seg(unsigned ka, unsigned va, int ks, int vs, int n, const bf16x8 qf, const f32x16 c0, f32x16& o) {
    int d;
    asm volatile(
        "ds_read_b128 v[248:251], %[ka]\n\tv_add_u32 %[ka], %[ks], %[ka]\n\tds_read_b128 v[236:239], %[ka]\n\tv_add_u32 %[ka], %[ks], %[ka]\n\t"
        "ds_read_b128 v[240:243], %[va]\n\tds_read_b128 v[244:247], %[va] offset:1024\n\tv_add_u32 %[va], %[vs], %[va]\n\t"
        "s_lshr_b32 %[d], %[n], 1\n\ts_waitcnt lgkmcnt(3)\n\t"
        "v_mfma_f32_32x32x16_bf16 v[204:219], v[248:251], %[q], %[c0]\n\t"
        "s_cmp_eq_u32 %[d], 0\n\ts_nop 10\n\ts_cbranch_scc1 Ltail_%=\n"
        "Lloop_%=:\n\t"
        "v_exp_f32 v204, v204\n\t"
        "v_exp_f32 v205, v205\n\t"
        "v_exp_f32 v206, v206\n\t"
        "v_exp_f32 v207, v207\n\t"
        "v_exp_f32 v208, v208\n\t"
        "v_exp_f32 v209, v209\n\t"
        "v_exp_f32 v210, v210\n\t"
        "v_exp_f32 v211, v211\n\t"
        "v_cvt_pk_bf16_f32 v204, v204, v205\n\t"
        "v_cvt_pk_bf16_f32 v205, v206, v207\n\t"
        "v_cvt_pk_bf16_f32 v206, v208, v209\n\t"
        "v_cvt_pk_bf16_f32 v207, v210, v211\n\t"
        "s_waitcnt lgkmcnt(1)\n\ts_nop 0\n\t"
        "v_mfma_f32_32x32x16_bf16 %[o], v[240:243], v[204:207], %[o]\n\t"
        "v_exp_f32 v212, v212\n\t"
        "v_exp_f32 v213, v213\n\t"
        "v_exp_f32 v214, v214\n\t"
        "v_exp_f32 v215, v215\n\t"
        "v_mfma_f32_32x32x16_bf16 v[220:235], v[236:239], %[q], 0\n\t"
        "ds_read_b128 v[236:239], %[ka]\n\tds_read_b128 v[248:251], %[va]\n\tds_read_b128 v[252:255], %[va] offset:1024\n\t"
        "v_add_u32 %[ka], %[ks], %[ka]\n\tv_add_u32 %[va], %[vs], %[va]\n\t"
        "v_exp_f32 v216, v216\n\t"
        "v_exp_f32 v217, v217\n\t"
        "v_exp_f32 v218, v218\n\t"
        "v_exp_f32 v219, v219\n\t"
        "v_cvt_pk_bf16_f32 v212, v212, v213\n\t"
        "v_cvt_pk_bf16_f32 v213, v214, v215\n\t"
        "v_cvt_pk_bf16_f32 v214, v216, v217\n\t"
        "v_cvt_pk_bf16_f32 v215, v218, v219\n\t"
        "s_waitcnt lgkmcnt(3)\n\ts_nop 0\n\t"
        "v_mfma_f32_32x32x16_bf16 %[o], v[244:247], v[212:215], %[o]\n\t"
        "v_exp_f32 v220, v220\n\t"
        "v_exp_f32 v221, v221\n\t"
        "v_exp_f32 v222, v222\n\t"
        "v_exp_f32 v223, v223\n\t"
        "v_exp_f32 v224, v224\n\t"
        "v_exp_f32 v225, v225\n\t"
        "v_exp_f32 v226, v226\n\t"
        "v_exp_f32 v227, v227\n\t"
        "v_cvt_pk_bf16_f32 v220, v220, v221\n\t"
        "v_cvt_pk_bf16_f32 v221, v222, v223\n\t"
        "v_cvt_pk_bf16_f32 v222, v224, v225\n\t"
        "v_cvt_pk_bf16_f32 v223, v226, v227\n\t"
        "s_waitcnt lgkmcnt(1)\n\ts_nop 0\n\t"
        "v_mfma_f32_32x32x16_bf16 %[o], v[248:251], v[220:223], %[o]\n\t"
        "v_exp_f32 v228, v228\n\t"
        "v_exp_f32 v229, v229\n\t"
        "v_exp_f32 v230, v230\n\t"
        "v_exp_f32 v231, v231\n\t"
        "v_mfma_f32_32x32x16_bf16 v[204:219], v[236:239], %[q], 0\n\t"
        "ds_read_b128 v[236:239], %[ka]\n\tds_read_b128 v[240:243], %[va]\n\tds_read_b128 v[244:247], %[va] offset:1024\n\t"
        "v_add_u32 %[ka], %[ks], %[ka]\n\tv_add_u32 %[va], %[vs], %[va]\n\t"
        "v_exp_f32 v232, v232\n\t"
        "v_exp_f32 v233, v233\n\t"
        "v_exp_f32 v234, v234\n\t"
        "v_exp_f32 v235, v235\n\t"
        "v_cvt_pk_bf16_f32 v228, v228, v229\n\t"
        "v_cvt_pk_bf16_f32 v229, v230, v231\n\t"
        "v_cvt_pk_bf16_f32 v230, v232, v233\n\t"
        "v_cvt_pk_bf16_f32 v231, v234, v235\n\t"
        "s_waitcnt lgkmcnt(3)\n\ts_nop 0\n\t"
        "v_mfma_f32_32x32x16_bf16 %[o], v[252:255], v[228:231], %[o]\n\t"
        "s_sub_u32 %[d], %[d], 1\n\ts_cmp_lg_u32 %[d], 0\n\ts_cbranch_scc1 Lloop_%=\n"
        "Ltail_%=:\n\t"
        "s_bitcmp1_b32 %[n], 0\n\ts_waitcnt lgkmcnt(0)\n\ts_cbranch_scc0 Lend_%=\n\t"
        "v_exp_f32 v204, v204\n\t"
        "v_exp_f32 v205, v205\n\t"
        "v_exp_f32 v206, v206\n\t"
        "v_exp_f32 v207, v207\n\t"
        "v_exp_f32 v208, v208\n\t"
        "v_exp_f32 v209, v209\n\t"
        "v_exp_f32 v210, v210\n\t"
        "v_exp_f32 v211, v211\n\t"
        "v_cvt_pk_bf16_f32 v204, v204, v205\n\t"
        "v_cvt_pk_bf16_f32 v205, v206, v207\n\t"
        "v_cvt_pk_bf16_f32 v206, v208, v209\n\t"
        "v_cvt_pk_bf16_f32 v207, v210, v211\n\t"
        "s_nop 1\n\t"
        "v_mfma_f32_32x32x16_bf16 %[o], v[240:243], v[204:207], %[o]\n\t"
        "v_exp_f32 v212, v212\n\t"
        "v_exp_f32 v213, v213\n\t"
        "v_exp_f32 v214, v214\n\t"
        "v_exp_f32 v215, v215\n\t"
        "v_exp_f32 v216, v216\n\t"
        "v_exp_f32 v217, v217\n\t"
        "v_exp_f32 v218, v218\n\t"
        "v_exp_f32 v219, v219\n\t"
        "v_cvt_pk_bf16_f32 v212, v212, v213\n\t"
        "v_cvt_pk_bf16_f32 v213, v214, v215\n\t"
        "v_cvt_pk_bf16_f32 v214, v216, v217\n\t"
        "v_cvt_pk_bf16_f32 v215, v218, v219\n\t"
        "s_nop 1\n\t"
        "v_mfma_f32_32x32x16_bf16 %[o], v[244:247], v[212:215], %[o]\n"
        "Lend_%=:\n\ts_nop 11"
        : [o] "+v"(o), [ka] "+v"(ka), [va] "+v"(va), [d] "=&s"(d)
        : [q] "v"(qf), [c0] "v"(c0), [ks] "s"(ks), [vs] "v"(vs), [n] "s"(n)
        : "memory", "scc", "v204", "v205", "v206", "v207", "v208", "v209", "v210", "v211", "v212", "v213", "v214", "v215", "v216", "v217", "v218", "v219", "v220", "v221", "v222", "v223", "v224", "v225", "v226", "v227", "v228", "v229", "v230", "v231", "v232", "v233", "v234", "v235", "v236", "v237", "v238", "v239", "v240", "v241", "v242", "v243", "v244", "v245", "v246", "v247", "v248", "v249", "v250", "v251", "v252", "v253", "v254", "v255");
}
constexpr float BOUND2 = 4096.f;
__device__ __attribute__((noinline)) f32x16 attn_exact(LAS char* lds, int tw, int hd, int s0, bool first_masked, const bf16x8 qf, int lane) {
    const int h = lane >> 5, tok = lane & 31;
    const LAS char* kb = lds + L_KV + hd * 1024 + lane * 16;
    const bool isdata = ((lane >> 4) & 1) == hd;
    const LAS char* vb = isdata ? (lds + L_KV + 2048 + lane * 16) : (lds + L_ONES);
    const int vstep = isdata ? 4096 : 0;
    const f32x16 zero = {};
    f32x16 o = {};
    float mx = -INFINITY;
    for (int pass = 0; pass < 2; ++pass) {
        for (int sl = s0; sl <= 8 + tw; ++sl) {
            f32x16 s = mfma32(*(const LAS bf16x8*)(kb + sl * 4096), qf, zero);
            const bool diag = sl == 8 + tw, first = first_masked && sl == s0;
#pragma unroll
            for (int i = 0; i < 16; ++i) { const int kk = (i & 3) + 8 * (i >> 2) + 4 * h; if ((diag && kk > tok) || (first && kk <= tok)) s[i] = -INFINITY; }
            if (pass == 0) mx = max16(mx, s);
            else {
#pragma unroll
                for (int i = 0; i < 16; ++i) s[i] -= mx;
                u32x4 p0, p1; exp_pack(s, p0, p1);
                o = mfma32(*(const LAS bf16x8*)(vb + sl * vstep), asfrag(p0), o);
                o = mfma32(*(const LAS bf16x8*)(vb + sl * vstep + 1024), asfrag(p1), o);
            }
        }
        if (pass == 0) mx = max_xor32(mx);
    }
    return o;
}
DI void attn_phase(const Params& p, LAS char* lds, rsrc_t rs, int w, int g, int r, int lyr, int lane, unsigned tag TS_PARAM) {
    const int h = lane >> 5, tok = lane & 31;
    const int hd = w >> 2, tw = hd ? 3 - (w & 3) : (w & 3), T = 4 * g + tw;
    const bf16x8 qf = *(const LAS bf16x8*)(lds + L_Q + tw * 2048 + hd * 1024 + lane * 16);
    const unsigned kb = (unsigned)(uintptr_t)(lds + L_KV + hd * 1024 + lane * 16);
    const bool isdata = ((lane >> 4) & 1) == hd;
    const unsigned vb = (unsigned)(uintptr_t)(isdata ? (lds + L_KV + 2048 + lane * 16) : (lds + L_ONES));
    const int vstep = isdata ? 4096 : 0;
    const LAS float* kn = (const LAS float*)(lds + L_KN);
    const float qn = ((const LAS float*)(lds + L_QN))[tw * 2 + hd] * 1.05f;
    const float kown = fmaxf(fmaxf(kn[16 + hd], kn[18 + hd]), fmaxf(kn[20 + hd], kn[22 + hd]));
    bool fast = __builtin_amdgcn_readfirstlane(qn * kown <= BOUND2);
    f32x16 t, o = {};
#pragma unroll
    for (int i = 0; i < 16; ++i) t[i] = __builtin_fmaf((float)(tok - 4 * h), 1e30f, (0.5f - (float)((i & 3) + 8 * (i >> 2))) * 1e30f);
    const int s4 = 4 * hd, tile0 = 4 * g - 8 + s4;
    const bool loader = tw == 0 && tile0 >= 0;
    u32x4 hv[16]; f32x2 hn;
    if (loader) {
        wait_word(p, flagp(p, 1 + 5 * lyr, g - 2 + hd, r), tag);
        TS(tb + 1);
        const int kvo = KV_OFF + lyr * 4 * MB + (tile0 * 16 + r) * 4096;
#pragma unroll
        for (int i = 0; i < 16; ++i) hv[i] = ldw(rs, lane * 16 + (i & 3) * 1024, kvo + (i >> 2) * 65536);
        hn = ldst(rs, (lane & 3) * 128, KN_OFF + ((lyr * 64 + tile0) * 16 + r) * 8);
        SB();
    }
    if (fast) {
        f32x16 m;
#pragma unroll
        for (int i = 0; i < 16; ++i) m[i] = fminf(t[i], 0.f);
        attn_seg(kb + (8 + tw) * 4096, vb + (8 + tw) * vstep, -4096, -vstep, tw + 1, qf, m, o);
    }
    TS(tb);
    if (loader) {
        LAS char* kvl = lds + L_KV + s4 * 4096 + lane * 16;
#pragma unroll
        for (int i = 0; i < 16; ++i) *(LAS u32x4*)(kvl + i * 1024) = hv[i];
        if (lane < 4) *(LAS f32x2*)(lds + L_KN + (s4 + lane) * 8) = hn;
    }
    BAR();
    TS(tb + 2);
    const int s0 = T >= 8 ? tw : 8 - 4 * g;
    if (s0 < 8) {
        float kh = 0.f;
#pragma unroll
        for (int j = 0; j < 8; ++j) kh = fmaxf(kh, j >= 8 - 4 * g ? kn[2 * j + hd] : 0.f);
        fast = fast && __builtin_amdgcn_readfirstlane(qn * kh <= BOUND2);
        if (fast) {
            f32x16 m;
#pragma unroll
            for (int i = 0; i < 16; ++i) m[i] = T >= 8 ? fminf(-t[i], 0.f) : 0.f;
            attn_seg(kb + s0 * 4096, vb + s0 * vstep, 4096, vstep, 8 - s0, qf, m, o);
        }
    }
    if (!fast) o = attn_exact(lds, tw, hd, s0, T >= 8, qf, lane);
    attn_finish(rs, o, hd, T, r, lyr, lane);
}
DI void ln_stats(const f32x2 (&sv)[8], float& mean, float& rstd) {
    float S = 0.f, SS = 0.f;
#pragma unroll
    for (int i = 0; i < 8; ++i) { S += sv[i].x; SS += sv[i].y; }
    S = sum_xor32(S); SS = sum_xor32(SS);
    mean = S * (1.f / DM); const float var = fmaxf(SS * (1.f / DM) - mean * mean, 0.f); rstd = __builtin_amdgcn_rsqf(var + EPS);
}
DI unsigned read_tag(const Params& p) {
    return (unsigned)__builtin_amdgcn_readfirstlane(__hip_atomic_load((gu32*)(p.ws + EPOCH_OFF), __ATOMIC_RELAXED, __HIP_MEMORY_SCOPE_AGENT)) + 1u;
}
#define LDA(arr, s, base) arr[s] = ldw(rs, lane * 16 + ((s) & 3) * 1024, (base) + ((s) >> 2) * 4096)

#define WODELAY 36
#define PD0 4
#define PDV 12
constexpr int a_issued(int s, int total) { int v = PD0 + 2 * (s + 1); const int cap = s + 1 + PDV; v = v < cap ? v : cap; return v < total ? v : total; }

DI void main_path(const Params& p, LAS char* lds, rsrc_t rs, const int w, const int g_, const int r_) {
    unsigned tag = 0;
    TS_DECL();
    TS(0);
    {
        const int layer = 0; PHASE_VARS();
        const int T = 4 * g + (r & 3), u = (r >> 2) * 4 + w;
        const uint16_t* row = p.x + (size_t)(T * 32 + tok) * DM + 32 * u + 4 * h;
        u32x2 a0 = *(const GAS u32x2*)(row), a1 = *(const GAS u32x2*)(row + 8), a2 = *(const GAS u32x2*)(row + 16), a3 = *(const GAS u32x2*)(row + 24);
        tag = read_tag(p); asm volatile("" :: "s"(tag) : "memory");
        stw(rs, lane * 16, XF_OFF + (T * 32 + 2 * u) * 1024, u32x4{a0.x, a0.y, a1.x, a1.y});
        stw(rs, lane * 16, XF_OFF + (T * 32 + 2 * u + 1) * 1024, u32x4{a2.x, a2.y, a3.x, a3.y});
        const unsigned c[8] = {a0.x, a0.y, a1.x, a1.y, a2.x, a2.y, a3.x, a3.y};
        stats_store(rs, 0, T, u, lane, c);
        DRAIN();
        BAR();
        if (w == 0 && lane == 0) { set_flag(p, 0, g, r, tag); __hip_atomic_store((gu32*)(p.ws + ACK_OFF) + blockIdx.x * 32, tag, __ATOMIC_RELAXED, __HIP_MEMORY_SCOPE_AGENT); }
    }
    TS(1);
    for (int layer = 0; layer < 2; ++layer) {
        const int tb = 2 + layer * 24; (void)tb;
        {
            PHASE_VARS();
            if (w == 0) {
                if (lyr == 0) {
                    const int j = lane & 15;
                    wait_word(p, (lane & 16) ? flagp(p, 0, j, r) : flagp(p, 0, g, j), tag);
                } else wait_group(p, 5, g, tag, lane);
            }
            BAR();
            TS(tb + 0);
            const int T = 4 * g + w;
            const LAS char* wb = lds + L_WQ + lane * 16;
            LAS float* csq = (LAS float*)(lds + L_CSQ);
            LAS float* stt = (LAS float*)(lds + L_STT);
            f32x16 aq = {}, ak = {}, av = {};
            u32x4 xa[32];
            f32x2 sv[8];
            const int sto = ST_OFF + 2 * lyr * ST_BUF + T * 4096, xo = XF_OFF + lyr * 2 * MB + T * 32768;
            if (lyr == 0) {
                const char* src = p.ws + WQT_OFF + r * 98304;
#pragma unroll
                for (int i = 0; i < 6; ++i) glds16s<true>(src + (4 * i + w) * 1024, lane * 16, (unsigned)(uintptr_t)lds + L_WQ + (4 * i + w) * 1024);
            }
#pragma unroll
            for (int s = 0; s < PD0; ++s) { LDA(xa, s, xo); SB(); }
            if (lyr == 0) { asm volatile("s_waitcnt vmcnt(%0)" :: "i"(PD0) : "memory"); BAR(); }
            bf16x8 wf[3][3];
#pragma unroll
            for (int s = 0; s < 2; ++s)
#pragma unroll
                for (int t = 0; t < 3; ++t) wf[s][t] = ldsfrag(wb + (s * 3 + t) * 1024);
#pragma unroll
            for (int s = 0; s < 32; ++s) {
                if ((s == 6 || s == 14 || s == 22) && lyr == 0) BAR();
#pragma unroll
                for (int j = (s == 0 ? PD0 : a_issued(s - 1, 32)); j < a_issued(s, 32); ++j) LDA(xa, j, xo);
                if (s >= 16 && s < 24) sv[s - 16] = ldst(rs, (8 * h * 32 + tok) * 8 + (s - 16) * 256, sto);
                if (s + 2 < 32) {
#pragma unroll
                    for (int t = 0; t < 3; ++t) wf[(s + 2) % 3][t] = ldsfrag(wb + ((s + 2) * 3 + t) * 1024);
                }
                const bf16x8 x = asfrag(xa[s]);
                aq = mfma32(wf[s % 3][0], x, aq); ak = mfma32(wf[s % 3][1], x, ak); av = mfma32(x, wf[s % 3][2], av);
                SGB(0x100, 1); SGB(0x8, 1); SGB(0x20, 1); SGB(0x100, 1); SGB(0x8, 1); SGB(0x20, 1); SGB(0x100, 1); SGB(0x8, 1);
                SB();
            }
            float mean, rstd; ln_stats(sv, mean, rstd);
            if (h == 0) { stt[w * 64 + tok] = rstd; stt[w * 64 + 32 + tok] = mean * rstd; }
            TS(tb + 1);
            BAR();
            TS(tb + 2);
            const float mr = mean * rstd;
            float q[16], k[16], v[16];
            const float cv = csq[64 + tok];
#pragma unroll
            for (int g4 = 0; g4 < 4; ++g4) {
                f32x4 cq = *(LAS f32x4*)(csq + 8 * g4 + 4 * h), ck = *(LAS f32x4*)(csq + 32 + 8 * g4 + 4 * h);
                f32x4 rsv = *(LAS f32x4*)(stt + w * 64 + 8 * g4 + 4 * h), ms = *(LAS f32x4*)(stt + w * 64 + 32 + 8 * g4 + 4 * h);
#pragma unroll
                for (int i = 0; i < 4; ++i) {
                    k[4 * g4 + i] = ak[4 * g4 + i] * rstd - mr * ck[i];
                    v[4 * g4 + i] = av[4 * g4 + i] * rsv[i] - ms[i] * cv;
                    q[4 * g4 + i] = (aq[4 * g4 + i] * rstd - mr * cq[i]) * QSCALE;
                }
            }
            const u32x4 k0 = {pk(k[0], k[1]), pk(k[2], k[3]), pk(k[4], k[5]), pk(k[6], k[7])};
            const u32x4 k1 = {pk(k[8], k[9]), pk(k[10], k[11]), pk(k[12], k[13]), pk(k[14], k[15])};
            const u32x4 v0 = {pk(v[0], v[1]), pk(v[2], v[3]), pk(v[4], v[5]), pk(v[6], v[7])};
            const u32x4 v1 = {pk(v[8], v[9]), pk(v[10], v[11]), pk(v[12], v[13]), pk(v[14], v[15])};
            const int kvo = KV_OFF + lyr * 4 * MB + (T * 16 + r) * 4096;
            stw(rs, lane * 16, kvo, k0); stw(rs, lane * 16 + 1024, kvo, k1); stw(rs, lane * 16 + 2048, kvo, v0); stw(rs, lane * 16 + 3072, kvo, v1);
            {
                float na = 0.f, nb = 0.f;
#pragma unroll
                for (int i = 0; i < 8; ++i) { na = __builtin_fmaf(k[i], k[i], na); nb = __builtin_fmaf(k[8 + i], k[8 + i], nb); }
                const f32x2 nn = {wave32_max(sum_xor32(na)), wave32_max(sum_xor32(nb))};
                if (lane == 0) { stst(rs, 0, KN_OFF + ((lyr * 64 + T) * 16 + r) * 8, nn); *(LAS f32x2*)(lds + L_KN + (8 + w) * 8) = nn; }
            }
            LAS char* kvl = lds + L_KV + (8 + w) * 4096 + lane * 16;
            *(LAS u32x4*)(kvl) = k0; *(LAS u32x4*)(kvl + 1024) = k1; *(LAS u32x4*)(kvl + 2048) = v0; *(LAS u32x4*)(kvl + 3072) = v1;
            LAS char* ql = lds + L_Q + w * 2048 + lane * 16;
            *(LAS u32x4*)(ql) = u32x4{pk(q[0], q[1]), pk(q[2], q[3]), pk(q[4], q[5]), pk(q[6], q[7])};
            *(LAS u32x4*)(ql + 1024) = u32x4{pk(q[8], q[9]), pk(q[10], q[11]), pk(q[12], q[13]), pk(q[14], q[15])};
            {
                float na = 0.f, nb = 0.f;
#pragma unroll
                for (int i = 0; i < 8; ++i) { na = __builtin_fmaf(q[i], q[i], na); nb = __builtin_fmaf(q[8 + i], q[8 + i], nb); }
                const f32x2 nn = {wave32_max(sum_xor32(na)), wave32_max(sum_xor32(nb))};
                if (lane == 0) *(LAS f32x2*)(lds + L_QN + w * 8) = nn;
            }
            DRAIN();
            BAR();
            if (w == 0 && lane == 0) set_flag(p, 1 + 5 * lyr, g, r, tag);
            TS(tb + 3);
        }
        {
            PHASE_VARS();
            attn_phase(p, lds, rs, w, g, r, lyr, lane, tag TS_ARG(layer == 0 ? 22 : 48));
            TS(tb + 4); TS(tb + 5); TS(tb + 6);
            DRAIN();
            BAR();
            if (w == 0 && lane == 0) set_flag(p, 2 + 5 * lyr, g, r, tag);
            TS(tb + 7);
        }
        {
            PHASE_VARS();
            const int mt = w & 1, kh = (w >> 1) & 1, T = 4 * g + 2 * m + mt, u = 2 * n + kh;
            const int xo = XF_OFF + lyr * 2 * MB + (T * 32 + 2 * u) * 1024;
            const u32x4 r0 = ldw(rs, lane * 16, xo), r1 = ldw(rs, lane * 16 + 1024, xo);
            if (w == 0) wait_group(p, 2 + 5 * lyr, g, tag, lane);
            BAR();
            TS(tb + 8);
            f32x16 acc[2] = {};
            u32x4 xa[16];
            const int oo = O_OFF + lyr * 2 * MB + (T * 32 + 16 * kh) * 1024;
#pragma unroll
            for (int s = 0; s < 16; ++s) { LDA(xa, s, oo); SB(); }
            const LAS char* wb = lds + 96 * KB + kh * 32768 + lane * 16;
            const LAS char* wlast = lds + (kh ? 94 * KB : 96 * KB + 15 * 2048) + lane * 16;
            bf16x8 wf[4][2];
            const int fo = kh * 1024, fs = 1024 - fo;
#pragma unroll
            for (int s = 0; s < 3; ++s) { wf[s][0] = ldsfrag(wb + fo + s * 2048); wf[s][1] = ldsfrag(wb + fs + s * 2048); }
#pragma unroll
            for (int s = 0; s < 16; ++s) {
                if (s + 3 < 15) { wf[(s + 3) & 3][0] = ldsfrag(wb + fo + (s + 3) * 2048); wf[(s + 3) & 3][1] = ldsfrag(wb + fs + (s + 3) * 2048); }
                if (s + 3 == 15) { wf[3][0] = ldsfrag(wlast + fo); wf[3][1] = ldsfrag(wlast + fs); }
                const bf16x8 x = asfrag(xa[s]);
                acc[0] = mfma32(wf[s & 3][0], x, acc[0]); acc[1] = mfma32(wf[s & 3][1], x, acc[1]);
                SGB(0x100, 1); SGB(0x8, 1); SGB(0x20, 2); SGB(0x100, 1); SGB(0x8, 1);
                SB();
            }
            const f32x16 snd = acc[1];
#pragma unroll
            for (int g4 = 0; g4 < 4; ++g4) *(LAS f32x4*)(lds + L_RED2 + ((w * 4 + g4) * 64 + lane) * 16) = f32x4{snd[4 * g4], snd[4 * g4 + 1], snd[4 * g4 + 2], snd[4 * g4 + 3]};
            TS(tb + 9);
            BAR();
            const f32x16 own = acc[0];
            const int pw = w ^ 2;
            float v[16];
#pragma unroll
            for (int g4 = 0; g4 < 4; ++g4) { f32x4 t = *(LAS f32x4*)(lds + L_RED2 + ((pw * 4 + g4) * 64 + lane) * 16);
#pragma unroll
                for (int i = 0; i < 4; ++i) v[4 * g4 + i] = own[4 * g4 + i] + t[i]; }
            const unsigned rr[8] = {r0.x, r0.y, r0.z, r0.w, r1.x, r1.y, r1.z, r1.w};
            unsigned c[8];
#pragma unroll
            for (int i = 0; i < 8; ++i) c[i] = pk(v[2 * i] + bflo(rr[i]), v[2 * i + 1] + bfhi(rr[i]));
            const int yo = X2_OFF + lyr * 2 * MB + (T * 32 + 2 * u) * 1024;
            stw(rs, lane * 16, yo, u32x4{c[0], c[1], c[2], c[3]});
            stw(rs, lane * 16 + 1024, yo, u32x4{c[4], c[5], c[6], c[7]});
            stats_store(rs, 2 * lyr + 1, T, u, lane, c);
            DRAIN();
            BAR();
            if (w == 0 && lane == 0) set_flag(p, 3 + 5 * lyr, g, r, tag);
            TS(tb + 10);
        }
        {
            PHASE_VARS();
            if (w == 0) wait_group(p, 3 + 5 * lyr, g, tag, lane);
            BAR();
            TS(tb + 11);
            const int T = 4 * g + w;
            const LAS char* wbA = lds + lane * 16;
            const LAS char* wbB = lds + 3072 + (h * 16 + (lane & 15)) * 16;
            LAS float* cs1 = (LAS float*)(lds + L_CS1);
            f32x16 acc[4] = {};
            u32x4 xa[32];
            f32x2 sv[8];
            const int sto = ST_OFF + (2 * lyr + 1) * ST_BUF + T * 4096, xo = X2_OFF + lyr * 2 * MB + T * 32768;
#pragma unroll
            for (int s = 0; s < PD0; ++s) { LDA(xa, s, xo); SB(); }
            auto ldW = [&](int s, int t) { return t < 3 ? ldsfrag(wbA + w1_off(s) + t * 1024) : ldsfrag(wbB + w1_off(s)); };
            bf16x8 wf[2][4];
#pragma unroll
            for (int t = 0; t < 4; ++t) wf[0][t] = ldW(0, t);
#pragma unroll
            for (int s = 0; s < 32; ++s) {
                if (s == 20) BAR();
#pragma unroll
                for (int j = (s == 0 ? PD0 : a_issued(s - 1, 32)); j < a_issued(s, 32); ++j) LDA(xa, j, xo);
                if (s >= 16 && s < 24) sv[s - 16] = ldst(rs, (8 * h * 32 + tok) * 8 + (s - 16) * 256, sto);
                const bf16x8 x = asfrag(xa[s]);
#pragma unroll
                for (int t = 0; t < 4; ++t) {
                    if (s + 1 < 32) wf[(s + 1) & 1][t] = ldW(s + 1, t);
                    acc[t] = mfma32(wf[s & 1][t], x, acc[t]);
                }
                SGB(0x100, 1); SGB(0x8, 1); SGB(0x20, 1); SGB(0x100, 1); SGB(0x8, 1); SGB(0x20, 1); SGB(0x100, 1); SGB(0x8, 1); SGB(0x100, 1); SGB(0x8, 1);
                SB();
            }
            float mean, rstd; ln_stats(sv, mean, rstd);
            const float mr = mean * rstd;
            TS(tb + 12);
            BAR();
            TS(tb + 13);
            const int ho = H_OFF + lyr * 8 * MB + (T * 112 + 7 * r) * 1024;
            const f32x2 nm2 = {-mean, -mean}, rs2 = {rstd, rstd}, ry2 = {-LOG2E * rstd, -LOG2E * rstd}, one2 = {1.f, 1.f};
#pragma unroll
            for (int t = 0; t < 4; ++t) {
                constexpr int dummy = 0; (void)dummy;
                const int np = t < 3 ? 8 : 4;
                f32x2 tt[8], z[8], e[8];
#pragma unroll
                for (int g4 = 0; g4 < 4; ++g4) {
                    if (2 * g4 < np) {
                        const f32x4 c4 = *(LAS f32x4*)(cs1 + t * 32 + 8 * g4 + 4 * h);
                        tt[2 * g4] = __builtin_elementwise_fma(nm2, f32x2{c4[0], c4[1]}, f32x2{acc[t][4 * g4], acc[t][4 * g4 + 1]});
                        tt[2 * g4 + 1] = __builtin_elementwise_fma(nm2, f32x2{c4[2], c4[3]}, f32x2{acc[t][4 * g4 + 2], acc[t][4 * g4 + 3]});
                    }
                }
#pragma unroll
                for (int i = 0; i < 8; ++i) if (i < np) e[i] = tt[i] * ry2;
                SB();
#pragma unroll
                for (int i = 0; i < 8; ++i) if (i < np) e[i] = f32x2{__builtin_amdgcn_exp2f(e[i].x), __builtin_amdgcn_exp2f(e[i].y)};
#pragma unroll
                for (int i = 0; i < 8; ++i) if (i < np) z[i] = tt[i] * rs2;
                SB();
#pragma unroll
                for (int i = 0; i < 8; ++i) if (i < np) e[i] = e[i] + one2;
                SB();
#pragma unroll
                for (int i = 0; i < 8; ++i) if (i < np) e[i] = f32x2{__builtin_amdgcn_rcpf(e[i].x), __builtin_amdgcn_rcpf(e[i].y)};
                SB();
#pragma unroll
                for (int i = 0; i < 8; ++i) if (i < np) z[i] = z[i] * e[i];
                stw(rs, lane * 16 + (2 * t) * 1024, ho, u32x4{pk(z[0].x, z[0].y), pk(z[1].x, z[1].y), pk(z[2].x, z[2].y), pk(z[3].x, z[3].y)});
                if (t < 3) stw(rs, lane * 16 + (2 * t + 1) * 1024, ho, u32x4{pk(z[4].x, z[4].y), pk(z[5].x, z[5].y), pk(z[6].x, z[6].y), pk(z[7].x, z[7].y)});
                SB();
            }
            DRAIN();
            BAR();
            if (w == 0 && lane == 0) set_flag(p, 4 + 5 * lyr, g, r, tag);
            TS(tb + 14);
        }
        {
            PHASE_VARS();
            const int mt = w & 1, kh = (w >> 1) & 1, T = 4 * g + 2 * m + mt, u = 2 * n + kh;
            const int yo = X2_OFF + lyr * 2 * MB + (T * 32 + 2 * u) * 1024;
            const u32x4 r0 = ldw(rs, lane * 16, yo), r1 = ldw(rs, lane * 16 + 1024, yo);
            if (w == 0) wait_group(p, 4 + 5 * lyr, g, tag, lane);
            BAR();
            TS(tb + 15);
            const LAS char* wb = lds + kh * 2048 + lane * 16;
            f32x16 acc[2] = {};
            const int ho = H_OFF + lyr * 8 * MB + (T * 112 + 56 * kh) * 1024;
            u32x4 xa[56];
#pragma unroll
            for (int s = 0; s < PD0; ++s) { LDA(xa, s, ho); SB(); }
            auto woff = [](int s) { return w2_slot(s); };
            bf16x8 wf[4][2];
            const int fo = kh * 1024, fs = 1024 - fo;
#pragma unroll
            for (int s = 0; s < 3; ++s) { wf[s][0] = ldsfrag(wb + fo + woff(s)); wf[s][1] = ldsfrag(wb + fs + woff(s)); }
#pragma unroll
            for (int s = 0; s < 56; ++s) {
                if (s == 18) { BAR(); TS(tb + 16); }
                if (s == 35) { BAR(); TS(tb + 17); }
#pragma unroll
                for (int j = (s == 0 ? PD0 : a_issued(s - 1, 56)); j < a_issued(s, 56); ++j) LDA(xa, j, ho);
                if (s + 3 < 56) { wf[(s + 3) & 3][0] = ldsfrag(wb + fo + woff(s + 3)); wf[(s + 3) & 3][1] = ldsfrag(wb + fs + woff(s + 3)); }
                const bf16x8 x = asfrag(xa[s]);
                acc[0] = mfma32(wf[s & 3][0], x, acc[0]); acc[1] = mfma32(wf[s & 3][1], x, acc[1]);
                SGB(0x100, 1); SGB(0x8, 1); SGB(0x20, 2); SGB(0x100, 1); SGB(0x8, 1);
                SB();
            }
            const f32x16 snd = acc[1];
#pragma unroll
            for (int g4 = 0; g4 < 4; ++g4) *(LAS f32x4*)(lds + L_RED4 + ((w * 4 + g4) * 64 + lane) * 16) = f32x4{snd[4 * g4], snd[4 * g4 + 1], snd[4 * g4 + 2], snd[4 * g4 + 3]};
            TS(tb + 18);
            BAR();
            const f32x16 own = acc[0];
            const int pw = w ^ 2;
            float v[16];
#pragma unroll
            for (int g4 = 0; g4 < 4; ++g4) { f32x4 t = *(LAS f32x4*)(lds + L_RED4 + ((pw * 4 + g4) * 64 + lane) * 16);
#pragma unroll
                for (int i = 0; i < 4; ++i) v[4 * g4 + i] = own[4 * g4 + i] + t[i]; }
            const unsigned rr[8] = {r0.x, r0.y, r0.z, r0.w, r1.x, r1.y, r1.z, r1.w};
            unsigned c[8];
#pragma unroll
            for (int i = 0; i < 8; ++i) c[i] = pk(v[2 * i] + bflo(rr[i]), v[2 * i + 1] + bfhi(rr[i]));
            if (lyr == 0) {
                stw(rs, lane * 16, XF_OFF + 2 * MB + (T * 32 + 2 * u) * 1024, u32x4{c[0], c[1], c[2], c[3]});
                stw(rs, lane * 16 + 1024, XF_OFF + 2 * MB + (T * 32 + 2 * u) * 1024, u32x4{c[4], c[5], c[6], c[7]});
                stats_store(rs, 2, T, u, lane, c);
                DRAIN();
                BAR();
                if (w == 0 && lane == 0) set_flag(p, 5, g, r, tag);
            } else {
                auto s0 = __builtin_amdgcn_permlane32_swap(c[0], c[2], false, false);
                auto s1 = __builtin_amdgcn_permlane32_swap(c[1], c[3], false, false);
                auto s2 = __builtin_amdgcn_permlane32_swap(c[4], c[6], false, false);
                auto s3 = __builtin_amdgcn_permlane32_swap(c[5], c[7], false, false);
                uint16_t* orow = p.out + (size_t)(T * 32 + (lane & 31)) * DM + 32 * u + 8 * h;
                *(GAS u32x4*)(orow) = u32x4{s0[0], s1[0], s0[1], s1[1]};
                *(GAS u32x4*)(orow + 16) = u32x4{s2[0], s3[0], s2[1], s3[1]};
            }
            TS(tb + 19);
        }
    }
    asm volatile("s_waitcnt lgkmcnt(0)" ::: "memory");
    if (w == 0) TS_DUMP();
}

template <int PART> DI void pretile(const Params& p, LAS char* lds, unsigned lds0, rsrc_t rs, int hw, int g, int r, int lane) {
    const int h = lane >> 5, a = (lane & 15) >> 2, pp = lane & 3, ch = (lane >> 4) & 1;
    const int n = r >> 1, j = 2 * g + (r & 1);
    const unsigned scr0 = lds0 + L_SCR + hw * 8192;
    const LAS char* scr = lds + L_SCR + hw * 8192;
    if (hw == 0) {
        const char* base = (const char*)(p.wqkv + (size_t)32 * g * 1536 + 32 * r);
        if (PART == 0) {
#pragma unroll
            for (int i = 0; i < 6; ++i) { const int L = 64 * i + lane, k = L / 12, c = L % 12; glds16s<false>(base, (unsigned)(k * 1536 + (c >> 2) * 512 + (c & 3) * 8) * 2, scr0 + i * 1024); }
            return;
        }
        DRAIN();
        const LAS char* wb = scr + (4 * h + a) * 192 + ch * 32 + pp * 8;
#pragma unroll
        for (int s = 0; s < 2; ++s)
#pragma unroll
            for (int t = 0; t < 3; ++t) stw(rs, lane * 16, WQT_OFF + ((r * 32 + 2 * g + s) * 3 + t) * 1024, wfrag_tr(wb + s * 16 * 192 + t * 64, 8 * 192));
    } else if (hw == 1) {
        const char* base = (const char*)(p.w1 + (size_t)32 * g * FF + 112 * r);
        if (PART == 0) {
#pragma unroll
            for (int b = 0; b < 7; ++b) {
                const int L = 64 * b + lane, G = L / 56, x = L - G * 56;
                const int aa = x < 48 ? ((x >> 2) & 3) : ((x - 48) >> 1);
                const int c = x < 48 ? ((x >> 4) * 4 + (x & 3)) : (12 + (x & 1));
                glds16s<false>(base, (unsigned)((4 * G + aa) * FF + c * 8) * 2, scr0 + b * 1024);
            }
            return;
        }
        DRAIN();
        const LAS char* wbA = scr + h * 896 + a * 64 + ch * 32 + pp * 8;
        const LAS char* wbB = scr + h * 896 + 768 + a * 32 + pp * 8;
#pragma unroll
        for (int s = 0; s < 2; ++s) {
            const int dst = W1T_OFF + r * 114688 + (2 * g + s) * 3584;
#pragma unroll
            for (int t = 0; t < 3; ++t) stw(rs, lane * 16, dst + t * 1024, wfrag_tr(wbA + s * 3584 + t * 256, 1792));
            const u32x4 f3 = wfrag_tr(wbB + s * 3584, 1792);
            if (ch == 0) stw(rs, (h * 16 + (lane & 15)) * 16, dst + 3072, f3);
        }
    } else {
        const int rr = lane >> 3, slot = lane & 7, chunk = slot ^ (((rr >> 1) & 1) << 2);
        const unsigned vo = (unsigned)(rr * DM + chunk * 8) * 2;
        const int s0 = hw == 3 ? j : (j < 16 ? 4 * j : 64 + 3 * (j - 16)), ns = hw == 3 ? 1 : (j < 16 ? 4 : 3);
        const char* base = (const char*)((hw == 3 ? p.wo : p.w2) + (size_t)16 * s0 * DM + 64 * n);
        if (PART == 0) {
            for (int i = 0; i < 2 * ns; ++i) glds16s<false>(base + (size_t)i * 8 * DM * 2, vo, scr0 + i * 1024);
            return;
        }
        DRAIN();
        const int sw = ((a >> 1) & 1) << 6;
        const LAS char* wb0 = scr + (4 * h + a) * 128 + (0 ^ sw) + ch * 32 + pp * 8;
        const LAS char* wb1 = scr + (4 * h + a) * 128 + (64 ^ sw) + ch * 32 + pp * 8;
        for (int i = 0; i < ns; ++i) {
            const int s = s0 + i;
            const int kh = s >= 56 ? 1 : 0, sl = s - 56 * kh;
            const int dst = hw == 3 ? WOT_OFF + n * 65536 + s * 2048 : W2T_OFF + (n * 56 + sl) * 4096 + kh * 2048;
            stw(rs, lane * 16, dst, wfrag_tr(wb0 + i * 2048, 1024));
            stw(rs, lane * 16, dst + 1024, wfrag_tr(wb1 + i * 2048, 1024));
        }
    }
    DRAIN();
}

constexpr int w2_regsp(int j) { return j < 10 ? j : j < 30 ? j + 4 : j + 8; }
DI void helper_path(const Params& p, LAS char* lds, unsigned lds0_, rsrc_t rs, const int w, const int g_, const int r_) {
    unsigned tag = 0;
    TS_DECL();
    {   const int layer = 0; PHASE_VARS(); HELPER_VARS();
        if (hw == 0) pretile<0>(p, lds, lds0, rs, hw, g, r, lane);
        tag = read_tag(p); asm volatile("" :: "s"(tag) : "memory");
        if (hw == 3) {
            unsigned keep;
            const unsigned vo = (unsigned)(lane < 44 ? lane : 43) << 20;
            asm volatile("s_mov_b32 %0, m0\n\ts_mov_b32 m0, %3\n\ts_nop 0\n\tglobal_load_lds_dword %1, %2\n\ts_mov_b32 m0, %0" : "=&s"(keep) : "v"(vo), "s"(p.ws), "s"(lds0 + 140 * KB) : "memory");
            const unsigned vo2 = (unsigned)(lane & 1) << 20;
            asm volatile("s_mov_b32 %0, m0\n\ts_mov_b32 m0, %3\n\ts_nop 0\n\tglobal_load_lds_dword %1, %2\n\ts_mov_b32 m0, %0" : "=&s"(keep) : "v"(vo2), "s"(p.out), "s"(lds0 + 141 * KB) : "memory");
        }
        if (hw > 0) pretile<0>(p, lds, lds0, rs, hw, g, r, lane);
        if (hw == 0) { pretile<1>(p, lds, lds0, rs, hw, g, r, lane); BAR(); }
        else { BAR(); pretile<1>(p, lds, lds0, rs, hw, g, r, lane); }
    }
    for (int layer = 0; layer < 2; ++layer) {
        {
            PHASE_VARS(); HELPER_VARS();
            HTS(layer * 20 + 0);
            BAR();
            if (lyr == 0) {
                if (w == 6 && lane == 0) set_flag(p, 10, g, r, tag);
                auto wq_pieces = [&](int j0) { for (int j = j0 + hw; j < j0 + 24; j += 4) glds16s<true>(wsb + WQT_OFF + r * 98304 + j * 1024, lane * 16, lds0 + L_WQ + j * 1024); };
                auto wo_pieces = [&](int j0, int j1) { for (int j = j0 + hw; j < j1; j += 4) glds16s<true>(wsb + WOT_OFF + n * 65536 + (j < 62 ? j : j - 4) * 1024, lane * 16, lds0 + 96 * KB + (j < 62 ? j : j - 4) * 1024); };
                float acc = 0.f;
                const LAS char* wbq = lds + L_WQ + (hw < 3 ? hw : 0) * 1024 + lane * 16;
                wq_pieces(24);
                BAR();
                wq_pieces(48);
                wait_word(p, flagp(p, 10, lane & 15, (lane & 16) ? (r ^ 1) : r), tag);
                DRAIN(); BAR();
                wq_pieces(72);
                acc = colsum_frags<8>(wbq, [](int i) { return (0 + i) * 3072; }, acc);
                BAR();
                wo_pieces(0, 32);
                acc = colsum_frags<8>(wbq, [](int i) { return (8 + i) * 3072; }, acc);
                WAITV(8); BAR();
                acc = colsum_frags<8>(wbq, [](int i) { return (16 + i) * 3072; }, acc);
                acc = colsum_frags<8>(wbq, [](int i) { return (24 + i) * 3072; }, acc);
                acc = sum_xor32(acc);
                if (h == 0 && hw < 3) ((LAS float*)(lds + L_CSQ))[hw * 32 + tok] = acc;
                wo_pieces(32, 64);
            } else {
                __builtin_amdgcn_s_sleep(WODELAY);
                for (int i = hw; i < 62; i += 4) glds16s<true>(wsb + WOT_OFF + n * 65536 + i * 1024, lane * 16, lds0 + 96 * KB + i * 1024);
            }
            BAR();
            if (w == 7 && lane < 2) *(LAS u32x4*)(lds + L_ONES + lane * 1024) = u32x4{0x3f803f80u, 0x3f803f80u, 0x3f803f80u, 0x3f803f80u};
            HTS(layer * 20 + 2);
            stage_lin(wsb, lds0, WOT_OFF + n * 65536 + 62 * KB, 94 * KB, 2, hw, lane);
            stage_lin(wsb, lds0, W1T_OFF + r * 114688, 56 * KB, 35, hw, lane);
            HTS(layer * 20 + 3);
            BAR();
        }
        u32x4 buf[48];
        {   PHASE_VARS(); HELPER_VARS();

            attn_phase(p, lds, rs, w, g, r, lyr, lane, tag TS_ARG(layer == 0 ? 40 : 44));
            HTS(layer * 20 + 4);
            DRAIN();
            HTS(layer * 20 + 5);
            BAR();
            stage_lin(wsb, lds0, W1T_OFF + r * 114688 + 35 * KB, 0, 39, hw, lane);
            HTS(layer * 20 + 6);
        }
        {
            PHASE_VARS(); HELPER_VARS();
            const int wsrc = W2T_OFF + n * 56 * 4096 + hw * 1024;
            BAR();
#pragma unroll
            for (int j = 0; j < 12; ++j) { buf[j] = ldw(rs, lane * 16, wsrc + w2_regsp(j) * 4096); SB(); }
            BAR();
#pragma unroll
            for (int j = 12; j < 22; ++j) { buf[j] = ldw(rs, lane * 16, wsrc + w2_regsp(j) * 4096); SB(); }
            BAR();
            stage_lin(wsb, lds0, W1T_OFF + r * 114688 + 75264, 94 * KB, 39, hw, lane);
        }
        {
            PHASE_VARS(); HELPER_VARS();
            HTS(layer * 20 + 8);
            WAITV(31);
            BAR();
            const int wsrc = W2T_OFF + n * 56 * 4096 + hw * 1024;
            const LAS char* wbc = hw < 3 ? lds + hw * 1024 + lane * 16 : lds + 3072 + (h * 16 + (lane & 15)) * 16;
            float cs_part = 0.f;
            if (lyr == 0) {
                cs_part = colsum_frags<11>(wbc, [](int i) { return w1_off(i); }, 0.f);
                cs_part = colsum_frags<10>(wbc, [](int i) { return w1_off(11 + i); }, cs_part);
            }
            stage_lin(wsb, lds0, W2T_OFF + (n * 56 + 10) * 4096, 40 * KB, 16, hw, lane);
            stage_lin(wsb, lds0, W2T_OFF + (n * 56 + 34) * 4096, 136 * KB, 16, hw, lane);
#pragma unroll
            for (int j = 22; j < 34; ++j) { buf[j] = ldw(rs, lane * 16, wsrc + w2_regsp(j) * 4096); SB(); }
            WAITV(20);
            HTS(layer * 20 + 10);
            BAR();
#pragma unroll
            for (int j = 34; j < 48; ++j) { buf[j] = ldw(rs, lane * 16, wsrc + w2_regsp(j) * 4096); SB(); }
            if (lyr == 0) {
                LAS float* cs1 = (LAS float*)(lds + L_CS1);
                cs_part = colsum_frags<11>(wbc, [](int i) { return w1_off(21 + i); }, cs_part);
                const float s = sum_xor32(cs_part);
                if (h == 0 && (hw < 3 || tok < 16)) cs1[hw * 32 + tok] = s;
            }
            WAITV(14);
            BAR();
            HTS(layer * 20 + 11);
#pragma unroll
            for (int j = 0; j < 30; ++j) *(LAS u32x4*)(lds + w2_slot(w2_regsp(j)) + hw * 1024 + lane * 16) = buf[j];
            HTS(layer * 20 + 12);
            BAR();
            if (lyr == 0) {
#pragma unroll
                for (int j = 0; j < 24; ++j) { buf[j] = ldw(rs, lane * 16, WQT_OFF + r * 98304 + (4 * j + hw) * 1024); SB(); }
            }
        }
        {
            PHASE_VARS(); HELPER_VARS();
            BAR();
            BAR();
#pragma unroll
            for (int j = 30; j < 48; ++j) *(LAS u32x4*)(lds + w2_slot(w2_regsp(j)) + hw * 1024 + lane * 16) = buf[j];
            if (lyr == 1 && blockIdx.x == 0 && w == 7) {
                bool ok = false;
                for (unsigned spins = 0; spins < (1u << 16) && !ok; ++spins) {
                    bool mine = true;
#pragma unroll
                    for (int j = 0; j < 4; ++j) mine &= __hip_atomic_load((gu32*)(p.ws + ACK_OFF) + (lane + 64 * j) * 32, __ATOMIC_RELAXED, __HIP_MEMORY_SCOPE_AGENT) == tag;
                    ok = __all(mine);
                }
                if (lane == 0) __hip_atomic_store((gu32*)(p.ws + EPOCH_OFF), tag, __ATOMIC_RELAXED, __HIP_MEMORY_SCOPE_AGENT);
            }
            HTS(layer * 20 + 15);
            BAR();
            BAR();
            if (lyr == 0) {
                BAR();
#pragma unroll
                for (int j = 0; j < 24; ++j) *(LAS u32x4*)(lds + L_WQ + (4 * j + hw) * 1024 + lane * 16) = buf[j];
                HTS(17);
            }
        }
    }
    asm volatile("s_waitcnt lgkmcnt(0)" ::: "memory");
    if (w == 4) HTS_DUMP();
}

__global__ __launch_bounds__(512, 2) void k_mega(Params p) {
    extern __shared__ __attribute__((aligned(16))) char smem_[];
    LAS char* lds = (LAS char*)smem_;
    const unsigned lds0 = (unsigned)(uintptr_t)smem_;
    const int b = blockIdx.x, idx = b >> 3;
    const int g_ = (b & 3) * 4 + (idx >> 3), r_ = ((b >> 2) & 1) * 8 + (idx & 7);
    const int w = __builtin_amdgcn_readfirstlane(threadIdx.x >> 6);
    const rsrc_t rs = __builtin_amdgcn_make_buffer_rsrc((void*)p.ws, 0, WS_USED, 0x00020000);
    if (w < 4) main_path(p, lds, rs, w, g_, r_);
    else helper_path(p, lds, lds0, rs, w, g_, r_);
}
}

extern "C" void kernel_launch(void* const* d_in, const int* in_sizes, int n_in, void* d_out, int out_size, void* d_ws, size_t ws_size, hipStream_t stream) {
    Params p;
    p.x = (const uint16_t*)d_in[0]; p.wqkv = (const uint16_t*)d_in[1]; p.wo = (const uint16_t*)d_in[2];
    p.w1 = (const uint16_t*)d_in[4]; p.w2 = (const uint16_t*)d_in[5];
    p.out = (uint16_t*)d_out; p.ws = (char*)d_ws;
    (void)hipFuncSetAttribute((const void*)k_mega, hipFuncAttributeMaxDynamicSharedMemorySize, LDS_BYTES);
    k_mega<<<256, 512, LDS_BYTES, stream>>>(p);
}
```

```cpp
#include <hip/hip_runtime.h>
#include <hip/hip_bf16.h>
#include <stdint.h>

#define GAS __attribute__((address_space(1)))
#define LAS __attribute__((address_space(3)))
#define DI __device__ __forceinline__

typedef __bf16 bf16x8 __attribute__((ext_vector_type(8)));
typedef __bf16 bf16x2 __attribute__((ext_vector_type(2)));
typedef short s16x4 __attribute__((ext_vector_type(4)));
typedef float f32x16 __attribute__((ext_vector_type(16)));
typedef float f32x4 __attribute__((ext_vector_type(4)));
typedef float f32x2 __attribute__((ext_vector_type(2)));
typedef unsigned u32x4 __attribute__((ext_vector_type(4)));
typedef unsigned u32x2 __attribute__((ext_vector_type(2)));
typedef GAS unsigned gu32;

namespace {
constexpr int DM = 512, FF = 1792;
constexpr float EPS = 1e-5f;
constexpr float LOG2E = 1.4426950408889634f;
constexpr float QSCALE = 0.25f * LOG2E;

constexpr int MB = 1 << 20, KB = 1024;
constexpr int XF_OFF = 0;
constexpr int O_OFF = 4 * MB;
constexpr int X2_OFF = 8 * MB;
constexpr int H_OFF = 12 * MB;
constexpr int KV_OFF = 28 * MB;
constexpr int WQT_OFF = 36 * MB;
constexpr int WOT_OFF = WQT_OFF + 1536 * KB;
constexpr int W1T_OFF = 38 * MB;
constexpr int W2T_OFF = 40 * MB;
constexpr int ST_OFF = 42 * MB;
constexpr int ST_BUF = 262144;
constexpr int KN_OFF = 43 * MB;
constexpr int FLAG_OFF = 43 * MB + 65536;
constexpr int FLAG_BYTES = 11 * 256 * 128;
constexpr int TMO_OFF = FLAG_OFF + FLAG_BYTES;
constexpr int EPOCH_OFF = TMO_OFF + 256;
constexpr int ACK_OFF = EPOCH_OFF + 256;
constexpr int WS_USED = 44 * MB;

constexpr int L_WQ = 0;
constexpr int L_Q = 0, L_KV = 8 * KB, L_ONES = 91 * KB;
constexpr int L_RED2 = 40 * KB;
constexpr int L_RED4 = 96 * KB;
constexpr int L_SCR = 100 * KB;
constexpr int L_TAB = 158 * KB, L_CSQ = L_TAB, L_CS1 = L_TAB + 512, L_STT = L_TAB + 1024;
constexpr int L_KN = L_TAB + 384, L_QN = L_TAB + 384 + 96;
constexpr int LDS_BYTES = 160 * KB;
constexpr int w1_off(int s) { return s < 10 ? 56 * KB + s * 3584 : s < 21 ? (s - 10) * 3584 : 94 * KB + (s - 21) * 3584; }
constexpr int w2_slot(int sp) { return (sp < 38 ? sp : sp - 38) * 4096; }
constexpr int AUX_SC1 = 16;

struct Params {
    const uint16_t* x; const uint16_t* wqkv; const uint16_t* wo; const uint16_t* w1; const uint16_t* w2;
    uint16_t* out; char* ws;
};

DI int lane_id() { return (int)__builtin_amdgcn_mbcnt_hi(~0u, __builtin_amdgcn_mbcnt_lo(~0u, 0u)); }
DI f32x16 mfma32(bf16x8 a, bf16x8 b, f32x16 c) { return __builtin_amdgcn_mfma_f32_32x32x16_bf16(a, b, c, 0, 0, 0); }
DI unsigned pk(float a, float b) { bf16x2 v = {(__bf16)a, (__bf16)b}; return __builtin_bit_cast(unsigned, v); }
DI float bflo(unsigned u) { return __uint_as_float(u << 16); }
DI float bfhi(unsigned u) { return __uint_as_float(u & 0xffff0000u); }
DI bf16x8 asfrag(u32x4 v) { return __builtin_bit_cast(bf16x8, v); }
DI s16x4 trd(const LAS char* p) { return __builtin_amdgcn_ds_read_tr16_b64_v4i16((LAS s16x4*)p); }
DI u32x4 wfrag_tr(const LAS char* p, int off2) {
    const u32x2 lo = __builtin_bit_cast(u32x2, trd(p)), hi = __builtin_bit_cast(u32x2, trd(p + off2));
    return u32x4{lo.x, lo.y, hi.x, hi.y};
}
DI bf16x8 ldsfrag(const LAS char* p) { return *(const LAS bf16x8*)p; }
template <int N, class F> DI float colsum_frags(const LAS char* p, F off, float acc) {
    u32x4 f[N];
#pragma unroll
    for (int i = 0; i < N; ++i) f[i] = *(const LAS u32x4*)(p + off(i));
    float a1 = 0.f;
#pragma unroll
    for (int i = 0; i < N; ++i) {
        unsigned w0 = f[i].x, w1 = f[i].y, w2 = f[i].z, w3 = f[i].w;
        asm("v_dot2c_f32_bf16 %0, %2, %6\n\tv_dot2c_f32_bf16 %1, %3, %6\n\tv_dot2c_f32_bf16 %0, %4, %6\n\tv_dot2c_f32_bf16 %1, %5, %6"
            : "+v"(acc), "+v"(a1) : "v"(w0), "v"(w1), "v"(w2), "v"(w3), "v"(0x3f803f80u));
    }
    asm volatile("s_nop 2" : "+v"(acc), "+v"(a1));
    return acc + a1;
}
DI float sum_xor32(float v) { auto r = __builtin_amdgcn_permlane32_swap(__float_as_uint(v), __float_as_uint(v), false, false); return __uint_as_float(r[0]) + __uint_as_float(r[1]); }
DI float max_xor32(float v) { auto r = __builtin_amdgcn_permlane32_swap(__float_as_uint(v), __float_as_uint(v), false, false); return fmaxf(__uint_as_float(r[0]), __uint_as_float(r[1])); }
template <bool SC1> DI void glds16s(const void* sbase, unsigned voff, unsigned lds_addr) {
    unsigned keep;
    if (SC1) asm volatile("s_mov_b32 %0, m0\n\ts_mov_b32 m0, %3\n\ts_nop 0\n\tglobal_load_lds_dwordx4 %1, %2 sc1\n\ts_mov_b32 m0, %0" : "=&s"(keep) : "v"(voff), "s"(sbase), "s"(lds_addr) : "memory");
    else asm volatile("s_mov_b32 %0, m0\n\ts_mov_b32 m0, %3\n\ts_nop 0\n\tglobal_load_lds_dwordx4 %1, %2\n\ts_mov_b32 m0, %0" : "=&s"(keep) : "v"(voff), "s"(sbase), "s"(lds_addr) : "memory");
}
#define BAR() asm volatile("s_waitcnt lgkmcnt(0)\n\ts_barrier" ::: "memory")
#define DRAIN() asm volatile("s_waitcnt vmcnt(0)" ::: "memory")
#define WAITV(n) asm volatile("s_waitcnt vmcnt(" #n ")" ::: "memory")
#define SB() __builtin_amdgcn_sched_barrier(0)
#define SGB(mask, n) __builtin_amdgcn_sched_group_barrier(mask, n, 0)

typedef __amdgpu_buffer_rsrc_t rsrc_t;
DI u32x4 ldw(rsrc_t rs, int voff, int soff) { return __builtin_amdgcn_raw_buffer_load_b128(rs, voff, soff, AUX_SC1); }
DI void stw(rsrc_t rs, int voff, int soff, u32x4 v) { __builtin_amdgcn_raw_buffer_store_b128(v, rs, voff + soff, 0, AUX_SC1); }
DI f32x2 ldst(rsrc_t rs, int voff, int soff) { return __builtin_bit_cast(f32x2, __builtin_amdgcn_raw_buffer_load_b64(rs, voff, soff, AUX_SC1)); }
DI void stst(rsrc_t rs, int voff, int soff, f32x2 v) { __builtin_amdgcn_raw_buffer_store_b64(__builtin_bit_cast(u32x2, v), rs, voff, soff, AUX_SC1); }

DI gu32* flagp(const Params& p, int ph, int g, int r) { return (gu32*)(p.ws + FLAG_OFF) + ((ph * 16 + g) * 16 + r) * 32; }
DI void set_flag(const Params& p, int ph, int g, int r, unsigned tag) { __hip_atomic_store(flagp(p, ph, g, r), tag, __ATOMIC_RELAXED, __HIP_MEMORY_SCOPE_AGENT); }
DI void wait_word(const Params& p, gu32* a, unsigned tag) {
#pragma nounroll
    for (unsigned spins = 0;; ++spins) {
        const unsigned v = __hip_atomic_load(a, __ATOMIC_RELAXED, __HIP_MEMORY_SCOPE_AGENT);
        if (__all(v == tag)) break;
        if (spins > (1u << 20)) { __hip_atomic_store((gu32*)(p.ws + TMO_OFF), 1u, __ATOMIC_RELAXED, __HIP_MEMORY_SCOPE_AGENT); break; }
        __builtin_amdgcn_s_sleep(1);
    }
    __builtin_amdgcn_fence(__ATOMIC_ACQUIRE, "wavefront");
}
DI void wait_group(const Params& p, int ph, int g, unsigned tag, int lane) { wait_word(p, flagp(p, ph, g, lane & 15), tag); }

DI void put_row_stats(rsrc_t rs, int buf, int T, int u, int lane, const unsigned (&c)[8]) {
    float s = 0.f, s1 = 0.f, ss = 0.f, ss1 = 0.f;
#pragma unroll
    for (int i = 0; i < 8; i += 2) {
        unsigned w0 = c[i], w1 = c[i + 1];
        asm("v_dot2c_f32_bf16 %0, %4, %6\n\tv_dot2c_f32_bf16 %1, %5, %6\n\tv_dot2c_f32_bf16 %2, %4, %4\n\tv_dot2c_f32_bf16 %3, %5, %5"
            : "+v"(s), "+v"(s1), "+v"(ss), "+v"(ss1) : "v"(w0), "v"(w1), "v"(0x3f803f80u));
    }
    asm volatile("s_nop 2" : "+v"(s), "+v"(s1), "+v"(ss), "+v"(ss1));
    s = sum_xor32(s + s1); ss = sum_xor32(ss + ss1);
    if (lane < 32) stst(rs, lane * 8, ST_OFF + buf * ST_BUF + (T * 16 + u) * 256, f32x2{s, ss});
}

DI void stage_lin(const char* wsb, unsigned lds0, int src, int dst, int n, int hw, int lane, int nw = 4) {
    const unsigned vo = lane * 16;
    for (int i = hw; i < n; i += nw) glds16s<true>(wsb + src + i * 1024, vo, lds0 + dst + i * 1024);
}

#define HELPER_VARS() \
    int hw = w_ - 4; asm volatile("" : "+s"(hw)); \
    const char* wsb = p.ws; asm volatile("" : "+s"(wsb)); \
    unsigned lds0 = lds0_; asm volatile("" : "+s"(lds0)); (void)hw; (void)wsb; (void)lds0;
#define PHASE_VARS() \
    int lane = lane_id(); asm volatile("" : "+v"(lane)); \
    int lyr = layer; asm volatile("" : "+s"(lyr)); \
    int g = g_, r = r_, w = w_; asm volatile("" : "+s"(g), "+s"(r), "+s"(w)); \
    const int h = lane >> 5, tok = lane & 31; const int m = r & 1, n = r >> 1; (void)h; (void)tok; (void)m; (void)n; (void)lyr;

DI void attn_finish(rsrc_t rs, const f32x16& o, int hd, int T, int r, int lyr, int lane) {
    float ov[8]; float l;
    if (hd == 0) { l = o[8];
#pragma unroll
        for (int i = 0; i < 8; ++i) ov[i] = o[i];
    } else { l = o[0];
#pragma unroll
        for (int i = 0; i < 8; ++i) ov[i] = o[8 + i];
    }
    const float inv = __builtin_amdgcn_rcpf(l);
    stw(rs, lane * 16, O_OFF + lyr * 2 * MB + (T * 32 + 2 * r + hd) * 1024,
        u32x4{pk(ov[0] * inv, ov[1] * inv), pk(ov[2] * inv, ov[3] * inv), pk(ov[4] * inv, ov[5] * inv), pk(ov[6] * inv, ov[7] * inv)});
}
DI float max16(float m, const f32x16& s) {
#pragma unroll
    for (int i = 0; i < 16; i += 2) m = fmaxf(fmaxf(m, s[i]), s[i + 1]);
    return m;
}
DI void exp_pack(const f32x16& s, u32x4& p0, u32x4& p1) {
    unsigned pw[8];
#pragma unroll
    for (int i = 0; i < 8; ++i) pw[i] = pk(__builtin_amdgcn_exp2f(s[2 * i]), __builtin_amdgcn_exp2f(s[2 * i + 1]));
    p0 = u32x4{pw[0], pw[1], pw[2], pw[3]}; p1 = u32x4{pw[4], pw[5], pw[6], pw[7]};
}
DI float wave32_max(float v) {
    int x = __float_as_int(v);
    x = max(x, __builtin_amdgcn_update_dpp(x, x, 0x111, 0xf, 0xf, false));
    x = max(x, __builtin_amdgcn_update_dpp(x, x, 0x112, 0xf, 0xf, false));
    x = max(x, __builtin_amdgcn_update_dpp(x, x, 0x114, 0xf, 0xf, false));
    x = max(x, __builtin_amdgcn_update_dpp(x, x, 0x118, 0xf, 0xf, false));
    x = max(x, __builtin_amdgcn_update_dpp(x, x, 0x142, 0xa, 0xf, false));
    return __int_as_float(__builtin_amdgcn_readlane(x, 31));
}
DI void attn_seg(unsigned ka, unsigned va, int ks, int vs, int n, const bf16x8 qf, const f32x16 c0, f32x16& o) {
    int d;
    asm volatile(
        "ds_read_b128 v[248:251], %[ka]\n\tv_add_u32 %[ka], %[ks], %[ka]\n\tds_read_b128 v[236:239], %[ka]\n\tv_add_u32 %[ka], %[ks], %[ka]\n\t"
        "ds_read_b128 v[240:243], %[va]\n\tds_read_b128 v[244:247], %[va] offset:1024\n\tv_add_u32 %[va], %[vs], %[va]\n\t"
        "s_lshr_b32 %[d], %[n], 1\n\ts_waitcnt lgkmcnt(3)\n\t"
        "v_mfma_f32_32x32x16_bf16 v[204:219], v[248:251], %[q], %[c0]\n\t"
        "s_cmp_eq_u32 %[d], 0\n\ts_nop 10\n\ts_cbranch_scc1 Ltail_%=\n"
        "Lloop_%=:\n\t"
        "v_exp_f32 v204, v204\n\t"
        "v_exp_f32 v205, v205\n\t"
        "v_exp_f32 v206, v206\n\t"
        "v_exp_f32 v207, v207\n\t"
        "v_exp_f32 v208, v208\n\t"
        "v_exp_f32 v209, v209\n\t"
        "v_exp_f32 v210, v210\n\t"
        "v_exp_f32 v211, v211\n\t"
        "v_cvt_pk_bf16_f32 v204, v204, v205\n\t"
        "v_cvt_pk_bf16_f32 v205, v206, v207\n\t"
        "v_cvt_pk_bf16_f32 v206, v208, v209\n\t"
        "v_cvt_pk_bf16_f32 v207, v210, v211\n\t"
        "s_waitcnt lgkmcnt(1)\n\ts_nop 0\n\t"
        "v_mfma_f32_32x32x16_bf16 %[o], v[240:243], v[204:207], %[o]\n\t"
        "v_exp_f32 v212, v212\n\t"
        "v_exp_f32 v213, v213\n\t"
        "v_exp_f32 v214, v214\n\t"
        "v_exp_f32 v215, v215\n\t"
        "v_mfma_f32_32x32x16_bf16 v[220:235], v[236:239], %[q], 0\n\t"
        "ds_read_b128 v[236:239], %[ka]\n\tds_read_b128 v[248:251], %[va]\n\tds_read_b128 v[252:255], %[va] offset:1024\n\t"
        "v_add_u32 %[ka], %[ks], %[ka]\n\tv_add_u32 %[va], %[vs], %[va]\n\t"
        "v_exp_f32 v216, v216\n\t"
        "v_exp_f32 v217, v217\n\t"
        "v_exp_f32 v218, v218\n\t"
        "v_exp_f32 v219, v219\n\t"
        "v_cvt_pk_bf16_f32 v212, v212, v213\n\t"
        "v_cvt_pk_bf16_f32 v213, v214, v215\n\t"
        "v_cvt_pk_bf16_f32 v214, v216, v217\n\t"
        "v_cvt_pk_bf16_f32 v215, v218, v219\n\t"
        "s_waitcnt lgkmcnt(3)\n\ts_nop 0\n\t"
        "v_mfma_f32_32x32x16_bf16 %[o], v[244:247], v[212:215], %[o]\n\t"
        "v_exp_f32 v220, v220\n\t"
        "v_exp_f32 v221, v221\n\t"
        "v_exp_f32 v222, v222\n\t"
        "v_exp_f32 v223, v223\n\t"
        "v_exp_f32 v224, v224\n\t"
        "v_exp_f32 v225, v225\n\t"
        "v_exp_f32 v226, v226\n\t"
        "v_exp_f32 v227, v227\n\t"
        "v_cvt_pk_bf16_f32 v220, v220, v221\n\t"
        "v_cvt_pk_bf16_f32 v221, v222, v223\n\t"
        "v_cvt_pk_bf16_f32 v222, v224, v225\n\t"
        "v_cvt_pk_bf16_f32 v223, v226, v227\n\t"
        "s_waitcnt lgkmcnt(1)\n\ts_nop 0\n\t"
        "v_mfma_f32_32x32x16_bf16 %[o], v[248:251], v[220:223], %[o]\n\t"
        "v_exp_f32 v228, v228\n\t"
        "v_exp_f32 v229, v229\n\t"
        "v_exp_f32 v230, v230\n\t"
        "v_exp_f32 v231, v231\n\t"
        "v_mfma_f32_32x32x16_bf16 v[204:219], v[236:239], %[q], 0\n\t"
        "ds_read_b128 v[236:239], %[ka]\n\tds_read_b128 v[240:243], %[va]\n\tds_read_b128 v[244:247], %[va] offset:1024\n\t"
        "v_add_u32 %[ka], %[ks], %[ka]\n\tv_add_u32 %[va], %[vs], %[va]\n\t"
        "v_exp_f32 v232, v232\n\t"
        "v_exp_f32 v233, v233\n\t"
        "v_exp_f32 v234, v234\n\t"
        "v_exp_f32 v235, v235\n\t"
        "v_cvt_pk_bf16_f32 v228, v228, v229\n\t"
        "v_cvt_pk_bf16_f32 v229, v230, v231\n\t"
        "v_cvt_pk_bf16_f32 v230, v232, v233\n\t"
        "v_cvt_pk_bf16_f32 v231, v234, v235\n\t"
        "s_waitcnt lgkmcnt(3)\n\ts_nop 0\n\t"
        "v_mfma_f32_32x32x16_bf16 %[o], v[252:255], v[228:231], %[o]\n\t"
        "s_sub_u32 %[d], %[d], 1\n\ts_cmp_lg_u32 %[d], 0\n\ts_cbranch_scc1 Lloop_%=\n"
        "Ltail_%=:\n\t"
        "s_bitcmp1_b32 %[n], 0\n\ts_waitcnt lgkmcnt(0)\n\ts_cbranch_scc0 Lend_%=\n\t"
        "v_exp_f32 v204, v204\n\t"
        "v_exp_f32 v205, v205\n\t"
        "v_exp_f32 v206, v206\n\t"
        "v_exp_f32 v207, v207\n\t"
        "v_exp_f32 v208, v208\n\t"
        "v_exp_f32 v209, v209\n\t"
        "v_exp_f32 v210, v210\n\t"
        "v_exp_f32 v211, v211\n\t"
        "v_cvt_pk_bf16_f32 v204, v204, v205\n\t"
        "v_cvt_pk_bf16_f32 v205, v206, v207\n\t"
        "v_cvt_pk_bf16_f32 v206, v208, v209\n\t"
        "v_cvt_pk_bf16_f32 v207, v210, v211\n\t"
        "s_nop 1\n\t"
        "v_mfma_f32_32x32x16_bf16 %[o], v[240:243], v[204:207], %[o]\n\t"
        "v_exp_f32 v212, v212\n\t"
        "v_exp_f32 v213, v213\n\t"
        "v_exp_f32 v214, v214\n\t"
        "v_exp_f32 v215, v215\n\t"
        "v_exp_f32 v216, v216\n\t"
        "v_exp_f32 v217, v217\n\t"
        "v_exp_f32 v218, v218\n\t"
        "v_exp_f32 v219, v219\n\t"
        "v_cvt_pk_bf16_f32 v212, v212, v213\n\t"
        "v_cvt_pk_bf16_f32 v213, v214, v215\n\t"
        "v_cvt_pk_bf16_f32 v214, v216, v217\n\t"
        "v_cvt_pk_bf16_f32 v215, v218, v219\n\t"
        "s_nop 1\n\t"
        "v_mfma_f32_32x32x16_bf16 %[o], v[244:247], v[212:215], %[o]\n"
        "Lend_%=:\n\ts_nop 11"
        : [o] "+v"(o), [ka] "+v"(ka), [va] "+v"(va), [d] "=&s"(d)
        : [q] "v"(qf), [c0] "v"(c0), [ks] "s"(ks), [vs] "v"(vs), [n] "s"(n)
        : "memory", "scc", "v204", "v205", "v206", "v207", "v208", "v209", "v210", "v211", "v212", "v213", "v214", "v215", "v216", "v217", "v218", "v219", "v220", "v221", "v222", "v223", "v224", "v225", "v226", "v227", "v228", "v229", "v230", "v231", "v232", "v233", "v234", "v235", "v236", "v237", "v238", "v239", "v240", "v241", "v242", "v243", "v244", "v245", "v246", "v247", "v248", "v249", "v250", "v251", "v252", "v253", "v254", "v255");
}
constexpr float BOUND2 = 4096.f;
DI f32x16 attn_exact(LAS char* lds, int tw, int hd, int s0, bool first_masked, const bf16x8 qf, int lane) {
    const int h = lane >> 5, tok = lane & 31;
    const LAS char* kb = lds + L_KV + hd * 1024 + lane * 16;
    const bool isdata = ((lane >> 4) & 1) == hd;
    const LAS char* vb = isdata ? (lds + L_KV + 2048 + lane * 16) : (lds + L_ONES);
    const int vstep = isdata ? 4096 : 0;
    const f32x16 zero = {};
    f32x16 o = {};
    float mx = -INFINITY;
    for (int pass = 0; pass < 2; ++pass) {
        for (int sl = s0; sl <= 8 + tw; ++sl) {
            f32x16 s = mfma32(*(const LAS bf16x8*)(kb + sl * 4096), qf, zero);
            const bool diag = sl == 8 + tw, first = first_masked && sl == s0;
#pragma unroll
            for (int i = 0; i < 16; ++i) { const int kk = (i & 3) + 8 * (i >> 2) + 4 * h; if ((diag && kk > tok) || (first && kk <= tok)) s[i] = -INFINITY; }
            if (pass == 0) mx = max16(mx, s);
            else {
#pragma unroll
                for (int i = 0; i < 16; ++i) s[i] -= mx;
                u32x4 p0, p1; exp_pack(s, p0, p1);
                o = mfma32(*(const LAS bf16x8*)(vb + sl * vstep), asfrag(p0), o);
                o = mfma32(*(const LAS bf16x8*)(vb + sl * vstep + 1024), asfrag(p1), o);
            }
        }
        if (pass == 0) mx = max_xor32(mx);
    }
    return o;
}
DI void attn_phase(const Params& p, LAS char* lds, rsrc_t rs, int w, int g, int r, int lyr, int lane, unsigned tag) {
    const int h = lane >> 5, tok = lane & 31;
    const int hd = w >> 2, tw = hd ? 3 - (w & 3) : (w & 3), T = 4 * g + tw;
    const bf16x8 qf = *(const LAS bf16x8*)(lds + L_Q + tw * 2048 + hd * 1024 + lane * 16);
    const unsigned kb = (unsigned)(uintptr_t)(lds + L_KV + hd * 1024 + lane * 16);
    const bool isdata = ((lane >> 4) & 1) == hd;
    const unsigned vb = (unsigned)(uintptr_t)(isdata ? (lds + L_KV + 2048 + lane * 16) : (lds + L_ONES));
    const int vstep = isdata ? 4096 : 0;
    const LAS float* kn = (const LAS float*)(lds + L_KN);
    const float qn = ((const LAS float*)(lds + L_QN))[tw * 2 + hd] * 1.05f;
    const float kown = fmaxf(fmaxf(kn[16 + hd], kn[18 + hd]), fmaxf(kn[20 + hd], kn[22 + hd]));
    bool fast = __builtin_amdgcn_readfirstlane(qn * kown <= BOUND2);
    f32x16 t, o = {};
#pragma unroll
    for (int i = 0; i < 16; ++i) t[i] = __builtin_fmaf((float)(tok - 4 * h), 1e30f, (0.5f - (float)((i & 3) + 8 * (i >> 2))) * 1e30f);
    const int s4 = 4 * hd, tile0 = 4 * g - 8 + s4;
    const bool loader = tw == 0 && tile0 >= 0;
    u32x4 hv[16]; f32x2 hn;
    if (loader) {
        wait_word(p, flagp(p, 1 + 5 * lyr, g - 2 + hd, r), tag);
        const int kvo = KV_OFF + lyr * 4 * MB + (tile0 * 16 + r) * 4096;
#pragma unroll
        for (int i = 0; i < 16; ++i) hv[i] = ldw(rs, lane * 16 + (i & 3) * 1024, kvo + (i >> 2) * 65536);
        hn = ldst(rs, (lane & 3) * 128, KN_OFF + ((lyr * 64 + tile0) * 16 + r) * 8);
        SB();
    }
    if (fast) {
        f32x16 m;
#pragma unroll
        for (int i = 0; i < 16; ++i) m[i] = fminf(t[i], 0.f);
        attn_seg(kb + (8 + tw) * 4096, vb + (8 + tw) * vstep, -4096, -vstep, tw + 1, qf, m, o);
    }
    if (loader) {
        LAS char* kvl = lds + L_KV + s4 * 4096 + lane * 16;
#pragma unroll
        for (int i = 0; i < 16; ++i) *(LAS u32x4*)(kvl + i * 1024) = hv[i];
        if (lane < 4) *(LAS f32x2*)(lds + L_KN + (s4 + lane) * 8) = hn;
    }
    BAR();
    const int s0 = T >= 8 ? tw : 8 - 4 * g;
    if (s0 < 8) {
        float kh = 0.f;
#pragma unroll
        for (int j = 0; j < 8; ++j) kh = fmaxf(kh, j >= 8 - 4 * g ? kn[2 * j + hd] : 0.f);
        fast = fast && __builtin_amdgcn_readfirstlane(qn * kh <= BOUND2);
        if (fast) {
            f32x16 m;
#pragma unroll
            for (int i = 0; i < 16; ++i) m[i] = T >= 8 ? fminf(-t[i], 0.f) : 0.f;
            attn_seg(kb + s0 * 4096, vb + s0 * vstep, 4096, vstep, 8 - s0, qf, m, o);
        }
    }
    if (!fast) o = attn_exact(lds, tw, hd, s0, T >= 8, qf, lane);
    attn_finish(rs, o, hd, T, r, lyr, lane);
}
DI void ln_stats(const f32x2 (&sv)[8], float& mean, float& rstd) {
    float S = 0.f, SS = 0.f;
#pragma unroll
    for (int i = 0; i < 8; ++i) { S += sv[i].x; SS += sv[i].y; }
    S = sum_xor32(S); SS = sum_xor32(SS);
    mean = S * (1.f / DM); const float var = fmaxf(SS * (1.f / DM) - mean * mean, 0.f); rstd = __builtin_amdgcn_rsqf(var + EPS);
}
DI unsigned read_tag(const Params& p) {
    return (unsigned)__builtin_amdgcn_readfirstlane(__hip_atomic_load((gu32*)(p.ws + EPOCH_OFF), __ATOMIC_RELAXED, __HIP_MEMORY_SCOPE_AGENT)) + 1u;
}
#define LDA(arr, s, base) arr[s] = ldw(rs, lane * 16 + ((s) & 3) * 1024, (base) + ((s) >> 2) * 4096)

constexpr int WODELAY = 36;
constexpr int PD0 = 4;
constexpr int PDV = 12;
constexpr int a_issued(int s, int total) { int v = PD0 + 2 * (s + 1); const int cap = s + 1 + PDV; v = v < cap ? v : cap; return v < total ? v : total; }

DI void main_path(const Params& p, LAS char* lds, rsrc_t rs, const int w_, const int g_, const int r_) {
    unsigned tag = 0;
    {
        const int layer = 0; PHASE_VARS();
        const int T = 4 * g + (r & 3), u = (r >> 2) * 4 + w;
        const uint16_t* row = p.x + (size_t)(T * 32 + tok) * DM + 32 * u + 4 * h;
        u32x2 a0 = *(const GAS u32x2*)(row), a1 = *(const GAS u32x2*)(row + 8), a2 = *(const GAS u32x2*)(row + 16), a3 = *(const GAS u32x2*)(row + 24);
        tag = read_tag(p); asm volatile("" :: "s"(tag) : "memory");
        stw(rs, lane * 16, XF_OFF + (T * 32 + 2 * u) * 1024, u32x4{a0.x, a0.y, a1.x, a1.y});
        stw(rs, lane * 16, XF_OFF + (T * 32 + 2 * u + 1) * 1024, u32x4{a2.x, a2.y, a3.x, a3.y});
        const unsigned c[8] = {a0.x, a0.y, a1.x, a1.y, a2.x, a2.y, a3.x, a3.y};
        put_row_stats(rs, 0, T, u, lane, c);
        DRAIN();
        BAR();
        if (w == 0 && lane == 0) { set_flag(p, 0, g, r, tag); __hip_atomic_store((gu32*)(p.ws + ACK_OFF) + blockIdx.x * 32, tag, __ATOMIC_RELAXED, __HIP_MEMORY_SCOPE_AGENT); }
    }
    for (int layer = 0; layer < 2; ++layer) {
        {
            PHASE_VARS();
            if (w == 0) {
                if (lyr == 0) {
                    const int j = lane & 15;
                    wait_word(p, (lane & 16) ? flagp(p, 0, j, r) : flagp(p, 0, g, j), tag);
                } else wait_group(p, 5, g, tag, lane);
            }
            BAR();
            const int T = 4 * g + w;
            const LAS char* wb = lds + L_WQ + lane * 16;
            LAS float* csq = (LAS float*)(lds + L_CSQ);
            LAS float* stt = (LAS float*)(lds + L_STT);
            f32x16 aq = {}, ak = {}, av = {};
            u32x4 xa[32];
            f32x2 sv[8];
            const int sto = ST_OFF + 2 * lyr * ST_BUF + T * 4096, xo = XF_OFF + lyr * 2 * MB + T * 32768;
            if (lyr == 0) {
                const char* src = p.ws + WQT_OFF + r * 98304;
#pragma unroll
                for (int i = 0; i < 6; ++i) glds16s<true>(src + (4 * i + w) * 1024, lane * 16, (unsigned)(uintptr_t)lds + L_WQ + (4 * i + w) * 1024);
            }
#pragma unroll
            for (int s = 0; s < PD0; ++s) { LDA(xa, s, xo); SB(); }
            if (lyr == 0) { asm volatile("s_waitcnt vmcnt(%0)" :: "i"(PD0) : "memory"); BAR(); }
            bf16x8 wf[3][3];
#pragma unroll
            for (int s = 0; s < 2; ++s)
#pragma unroll
                for (int t = 0; t < 3; ++t) wf[s][t] = ldsfrag(wb + (s * 3 + t) * 1024);
#pragma unroll
            for (int s = 0; s < 32; ++s) {
                if ((s == 6 || s == 14 || s == 22) && lyr == 0) BAR();
#pragma unroll
                for (int j = (s == 0 ? PD0 : a_issued(s - 1, 32)); j < a_issued(s, 32); ++j) LDA(xa, j, xo);
                if (s >= 16 && s < 24) sv[s - 16] = ldst(rs, (8 * h * 32 + tok) * 8 + (s - 16) * 256, sto);
                if (s + 2 < 32) {
#pragma unroll
                    for (int t = 0; t < 3; ++t) wf[(s + 2) % 3][t] = ldsfrag(wb + ((s + 2) * 3 + t) * 1024);
                }
                const bf16x8 x = asfrag(xa[s]);
                aq = mfma32(wf[s % 3][0], x, aq); ak = mfma32(wf[s % 3][1], x, ak); av = mfma32(x, wf[s % 3][2], av);
                SGB(0x100, 1); SGB(0x8, 1); SGB(0x20, 1); SGB(0x100, 1); SGB(0x8, 1); SGB(0x20, 1); SGB(0x100, 1); SGB(0x8, 1);
                SB();
            }
            float mean, rstd; ln_stats(sv, mean, rstd);
            if (h == 0) { stt[w * 64 + tok] = rstd; stt[w * 64 + 32 + tok] = mean * rstd; }
            BAR();
            const float mr = mean * rstd;
            {
                float k[16], v[16];
                const float cv = csq[64 + tok];
                f32x4 ck[4], rsv[4], ms[4];
#pragma unroll
                for (int g4 = 0; g4 < 4; ++g4) {
                    ck[g4] = *(LAS f32x4*)(csq + 32 + 8 * g4 + 4 * h);
                    rsv[g4] = *(LAS f32x4*)(stt + w * 64 + 8 * g4 + 4 * h); ms[g4] = *(LAS f32x4*)(stt + w * 64 + 32 + 8 * g4 + 4 * h);
                }
                SB();
#pragma unroll
                for (int i = 0; i < 16; ++i) k[i] = ak[i] * rstd - mr * ck[i >> 2][i & 3];
                const u32x4 k0 = {pk(k[0], k[1]), pk(k[2], k[3]), pk(k[4], k[5]), pk(k[6], k[7])};
                const u32x4 k1 = {pk(k[8], k[9]), pk(k[10], k[11]), pk(k[12], k[13]), pk(k[14], k[15])};
                const int kvo = KV_OFF + lyr * 4 * MB + (T * 16 + r) * 4096;
                stw(rs, lane * 16, kvo, k0); stw(rs, lane * 16 + 1024, kvo, k1);
                float na = 0.f, nb = 0.f;
#pragma unroll
                for (int i = 0; i < 8; ++i) { na = __builtin_fmaf(k[i], k[i], na); nb = __builtin_fmaf(k[8 + i], k[8 + i], nb); }
                const f32x2 nn = {wave32_max(sum_xor32(na)), wave32_max(sum_xor32(nb))};
                if (lane == 0) { stst(rs, 0, KN_OFF + ((lyr * 64 + T) * 16 + r) * 8, nn); *(LAS f32x2*)(lds + L_KN + (8 + w) * 8) = nn; }
                SB();
#pragma unroll
                for (int i = 0; i < 16; ++i) v[i] = av[i] * rsv[i >> 2][i & 3] - ms[i >> 2][i & 3] * cv;
                const u32x4 v0 = {pk(v[0], v[1]), pk(v[2], v[3]), pk(v[4], v[5]), pk(v[6], v[7])};
                const u32x4 v1 = {pk(v[8], v[9]), pk(v[10], v[11]), pk(v[12], v[13]), pk(v[14], v[15])};
                stw(rs, lane * 16 + 2048, kvo, v0); stw(rs, lane * 16 + 3072, kvo, v1);
                SB();
                LAS char* kvl = lds + L_KV + (8 + w) * 4096 + lane * 16;
                *(LAS u32x4*)(kvl) = k0; *(LAS u32x4*)(kvl + 1024) = k1; *(LAS u32x4*)(kvl + 2048) = v0; *(LAS u32x4*)(kvl + 3072) = v1;
            }
            SB();
            {
                float q[16];
#pragma unroll
                for (int g4 = 0; g4 < 4; ++g4) {
                    const f32x4 cq = *(LAS f32x4*)(csq + 8 * g4 + 4 * h);
#pragma unroll
                    for (int i = 0; i < 4; ++i) q[4 * g4 + i] = (aq[4 * g4 + i] * rstd - mr * cq[i]) * QSCALE;
                }
                LAS char* ql = lds + L_Q + w * 2048 + lane * 16;
                *(LAS u32x4*)(ql) = u32x4{pk(q[0], q[1]), pk(q[2], q[3]), pk(q[4], q[5]), pk(q[6], q[7])};
                *(LAS u32x4*)(ql + 1024) = u32x4{pk(q[8], q[9]), pk(q[10], q[11]), pk(q[12], q[13]), pk(q[14], q[15])};
                float na = 0.f, nb = 0.f;
#pragma unroll
                for (int i = 0; i < 8; ++i) { na = __builtin_fmaf(q[i], q[i], na); nb = __builtin_fmaf(q[8 + i], q[8 + i], nb); }
                const f32x2 nn = {wave32_max(sum_xor32(na)), wave32_max(sum_xor32(nb))};
                if (lane == 0) *(LAS f32x2*)(lds + L_QN + w * 8) = nn;
            }
            DRAIN();
            BAR();
            if (w == 0 && lane == 0) set_flag(p, 1 + 5 * lyr, g, r, tag);
        }
        {
            PHASE_VARS();
            attn_phase(p, lds, rs, w, g, r, lyr, lane, tag);
            DRAIN();
            BAR();
            if (w == 0 && lane == 0) set_flag(p, 2 + 5 * lyr, g, r, tag);
        }
        {
            PHASE_VARS();
            const int mt = w & 1, kh = (w >> 1) & 1, T = 4 * g + 2 * m + mt, u = 2 * n + kh;
            const int xo = XF_OFF + lyr * 2 * MB + (T * 32 + 2 * u) * 1024;
            const u32x4 r0 = ldw(rs, lane * 16, xo), r1 = ldw(rs, lane * 16 + 1024, xo);
            if (w == 0) wait_group(p, 2 + 5 * lyr, g, tag, lane);
            BAR();
            f32x16 acc[2] = {};
            u32x4 xa[16];
            const int oo = O_OFF + lyr * 2 * MB + (T * 32 + 16 * kh) * 1024;
#pragma unroll
            for (int s = 0; s < 16; ++s) { LDA(xa, s, oo); SB(); }
            const LAS char* wb = lds + 96 * KB + kh * 32768 + lane * 16;
            const LAS char* wlast = lds + (kh ? 94 * KB : 96 * KB + 15 * 2048) + lane * 16;
            bf16x8 wf[4][2];
            const int fo = kh * 1024, fs = 1024 - fo;
#pragma unroll
            for (int s = 0; s < 3; ++s) { wf[s][0] = ldsfrag(wb + fo + s * 2048); wf[s][1] = ldsfrag(wb + fs + s * 2048); }
#pragma unroll
            for (int s = 0; s < 16; ++s) {
                if (s + 3 < 15) { wf[(s + 3) & 3][0] = ldsfrag(wb + fo + (s + 3) * 2048); wf[(s + 3) & 3][1] = ldsfrag(wb + fs + (s + 3) * 2048); }
                if (s + 3 == 15) { wf[3][0] = ldsfrag(wlast + fo); wf[3][1] = ldsfrag(wlast + fs); }
                const bf16x8 x = asfrag(xa[s]);
                acc[0] = mfma32(wf[s & 3][0], x, acc[0]); acc[1] = mfma32(wf[s & 3][1], x, acc[1]);
                SGB(0x100, 1); SGB(0x8, 1); SGB(0x20, 2); SGB(0x100, 1); SGB(0x8, 1);
                SB();
            }
            const f32x16 snd = acc[1];
#pragma unroll
            for (int g4 = 0; g4 < 4; ++g4) *(LAS f32x4*)(lds + L_RED2 + ((w * 4 + g4) * 64 + lane) * 16) = f32x4{snd[4 * g4], snd[4 * g4 + 1], snd[4 * g4 + 2], snd[4 * g4 + 3]};
            BAR();
            const f32x16 own = acc[0];
            const int pw = w ^ 2;
            float v[16];
#pragma unroll
            for (int g4 = 0; g4 < 4; ++g4) { f32x4 t = *(LAS f32x4*)(lds + L_RED2 + ((pw * 4 + g4) * 64 + lane) * 16);
#pragma unroll
                for (int i = 0; i < 4; ++i) v[4 * g4 + i] = own[4 * g4 + i] + t[i]; }
            const unsigned rr[8] = {r0.x, r0.y, r0.z, r0.w, r1.x, r1.y, r1.z, r1.w};
            unsigned c[8];
#pragma unroll
            for (int i = 0; i < 8; ++i) c[i] = pk(v[2 * i] + bflo(rr[i]), v[2 * i + 1] + bfhi(rr[i]));
            const int yo = X2_OFF + lyr * 2 * MB + (T * 32 + 2 * u) * 1024;
            stw(rs, lane * 16, yo, u32x4{c[0], c[1], c[2], c[3]});
            stw(rs, lane * 16 + 1024, yo, u32x4{c[4], c[5], c[6], c[7]});
            put_row_stats(rs, 2 * lyr + 1, T, u, lane, c);
            DRAIN();
            BAR();
            if (w == 0 && lane == 0) set_flag(p, 3 + 5 * lyr, g, r, tag);
        }
        {
            PHASE_VARS();
            if (w == 0) wait_group(p, 3 + 5 * lyr, g, tag, lane);
            BAR();
            const int T = 4 * g + w;
            const LAS char* wbA = lds + lane * 16;
            const LAS char* wbB = lds + 3072 + (h * 16 + (lane & 15)) * 16;
            LAS float* cs1 = (LAS float*)(lds + L_CS1);
            f32x16 acc[4] = {};
            u32x4 xa[32];
            f32x2 sv[8];
            const int sto = ST_OFF + (2 * lyr + 1) * ST_BUF + T * 4096, xo = X2_OFF + lyr * 2 * MB + T * 32768;
#pragma unroll
            for (int s = 0; s < PD0; ++s) { LDA(xa, s, xo); SB(); }
            auto ldW = [&](int s, int t) { return t < 3 ? ldsfrag(wbA + w1_off(s) + t * 1024) : ldsfrag(wbB + w1_off(s)); };
            bf16x8 wf[2][4];
#pragma unroll
            for (int t = 0; t < 4; ++t) wf[0][t] = ldW(0, t);
#pragma unroll
            for (int s = 0; s < 32; ++s) {
                if (s == 20) BAR();
#pragma unroll
                for (int j = (s == 0 ? PD0 : a_issued(s - 1, 32)); j < a_issued(s, 32); ++j) LDA(xa, j, xo);
                if (s >= 16 && s < 24) sv[s - 16] = ldst(rs, (8 * h * 32 + tok) * 8 + (s - 16) * 256, sto);
                const bf16x8 x = asfrag(xa[s]);
#pragma unroll
                for (int t = 0; t < 4; ++t) {
                    if (s + 1 < 32) wf[(s + 1) & 1][t] = ldW(s + 1, t);
                    acc[t] = mfma32(wf[s & 1][t], x, acc[t]);
                }
                SGB(0x100, 1); SGB(0x8, 1); SGB(0x20, 1); SGB(0x100, 1); SGB(0x8, 1); SGB(0x20, 1); SGB(0x100, 1); SGB(0x8, 1); SGB(0x100, 1); SGB(0x8, 1);
                SB();
            }
            float mean, rstd; ln_stats(sv, mean, rstd);
            BAR();
            const int ho = H_OFF + lyr * 8 * MB + (T * 112 + 7 * r) * 1024;
            const f32x2 nm2 = {-mean, -mean}, rs2 = {rstd, rstd}, ry2 = {-LOG2E * rstd, -LOG2E * rstd}, one2 = {1.f, 1.f};
#pragma unroll
            for (int t = 0; t < 4; ++t) {
                const int np = t < 3 ? 8 : 4;
                f32x2 tt[8], z[8], e[8];
#pragma unroll
                for (int g4 = 0; g4 < 4; ++g4) {
                    if (2 * g4 < np) {
                        const f32x4 c4 = *(LAS f32x4*)(cs1 + t * 32 + 8 * g4 + 4 * h);
                        tt[2 * g4] = __builtin_elementwise_fma(nm2, f32x2{c4[0], c4[1]}, f32x2{acc[t][4 * g4], acc[t][4 * g4 + 1]});
                        tt[2 * g4 + 1] = __builtin_elementwise_fma(nm2, f32x2{c4[2], c4[3]}, f32x2{acc[t][4 * g4 + 2], acc[t][4 * g4 + 3]});
                    }
                }
#pragma unroll
                for (int i = 0; i < 8; ++i) if (i < np) e[i] = tt[i] * ry2;
                SB();
#pragma unroll
                for (int i = 0; i < 8; ++i) if (i < np) e[i] = f32x2{__builtin_amdgcn_exp2f(e[i].x), __builtin_amdgcn_exp2f(e[i].y)};
#pragma unroll
                for (int i = 0; i < 8; ++i) if (i < np) z[i] = tt[i] * rs2;
                SB();
#pragma unroll
                for (int i = 0; i < 8; ++i) if (i < np) e[i] = e[i] + one2;
                SB();
#pragma unroll
                for (int i = 0; i < 8; ++i) if (i < np) e[i] = f32x2{__builtin_amdgcn_rcpf(e[i].x), __builtin_amdgcn_rcpf(e[i].y)};
                SB();
#pragma unroll
                for (int i = 0; i < 8; ++i) if (i < np) z[i] = z[i] * e[i];
                stw(rs, lane * 16 + (2 * t) * 1024, ho, u32x4{pk(z[0].x, z[0].y), pk(z[1].x, z[1].y), pk(z[2].x, z[2].y), pk(z[3].x, z[3].y)});
                if (t < 3) stw(rs, lane * 16 + (2 * t + 1) * 1024, ho, u32x4{pk(z[4].x, z[4].y), pk(z[5].x, z[5].y), pk(z[6].x, z[6].y), pk(z[7].x, z[7].y)});
                SB();
            }
            DRAIN();
            BAR();
            if (w == 0 && lane == 0) set_flag(p, 4 + 5 * lyr, g, r, tag);
        }
        {
            PHASE_VARS();
            const int mt = w & 1, kh = (w >> 1) & 1, T = 4 * g + 2 * m + mt, u = 2 * n + kh;
            const int yo = X2_OFF + lyr * 2 * MB + (T * 32 + 2 * u) * 1024;
            const u32x4 r0 = ldw(rs, lane * 16, yo), r1 = ldw(rs, lane * 16 + 1024, yo);
            if (w == 0) wait_group(p, 4 + 5 * lyr, g, tag, lane);
            BAR();
            const LAS char* wb = lds + kh * 2048 + lane * 16;
            f32x16 acc[2] = {};
            const int ho = H_OFF + lyr * 8 * MB + (T * 112 + 56 * kh) * 1024;
            u32x4 xa[56];
#pragma unroll
            for (int s = 0; s < PD0; ++s) { LDA(xa, s, ho); SB(); }
            auto woff = [](int s) { return w2_slot(s); };
            bf16x8 wf[4][2];
            const int fo = kh * 1024, fs = 1024 - fo;
#pragma unroll
            for (int s = 0; s < 3; ++s) { wf[s][0] = ldsfrag(wb + fo + woff(s)); wf[s][1] = ldsfrag(wb + fs + woff(s)); }
#pragma unroll
            for (int s = 0; s < 56; ++s) {
                if (s == 18) BAR();
                if (s == 35) BAR();
#pragma unroll
                for (int j = (s == 0 ? PD0 : a_issued(s - 1, 56)); j < a_issued(s, 56); ++j) LDA(xa, j, ho);
                if (s + 3 < 56) { wf[(s + 3) & 3][0] = ldsfrag(wb + fo + woff(s + 3)); wf[(s + 3) & 3][1] = ldsfrag(wb + fs + woff(s + 3)); }
                const bf16x8 x = asfrag(xa[s]);
                acc[0] = mfma32(wf[s & 3][0], x, acc[0]); acc[1] = mfma32(wf[s & 3][1], x, acc[1]);
                SGB(0x100, 1); SGB(0x8, 1); SGB(0x20, 2); SGB(0x100, 1); SGB(0x8, 1);
                SB();
            }
            const f32x16 snd = acc[1];
#pragma unroll
            for (int g4 = 0; g4 < 4; ++g4) *(LAS f32x4*)(lds + L_RED4 + ((w * 4 + g4) * 64 + lane) * 16) = f32x4{snd[4 * g4], snd[4 * g4 + 1], snd[4 * g4 + 2], snd[4 * g4 + 3]};
            BAR();
            const f32x16 own = acc[0];
            const int pw = w ^ 2;
            float v[16];
#pragma unroll
            for (int g4 = 0; g4 < 4; ++g4) { f32x4 t = *(LAS f32x4*)(lds + L_RED4 + ((pw * 4 + g4) * 64 + lane) * 16);
#pragma unroll
                for (int i = 0; i < 4; ++i) v[4 * g4 + i] = own[4 * g4 + i] + t[i]; }
            const unsigned rr[8] = {r0.x, r0.y, r0.z, r0.w, r1.x, r1.y, r1.z, r1.w};
            unsigned c[8];
#pragma unroll
            for (int i = 0; i < 8; ++i) c[i] = pk(v[2 * i] + bflo(rr[i]), v[2 * i + 1] + bfhi(rr[i]));
            if (lyr == 0) {
                stw(rs, lane * 16, XF_OFF + 2 * MB + (T * 32 + 2 * u) * 1024, u32x4{c[0], c[1], c[2], c[3]});
                stw(rs, lane * 16 + 1024, XF_OFF + 2 * MB + (T * 32 + 2 * u) * 1024, u32x4{c[4], c[5], c[6], c[7]});
                put_row_stats(rs, 2, T, u, lane, c);
                DRAIN();
                BAR();
                if (w == 0 && lane == 0) set_flag(p, 5, g, r, tag);
            } else {
                auto s0 = __builtin_amdgcn_permlane32_swap(c[0], c[2], false, false);
                auto s1 = __builtin_amdgcn_permlane32_swap(c[1], c[3], false, false);
                auto s2 = __builtin_amdgcn_permlane32_swap(c[4], c[6], false, false);
                auto s3 = __builtin_amdgcn_permlane32_swap(c[5], c[7], false, false);
                uint16_t* orow = p.out + (size_t)(T * 32 + (lane & 31)) * DM + 32 * u + 8 * h;
                *(GAS u32x4*)(orow) = u32x4{s0[0], s1[0], s0[1], s1[1]};
                *(GAS u32x4*)(orow + 16) = u32x4{s2[0], s3[0], s2[1], s3[1]};
            }
        }
    }
}

template <int PART> DI void pretile(const Params& p, LAS char* lds, unsigned lds0, rsrc_t rs, int hw, int g, int r, int lane) {
    const int h = lane >> 5, a = (lane & 15) >> 2, pp = lane & 3, ch = (lane >> 4) & 1;
    const int n = r >> 1, j = 2 * g + (r & 1);
    const unsigned scr0 = lds0 + L_SCR + hw * 8192;
    const LAS char* scr = lds + L_SCR + hw * 8192;
    if (hw == 0) {
        const char* base = (const char*)(p.wqkv + (size_t)32 * g * 1536 + 32 * r);
        if (PART == 0) {
#pragma unroll
            for (int i = 0; i < 6; ++i) { const int L = 64 * i + lane, k = L / 12, c = L % 12; glds16s<false>(base, (unsigned)(k * 1536 + (c >> 2) * 512 + (c & 3) * 8) * 2, scr0 + i * 1024); }
            return;
        }
        DRAIN();
        const LAS char* wb = scr + (4 * h + a) * 192 + ch * 32 + pp * 8;
#pragma unroll
        for (int s = 0; s < 2; ++s)
#pragma unroll
            for (int t = 0; t < 3; ++t) stw(rs, lane * 16, WQT_OFF + ((r * 32 + 2 * g + s) * 3 + t) * 1024, wfrag_tr(wb + s * 16 * 192 + t * 64, 8 * 192));
    } else if (hw == 1) {
        const char* base = (const char*)(p.w1 + (size_t)32 * g * FF + 112 * r);
        if (PART == 0) {
#pragma unroll
            for (int b = 0; b < 7; ++b) {
                const int L = 64 * b + lane, G = L / 56, x = L - G * 56;
                const int aa = x < 48 ? ((x >> 2) & 3) : ((x - 48) >> 1);
                const int c = x < 48 ? ((x >> 4) * 4 + (x & 3)) : (12 + (x & 1));
                glds16s<false>(base, (unsigned)((4 * G + aa) * FF + c * 8) * 2, scr0 + b * 1024);
            }
            return;
        }
        DRAIN();
        const LAS char* wbA = scr + h * 896 + a * 64 + ch * 32 + pp * 8;
        const LAS char* wbB = scr + h * 896 + 768 + a * 32 + pp * 8;
#pragma unroll
        for (int s = 0; s < 2; ++s) {
            const int dst = W1T_OFF + r * 114688 + (2 * g + s) * 3584;
#pragma unroll
            for (int t = 0; t < 3; ++t) stw(rs, lane * 16, dst + t * 1024, wfrag_tr(wbA + s * 3584 + t * 256, 1792));
            const u32x4 f3 = wfrag_tr(wbB + s * 3584, 1792);
            if (ch == 0) stw(rs, (h * 16 + (lane & 15)) * 16, dst + 3072, f3);
        }
    } else {
        const int rr = lane >> 3, slot = lane & 7, chunk = slot ^ (((rr >> 1) & 1) << 2);
        const unsigned vo = (unsigned)(rr * DM + chunk * 8) * 2;
        const int s0 = hw == 3 ? j : (j < 16 ? 4 * j : 64 + 3 * (j - 16)), ns = hw == 3 ? 1 : (j < 16 ? 4 : 3);
        const char* base = (const char*)((hw == 3 ? p.wo : p.w2) + (size_t)16 * s0 * DM + 64 * n);
        if (PART == 0) {
            for (int i = 0; i < 2 * ns; ++i) glds16s<false>(base + (size_t)i * 8 * DM * 2, vo, scr0 + i * 1024);
            return;
        }
        DRAIN();
        const int sw = ((a >> 1) & 1) << 6;
        const LAS char* wb0 = scr + (4 * h + a) * 128 + (0 ^ sw) + ch * 32 + pp * 8;
        const LAS char* wb1 = scr + (4 * h + a) * 128 + (64 ^ sw) + ch * 32 + pp * 8;
        for (int i = 0; i < ns; ++i) {
            const int s = s0 + i;
            const int kh = s >= 56 ? 1 : 0, sl = s - 56 * kh;
            const int dst = hw == 3 ? WOT_OFF + n * 65536 + s * 2048 : W2T_OFF + (n * 56 + sl) * 4096 + kh * 2048;
            stw(rs, lane * 16, dst, wfrag_tr(wb0 + i * 2048, 1024));
            stw(rs, lane * 16, dst + 1024, wfrag_tr(wb1 + i * 2048, 1024));
        }
    }
    DRAIN();
}

constexpr int w2_regsp(int j) { return j < 10 ? j : j < 30 ? j + 4 : j + 8; }
DI void helper_path(const Params& p, LAS char* lds, unsigned lds0_, rsrc_t rs, const int w_, const int g_, const int r_) {
    unsigned tag = 0;
    {   const int layer = 0; PHASE_VARS(); HELPER_VARS();
        if (hw == 0) pretile<0>(p, lds, lds0, rs, hw, g, r, lane);
        tag = read_tag(p); asm volatile("" :: "s"(tag) : "memory");
        if (hw > 0) pretile<0>(p, lds, lds0, rs, hw, g, r, lane);
        if (hw == 0) { pretile<1>(p, lds, lds0, rs, hw, g, r, lane); BAR(); }
        else { BAR(); pretile<1>(p, lds, lds0, rs, hw, g, r, lane); }
    }
    for (int layer = 0; layer < 2; ++layer) {
        {
            PHASE_VARS(); HELPER_VARS();
            BAR();
            if (lyr == 0) {
                if (w == 6 && lane == 0) set_flag(p, 10, g, r, tag);
                auto wq_pieces = [&](int j0) { for (int j = j0 + hw; j < j0 + 24; j += 4) glds16s<true>(wsb + WQT_OFF + r * 98304 + j * 1024, lane * 16, lds0 + L_WQ + j * 1024); };
                auto wo_pieces = [&](int j0, int j1) { for (int j = j0 + hw; j < j1; j += 4) glds16s<true>(wsb + WOT_OFF + n * 65536 + (j < 62 ? j : j - 4) * 1024, lane * 16, lds0 + 96 * KB + (j < 62 ? j : j - 4) * 1024); };
                float acc = 0.f;
                const LAS char* wbq = lds + L_WQ + (hw < 3 ? hw : 0) * 1024 + lane * 16;
                wq_pieces(24);
                BAR();
                wq_pieces(48);
                wait_word(p, flagp(p, 10, lane & 15, (lane & 16) ? (r ^ 1) : r), tag);
                DRAIN(); BAR();
                wq_pieces(72);
                acc = colsum_frags<8>(wbq, [](int i) { return (0 + i) * 3072; }, acc);
                BAR();
                wo_pieces(0, 32);
                acc = colsum_frags<8>(wbq, [](int i) { return (8 + i) * 3072; }, acc);
                WAITV(8); BAR();
                acc = colsum_frags<8>(wbq, [](int i) { return (16 + i) * 3072; }, acc);
                acc = colsum_frags<8>(wbq, [](int i) { return (24 + i) * 3072; }, acc);
                acc = sum_xor32(acc);
                if (h == 0 && hw < 3) ((LAS float*)(lds + L_CSQ))[hw * 32 + tok] = acc;
                wo_pieces(32, 64);
            } else {
                __builtin_amdgcn_s_sleep(WODELAY);
                for (int i = hw; i < 62; i += 4) glds16s<true>(wsb + WOT_OFF + n * 65536 + i * 1024, lane * 16, lds0 + 96 * KB + i * 1024);
            }
            BAR();
            if (w == 7 && lane < 2) *(LAS u32x4*)(lds + L_ONES + lane * 1024) = u32x4{0x3f803f80u, 0x3f803f80u, 0x3f803f80u, 0x3f803f80u};
            stage_lin(wsb, lds0, WOT_OFF + n * 65536 + 62 * KB, 94 * KB, 2, hw, lane);
            stage_lin(wsb, lds0, W1T_OFF + r * 114688, 56 * KB, 35, hw, lane);
            BAR();
        }
        u32x4 buf[48];
        {   PHASE_VARS(); HELPER_VARS();
            attn_phase(p, lds, rs, w, g, r, lyr, lane, tag);
            DRAIN();
            BAR();
            stage_lin(wsb, lds0, W1T_OFF + r * 114688 + 35 * KB, 0, 39, hw, lane);
        }
        {
            PHASE_VARS(); HELPER_VARS();
            const int wsrc = W2T_OFF + n * 56 * 4096 + hw * 1024;
            BAR();
#pragma unroll
            for (int j = 0; j < 12; ++j) { buf[j] = ldw(rs, lane * 16, wsrc + w2_regsp(j) * 4096); SB(); }
            BAR();
#pragma unroll
            for (int j = 12; j < 22; ++j) { buf[j] = ldw(rs, lane * 16, wsrc + w2_regsp(j) * 4096); SB(); }
            BAR();
            stage_lin(wsb, lds0, W1T_OFF + r * 114688 + 75264, 94 * KB, 39, hw, lane);
        }
        {
            PHASE_VARS(); HELPER_VARS();
            WAITV(31);
            BAR();
            const int wsrc = W2T_OFF + n * 56 * 4096 + hw * 1024;
            const LAS char* wbc = hw < 3 ? lds + hw * 1024 + lane * 16 : lds + 3072 + (h * 16 + (lane & 15)) * 16;
            float cs_part = 0.f;
            if (lyr == 0) {
                cs_part = colsum_frags<11>(wbc, [](int i) { return w1_off(i); }, 0.f);
                cs_part = colsum_frags<10>(wbc, [](int i) { return w1_off(11 + i); }, cs_part);
            }
            stage_lin(wsb, lds0, W2T_OFF + (n * 56 + 10) * 4096, 40 * KB, 16, hw, lane);
            stage_lin(wsb, lds0, W2T_OFF + (n * 56 + 34) * 4096, 136 * KB, 16, hw, lane);
#pragma unroll
            for (int j = 22; j < 34; ++j) { buf[j] = ldw(rs, lane * 16, wsrc + w2_regsp(j) * 4096); SB(); }
            WAITV(20);
            BAR();
#pragma unroll
            for (int j = 34; j < 48; ++j) { buf[j] = ldw(rs, lane * 16, wsrc + w2_regsp(j) * 4096); SB(); }
            if (lyr == 0) {
                LAS float* cs1 = (LAS float*)(lds + L_CS1);
                cs_part = colsum_frags<11>(wbc, [](int i) { return w1_off(21 + i); }, cs_part);
                const float s = sum_xor32(cs_part);
                if (h == 0 && (hw < 3 || tok < 16)) cs1[hw * 32 + tok] = s;
            }
            WAITV(14);
            BAR();
#pragma unroll
            for (int j = 0; j < 30; ++j) *(LAS u32x4*)(lds + w2_slot(w2_regsp(j)) + hw * 1024 + lane * 16) = buf[j];
            BAR();
            if (lyr == 0) {
#pragma unroll
                for (int j = 0; j < 24; ++j) { buf[j] = ldw(rs, lane * 16, WQT_OFF + r * 98304 + (4 * j + hw) * 1024); SB(); }
            }
        }
        {
            PHASE_VARS(); HELPER_VARS();
            BAR();
            BAR();
#pragma unroll
            for (int j = 30; j < 48; ++j) *(LAS u32x4*)(lds + w2_slot(w2_regsp(j)) + hw * 1024 + lane * 16) = buf[j];
            if (lyr == 1 && blockIdx.x == 0 && w == 7) {
                bool ok = false;
                for (unsigned spins = 0; spins < (1u << 16) && !ok; ++spins) {
                    bool mine = true;
#pragma unroll
                    for (int j = 0; j < 4; ++j) mine &= __hip_atomic_load((gu32*)(p.ws + ACK_OFF) + (lane + 64 * j) * 32, __ATOMIC_RELAXED, __HIP_MEMORY_SCOPE_AGENT) == tag;
                    ok = __all(mine);
                }
                if (lane == 0) __hip_atomic_store((gu32*)(p.ws + EPOCH_OFF), tag, __ATOMIC_RELAXED, __HIP_MEMORY_SCOPE_AGENT);
            }
            BAR();
            BAR();
            if (lyr == 0) {
                BAR();
#pragma unroll
                for (int j = 0; j < 24; ++j) *(LAS u32x4*)(lds + L_WQ + (4 * j + hw) * 1024 + lane * 16) = buf[j];
            }
        }
    }
}

__global__ __launch_bounds__(512, 2) void k_mega(Params p) {
    extern __shared__ __attribute__((aligned(16))) char smem_[];
    LAS char* lds = (LAS char*)smem_;
    const unsigned lds0 = (unsigned)(uintptr_t)smem_;
    const int b = blockIdx.x, idx = b >> 3;
    const int g_ = (b & 3) * 4 + (idx >> 3), r_ = ((b >> 2) & 1) * 8 + (idx & 7);
    const int w = __builtin_amdgcn_readfirstlane(threadIdx.x >> 6);
    const rsrc_t rs = __builtin_amdgcn_make_buffer_rsrc((void*)p.ws, 0, WS_USED, 0x00020000);
    if (w < 4) main_path(p, lds, rs, w, g_, r_);
    else helper_path(p, lds, lds0, rs, w, g_, r_);
}
}

extern "C" void kernel_launch(void* const* d_in, const int* in_sizes, int n_in, void* d_out, int out_size, void* d_ws, size_t ws_size, hipStream_t stream) {
    Params p;
    p.x = (const uint16_t*)d_in[0]; p.wqkv = (const uint16_t*)d_in[1]; p.wo = (const uint16_t*)d_in[2];
    p.w1 = (const uint16_t*)d_in[4]; p.w2 = (const uint16_t*)d_in[5];
    p.out = (uint16_t*)d_out; p.ws = (char*)d_ws;
    (void)hipFuncSetAttribute((const void*)k_mega, hipFuncAttributeMaxDynamicSharedMemorySize, LDS_BYTES);
    k_mega<<<256, 512, LDS_BYTES, stream>>>(p);
}
```
